```python
import torch

def _softmax(x, axis=-1):
    return torch.softmax(x, dim=axis)
def _gelu(x):
    return torch.nn.functional.gelu(x, approximate="tanh")
def _silu(x):
    return torch.nn.functional.silu(x)
def _sigmoid(x):
    return torch.sigmoid(x)
def _layernorm(x, axis=-1, eps=1e-5):
    m = x.mean(dim=axis, keepdim=True)
    v = x.var(dim=axis, keepdim=True, unbiased=False)
    return (x - m) / torch.sqrt(v + eps)
def _scatter_add(tgt, idx, src, axis=0):
    return tgt.clone().index_add_(axis, idx, src)
def _segment_sum(src, seg, n):
    out = torch.zeros((n,) + src.shape[1:], dtype=src.dtype)
    return out.index_add_(0, seg, src)
def _pad(x, pw):
    flat = tuple(v for pair in reversed(pw) for v in pair)
    return torch.nn.functional.pad(x, flat)

kernel_name = 'gated_mix__ssm_conv_h3__mixer_s_mlp__prompt'

def setup_inputs():

    _w = (lambda a: (lambda t: t.to(torch.bfloat16) if t.is_floating_point() else t)(torch.as_tensor(a)))
    def _A_setup():
        B, S, D = 1, 2048, 576
        ks = 4
        return {
            "q": _w((lambda *s: torch.randn(*s, dtype=torch.bfloat16) * 0.05)(B, S, D)),
            "k": _w((lambda *s: torch.randn(*s, dtype=torch.bfloat16) * 1.6)(B, S, D)),
            "v": _w((lambda *s: torch.randn(*s, dtype=torch.bfloat16) * 1.6)(B, S, D)),
            "shift_k": _w((lambda *s: torch.randn(*s, dtype=torch.bfloat16) * 1.6)(D, ks) * 0.5),
            "ssm_k": _w((lambda *s: torch.randn(*s, dtype=torch.bfloat16) * 3.2)(D, S) * (1.0 / S ** 0.5)),
            "ssm_D": _w((lambda *s: torch.randn(*s, dtype=torch.bfloat16) * 1.6)(D)),
        }
    _A_in = _A_setup()

    def _B_setup():
        B, S, D, F = 1, 2048, 576, 4608
        return {
            "x":  _w((lambda *s: torch.randn(*s, dtype=torch.bfloat16) * 0.05)(B, S, D)),
            "w1": _w((lambda *s: torch.randn(*s, dtype=torch.bfloat16) * 3.2)(D, D) * (D ** -0.5)),
            "w2": _w((lambda *s: torch.randn(*s, dtype=torch.bfloat16) * 3.2)(D, D) * (D ** -0.5)),
            "w3": _w((lambda *s: torch.randn(*s, dtype=torch.bfloat16) * 3.2)(D, F) * (D ** -0.5)),
            "w4": _w((lambda *s: torch.randn(*s, dtype=torch.bfloat16) * 1.6)(F, D) * (F ** -0.5)),
        }
    _B_in = _B_setup()

    return {
        "A_q": _A_in["q"],
        "A_k": _A_in["k"],
        "A_v": _A_in["v"],
        "A_shift_k": _A_in["shift_k"],
        "A_ssm_k": _A_in["ssm_k"],
        "A_ssm_D": _A_in["ssm_D"],
        "B_x": _B_in["x"],
        "B_w1": _B_in["w1"],
        "B_w2": _B_in["w2"],
        "B_w3": _B_in["w3"],
        "B_w4": _B_in["w4"],
        "gate_w": _w((lambda *s: torch.randn(*s, dtype=torch.bfloat16) * 0.05)(576) * 16.0),
    }

def reference(A_q, A_k, A_v, A_shift_k, A_ssm_k, A_ssm_D, B_x, B_w1, B_w2, B_w3, B_w4, gate_w):
    def _A_ref(q, k, v, shift_k, ssm_k, ssm_D):
        B, S, D = q.shape
        ks = shift_k.shape[-1]
        k_pad = _pad(k, ((0, 0), (ks - 1, 0), (0, 0)))
        k_shift = torch.zeros_like(k)
        for t in range(ks):
            k_shift = k_shift + k_pad[:, t:t + S, :] * shift_k[:, ks - 1 - t][None, None, :]
        kv = k_shift * v
        L = 2 * S
        kv_pad = _pad(kv, ((0, 0), (0, S), (0, 0)))
        kf_pad = _pad(ssm_k, ((0, 0), (0, S)))
        kv_f = (lambda x, axes=(-2,-1): torch.fft.rfft2(x.float(), dim=axes))(kv_pad[:, None, :, :], axes=(1, 2))
        kf_f = (lambda x, axes=(-2,-1): torch.fft.rfft2(x.float(), dim=axes))(kf_pad[:, None, :], axes=(1, 2))
        kf_f = (lambda x, *d: x.permute(*d))(kf_f, 1, 2, 0)
        y_f = kv_f * kf_f[None, :, :, :]
        y = (lambda x, s, axes=(-2,-1): torch.fft.irfft2(x, s=s, dim=axes).to(torch.bfloat16))(y_f, (1, L), axes=(1, 2))[:, 0, :S, :]
        y = y + kv * ssm_D[None, None, :]
        return q * y

    def _B_ref(x, w1, w2, w3, w4):
        B, S, D = x.shape
        h = _gelu(_layernorm(x, axis=-1) @ w1)
        c = D // 4
        g0 = _pad(h[:, :, 0*c:1*c], ((0, 0), (1, 0), (0, 0)))[:, :S, :]
        g1 = _pad(h[:, :, 1*c:2*c], ((0, 0), (0, 1), (0, 0)))[:, 1:, :]
        g2 = _pad(h[:, :, 2*c:3*c], ((0, 0), (2, 0), (0, 0)))[:, :S, :]
        g3 = _pad(h[:, :, 3*c:   ], ((0, 0), (0, 2), (0, 0)))[:, 2:, :]
        hs = torch.concatenate([g0, g1, g2, g3], axis=-1) @ w2
        x1 = x + hs
        h2 = _gelu(_layernorm(x1, axis=-1) @ w3) @ w4
        return x1 + h2

    x = A_q
    g = _sigmoid((x * gate_w).sum(-1, keepdims=True))
    a = _A_ref(x, A_k, A_v, A_shift_k, A_ssm_k, A_ssm_D)
    b = _B_ref(x, B_w1, B_w2, B_w3, B_w4)
    return g * a + (1 - g) * b
```

```cpp
#include <hip/hip_runtime.h>
#include <hip/hip_bf16.h>
#include <stdint.h>
typedef unsigned short u16;
typedef unsigned v4u __attribute__((ext_vector_type(4)));
typedef unsigned v2u __attribute__((ext_vector_type(2)));
typedef short s16x4 __attribute__((ext_vector_type(4)));
typedef short bf16x8 __attribute__((ext_vector_type(8)));
typedef float f32x4 __attribute__((ext_vector_type(4)));
typedef float f32x16 __attribute__((ext_vector_type(16)));
typedef float f32x2 __attribute__((ext_vector_type(2)));
typedef __bf16 bf16x2_t __attribute__((ext_vector_type(2)));
#define LAS __attribute__((address_space(3)))
#define DI __device__ __forceinline__
constexpr int S = 2048, D = 576, F = 4608;

DI float bf2f(u16 b) { return __builtin_bit_cast(float, (unsigned)b << 16); }
DI unsigned pk2(float lo, float hi) { f32x2 x = {lo, hi}; return __builtin_bit_cast(unsigned, __builtin_convertvector(x, bf16x2_t)); }
DI u16 f2bf(float x) { return (u16)(pk2(x, 0.f) & 0xffff); }
DI float gelu(float x) {
  const float c0 = -2.f * 0.7978845608028654f * 1.4426950408889634f, c1 = c0 * 0.044715f;
  float e = __builtin_amdgcn_exp2f(x * (c0 + c1 * x * x));
  return x * __builtin_amdgcn_rcpf(1.f + e);
}
DI s16x4 trread(const void* p) { return __builtin_amdgcn_ds_read_tr16_b64_v4i16((LAS s16x4*)p); }

__global__ void __launch_bounds__(256) k_ln(const u16* x, u16* xn, const u16* gw, float* g) {
  int row = blockIdx.x * 4 + (threadIdx.x >> 6), lane = threadIdx.x & 63;
  float v[9]; float s = 0, gs = 0;
  for (int i = 0; i < 9; ++i) { v[i] = bf2f(x[row * D + lane + 64 * i]); s += v[i]; if (gw) gs += v[i] * bf2f(gw[lane + 64 * i]); }
  for (int m = 32; m; m >>= 1) { s += __shfl_xor(s, m); gs += __shfl_xor(gs, m); }
  float mean = s / D, q = 0;
  for (int i = 0; i < 9; ++i) { v[i] -= mean; q += v[i] * v[i]; }
  for (int m = 32; m; m >>= 1) q += __shfl_xor(q, m);
  float r = rsqrtf(q / D + 1e-5f);
  for (int i = 0; i < 9; ++i) xn[row * D + lane + 64 * i] = f2bf(v[i] * r);
  if (gw && lane == 0) g[row] = 1.f / (1.f + __expf(-gs));
}
__global__ void __launch_bounds__(256) k_prep(const u16* k, const u16* v, const u16* sk, u16* U) {
  __shared__ u16 t[64][66];
  int c0 = (blockIdx.x % 9) * 64, s0 = (blockIdx.x / 9) * 64;
  int c = threadIdx.x & 63;
  float w[4]; for (int j = 0; j < 4; ++j) w[j] = bf2f(sk[(c0 + c) * 4 + j]);
  for (int i = threadIdx.x >> 6; i < 64; i += 4) {
    int s = s0 + i; float a = 0;
    for (int j = 0; j < 4; ++j) if (s - j >= 0) a += bf2f(k[(s - j) * D + c0 + c]) * w[j];
    a = bf2f(f2bf(a));
    t[c][i] = f2bf(a * bf2f(v[s * D + c0 + c]));
  }
  __syncthreads();
  for (int cc = threadIdx.x >> 6; cc < 64; cc += 4) {
    int i = threadIdx.x & 63; int r = i >> 4, z = i & 15;
    U[((c0 + cc) * 4 + r) * 512 + s0 / 4 + z] = t[cc][4 * z + r];
  }
}
constexpr int HP = 16, UP = 64;
constexpr int CONV_LDS = (HP + 2048) * 2 + 4 * (UP + 512) * 2;
__global__ void __launch_bounds__(256) k_conv(const u16* hk, const u16* U, const u16* sD, u16* yT) {
  extern __shared__ __attribute__((aligned(16))) char smem[];
  int wave = threadIdx.x >> 6, lane = threadIdx.x & 63;
  int c = blockIdx.x * 4 + wave;
  u16* hs = (u16*)(smem + wave * CONV_LDS);
  u16* us = hs + HP + 2048;
  if (lane < 2) *(v4u*)(hs + lane * 8) = (v4u){0, 0, 0, 0};
  for (int i = 0; i < 4; ++i) *(v4u*)(hs + HP + (i * 64 + lane) * 8) = *(const v4u*)(hk + c * 2048 + (i * 64 + lane) * 8);
  for (int r = 0; r < 4; ++r) {
    if (lane < 8) *(v4u*)(us + r * (UP + 512) + lane * 8) = (v4u){0, 0, 0, 0};
    *(v4u*)(us + r * (UP + 512) + UP + lane * 8) = *(const v4u*)(U + (c * 4 + r) * 512 + lane * 8);
  }
  __syncthreads();
  int n = lane & 15, g = lane >> 4, q4 = (lane & 15) >> 2, p4 = lane & 3;
  bf16x8 A[16];
#pragma unroll
  for (int q = 0; q < 16; ++q) {
    int m0 = -3 + 32 * q + 8 * g + 7;
    s16x4 lo = trread(hs + HP + 4 * (m0 - q4) + 4 * p4);
    s16x4 hi = trread(hs + HP + 4 * (m0 - 4 - q4) + 4 * p4);
    A[q] = __builtin_shufflevector(lo, hi, 0, 1, 2, 3, 4, 5, 6, 7);
  }
  float mainv[8][4], carry[8][3];
#pragma unroll
  for (int jb = 0; jb < 8; ++jb) { for (int e = 0; e < 4; ++e) mainv[jb][e] = 0; for (int e = 0; e < 3; ++e) carry[jb][e] = 0; }
#pragma unroll
  for (int r = 0; r < 4; ++r) {
    f32x4 acc[8];
#pragma unroll
    for (int jb = 0; jb < 8; ++jb) acc[jb] = (f32x4){0, 0, 0, 0};
#pragma unroll
    for (int e = -1; e < 15; ++e) {
      const u16* bp = us + r * (UP + 512) + UP + 32 * e + 4 * n - 8 * g - 4;
      v2u b0 = *(const v2u*)bp, b1 = *(const v2u*)(bp + 4);
      bf16x8 B = __builtin_bit_cast(bf16x8, (v4u){b0[0], b0[1], b1[0], b1[1]});
#pragma unroll
      for (int jb = 0; jb < 8; ++jb) { int q = 2 * jb - e; if (q >= 0 && q < 16) acc[jb] = __builtin_amdgcn_mfma_f32_16x16x32_bf16(A[q], B, acc[jb], 0, 0, 0); }
    }
#pragma unroll
    for (int jb = 0; jb < 8; ++jb)
#pragma unroll
      for (int e = 0; e < 4; ++e) { if (e + r < 4) mainv[jb][e + r] += acc[jb][e]; else carry[jb][e + r - 4] += acc[jb][e]; }
  }
  int src = g > 0 ? lane - 16 : ((n + 15) & 15) + 48;
  float dv = bf2f(sD[c]);
  float prevc[3] = {0, 0, 0};
#pragma unroll
  for (int jb = 0; jb < 8; ++jb) {
    float o[4];
#pragma unroll
    for (int e = 0; e < 3; ++e) { float t = __shfl(carry[jb][e], src); float use = lane == 0 ? prevc[e] : t; prevc[e] = t; mainv[jb][e] += use; }
    int z = 64 * jb + 4 * n + g;
#pragma unroll
    for (int e = 0; e < 4; ++e) o[e] = mainv[jb][e] + dv * bf2f(us[e * (UP + 512) + UP + z]);
    *(v2u*)(yT + c * 2048 + 4 * z) = (v2u){pk2(o[0], o[1]), pk2(o[2], o[3])};
  }
}
template <int MODE>
__global__ void __launch_bounds__(256) k_gemm(const u16* A, const u16* B, u16* C, int N, int K, const u16* res, const u16* q, const u16* yT, const float* g) {
  __shared__ __attribute__((aligned(16))) u16 As[64][72];
  __shared__ __attribute__((aligned(16))) u16 Bs[64][72];
  int nb = N / 64; int bn = blockIdx.x % nb, bm = blockIdx.x / nb;
  int tid = threadIdx.x, wave = tid >> 6, lane = tid & 63, wr = wave >> 1, wc = wave & 1;
  f32x16 acc; for (int i = 0; i < 16; ++i) acc[i] = 0;
  int lr = tid >> 3, lc = (tid & 7) * 8;
  v4u ra[2], rb[2];
  auto ld = [&](int k0) {
    for (int i = 0; i < 2; ++i) {
      int row = bm * 64 + lr + 32 * i, col = k0 + lc;
      if (MODE == 1) { int grp = col / 144; int sh = grp == 0 ? -1 : grp == 1 ? 1 : grp == 2 ? -2 : 2; row += sh; }
      ra[i] = (row >= 0 && row < S) ? *(const v4u*)(A + (size_t)row * K + col) : (v4u){0, 0, 0, 0};
      rb[i] = *(const v4u*)(B + (size_t)(k0 + lr + 32 * i) * N + bn * 64 + lc);
    }
  };
  ld(0);
  int h = lane >> 5, blk = (lane >> 4) & 1, q4 = (lane & 15) >> 2, p4 = lane & 3;
  for (int k0 = 0; k0 < K; k0 += 64) {
    __syncthreads();
    for (int i = 0; i < 2; ++i) { *(v4u*)&As[lr + 32 * i][lc] = ra[i]; *(v4u*)&Bs[lr + 32 * i][lc] = rb[i]; }
    __syncthreads();
    if (k0 + 64 < K) ld(k0 + 64);
#pragma unroll
    for (int ks = 0; ks < 4; ++ks) {
      bf16x8 a = *(const bf16x8*)&As[wr * 32 + (lane & 31)][ks * 16 + 8 * h];
      s16x4 lo = trread(&Bs[ks * 16 + 8 * h + q4][wc * 32 + blk * 16 + 4 * p4]);
      s16x4 hi = trread(&Bs[ks * 16 + 8 * h + 4 + q4][wc * 32 + blk * 16 + 4 * p4]);
      bf16x8 b = __builtin_shufflevector(lo, hi, 0, 1, 2, 3, 4, 5, 6, 7);
      acc = __builtin_amdgcn_mfma_f32_32x32x16_bf16(a, b, acc, 0, 0, 0);
    }
  }
  int col = bn * 64 + wc * 32 + (lane & 31);
#pragma unroll
  for (int i = 0; i < 16; ++i) {
    int row = bm * 64 + wr * 32 + (i & 3) + 8 * (i >> 2) + 4 * h;
    float v = acc[i];
    if (MODE == 0 || MODE == 2) v = gelu(v);
    if (MODE == 1) v += bf2f(res[row * D + col]);
    if (MODE == 3) { float b = v + bf2f(res[row * D + col]); float a = bf2f(q[row * D + col]) * bf2f(yT[col * S + row]); float gg = g[row]; v = gg * a + (1.f - gg) * b; }
    C[(size_t)row * N + col] = f2bf(v);
  }
}
extern "C" void kernel_launch(void* const* d_in, const int* in_sizes, int n_in, void* d_out, int out_size, void* d_ws, size_t ws_size, hipStream_t stream) {
  const u16 *q = (const u16*)d_in[0], *k = (const u16*)d_in[1], *v = (const u16*)d_in[2], *sk = (const u16*)d_in[3], *hk = (const u16*)d_in[4], *sD = (const u16*)d_in[5];
  const u16 *w1 = (const u16*)d_in[7], *w2 = (const u16*)d_in[8], *w3 = (const u16*)d_in[9], *w4 = (const u16*)d_in[10], *gw = (const u16*)d_in[11];
  char* ws = (char*)d_ws; const size_t T = (size_t)S * D * 2;
  u16 *xn = (u16*)ws, *h = (u16*)(ws + T), *x1 = (u16*)(ws + 2 * T), *x1n = (u16*)(ws + 3 * T), *U = (u16*)(ws + 4 * T), *yT = (u16*)(ws + 5 * T);
  float* g = (float*)(ws + 6 * T); u16* H = (u16*)(ws + 7 * T);
  k_ln<<<S / 4, 256, 0, stream>>>(q, xn, gw, g);
  k_prep<<<9 * 32, 256, 0, stream>>>(k, v, sk, U);
  k_conv<<<D / 4, 256, 4 * CONV_LDS, stream>>>(hk, U, sD, yT);
  k_gemm<0><<<32 * 9, 256, 0, stream>>>(xn, w1, h, D, D, nullptr, nullptr, nullptr, nullptr);
  k_gemm<1><<<32 * 9, 256, 0, stream>>>(h, w2, x1, D, D, q, nullptr, nullptr, nullptr);
  k_ln<<<S / 4, 256, 0, stream>>>(x1, x1n, nullptr, nullptr);
  k_gemm<2><<<32 * 72, 256, 0, stream>>>(x1n, w3, H, F, D, nullptr, nullptr, nullptr, nullptr);
  k_gemm<3><<<32 * 9, 256, 0, stream>>>(H, w4, (u16*)d_out, D, F, x1, q, yT, g);
}
```

```cpp
#include <hip/hip_runtime.h>
#include <stdint.h>
#include <type_traits>
#define CORE_C \
  "s_mov_b64 s[48:49], %[ws]\n\t" \
  "s_mov_b32 s50, 0x8000000\n\t" \
  "s_mov_b32 s51, 0x20000\n\t" \
  "v_mbcnt_lo_u32_b32 v224, -1, 0\n\t" \
  "v_mbcnt_hi_u32_b32 v224, -1, v224\n\t" \
  "v_lshlrev_b32 v244, 4, v224\n\t" \
  "s_mov_b32 s78, %[sx]\n\t" \
  "s_mov_b32 s82, 0\n\t" \
  "buffer_load_dwordx4 v[0:3], v244, s[48:51], s78 offen offset:0 sc1\n\t" \
  "buffer_load_dwordx4 v[4:7], v244, s[48:51], s78 offen offset:1024 sc1\n\t" \
  "buffer_load_dwordx4 v[8:11], v244, s[48:51], s78 offen offset:2048 sc1\n\t" \
  "buffer_load_dwordx4 v[12:15], v244, s[48:51], s78 offen offset:3072 sc1\n\t" \
  "s_add_u32 s78, s78, 4096\n\t" \
  "buffer_load_dwordx4 v[16:19], v244, s[48:51], s78 offen offset:0 sc1\n\t" \
  "buffer_load_dwordx4 v[20:23], v244, s[48:51], s78 offen offset:1024 sc1\n\t" \
  "buffer_load_dwordx4 v[24:27], v244, s[48:51], s78 offen offset:2048 sc1\n\t" \
  "buffer_load_dwordx4 v[28:31], v244, s[48:51], s78 offen offset:3072 sc1\n\t" \
  "s_add_u32 s78, s78, 4096\n\t" \
  "buffer_load_dwordx4 v[32:35], v244, s[48:51], s78 offen offset:0 sc1\n\t" \
  "buffer_load_dwordx4 v[36:39], v244, s[48:51], s78 offen offset:1024 sc1\n\t" \
  "buffer_load_dwordx4 v[40:43], v244, s[48:51], s78 offen offset:2048 sc1\n\t" \
  "buffer_load_dwordx4 v[44:47], v244, s[48:51], s78 offen offset:3072 sc1\n\t" \
  "s_mul_i32 s71, %[sl], 73728\n\t" \
  "s_add_u32 s71, s71, %[shx]\n\t" \
  "s_and_b32 s77, %[sl], 1\n\t" \
  "s_lshl_b32 s77, s77, 6\n\t" \
  "v_lshlrev_b32 v225, 5, v224\n\t" \
  "v_and_b32 v225, 0x780, v225\n\t" \
  "v_lshlrev_b32 v226, 2, v224\n\t" \
  "v_and_b32 v226, 0x60, v226\n\t" \
  "v_or_b32 v225, v225, v226\n\t" \
  "v_lshlrev_b32 v226, 3, v224\n\t" \
  "v_and_b32 v226, 0x18, v226\n\t" \
  "v_or_b32 v232, v225, v226\n\t" \
  "v_xor_b32 v233, 32, v232\n\t" \
  "v_xor_b32 v234, 64, v232\n\t" \
  "v_xor_b32 v235, 96, v232\n\t" \
  "v_add_u32 v236, 61440, v232\n\t" \
  "v_add_u32 v237, 61440, v233\n\t" \
  "v_add_u32 v238, 61440, v234\n\t" \
  "v_add_u32 v239, 61440, v235\n\t" \
  "v_add_u32 v240, 122880, v232\n\t" \
  "v_add_u32 v241, 122880, v233\n\t" \
  "v_add_u32 v242, 122880, v234\n\t" \
  "v_add_u32 v243, 122880, v235\n\t" \
  "s_mul_i32 s75, %[wi], 3072\n\t" \
  "v_add_u32 v228, s75, v244\n\t" \
  "v_add_u32 v229, 61440, v228\n\t" \
  "v_add_u32 v230, 122880, v228\n\t" \
  "s_lshl_b32 s75, %[wi], 7\n\t" \
  "s_add_u32 s75, s75, 159744\n\t" \
  "v_and_b32 v225, 15, v224\n\t" \
  "v_lshlrev_b32 v225, 2, v225\n\t" \
  "v_add_u32 v231, s75, v225\n\t" \
  "s_setprio 1\n\t" \
  "s_barrier\n\t" \
  "L_XS_%=:\n\t" \
  "v_mov_b32 v227, 0\n\t" \
  "ds_read_b64_tr_b16 v[192:193], v232 offset:0\n\t" \
  "ds_read_b64_tr_b16 v[194:195], v232 offset:2048\n\t" \
  "ds_read_b64_tr_b16 v[196:197], v232 offset:4096\n\t" \
  "ds_read_b64_tr_b16 v[198:199], v232 offset:6144\n\t" \
  "ds_read_b64_tr_b16 v[200:201], v232 offset:8192\n\t" \
  "ds_read_b64_tr_b16 v[202:203], v232 offset:10240\n\t" \
  "ds_read_b64_tr_b16 v[204:205], v233 offset:0\n\t" \
  "ds_read_b64_tr_b16 v[206:207], v233 offset:2048\n\t" \
  "ds_read_b64_tr_b16 v[208:209], v233 offset:4096\n\t" \
  "ds_read_b64_tr_b16 v[210:211], v233 offset:6144\n\t" \
  "ds_read_b64_tr_b16 v[212:213], v233 offset:8192\n\t" \
  "ds_read_b64_tr_b16 v[214:215], v233 offset:10240\n\t" \
  "s_waitcnt lgkmcnt(6)\n\t" \
  "s_waitcnt vmcnt(6)\n\t" \
  "v_mfma_f32_16x16x32_bf16 v[144:147], v[192:195], v[0:3], 0\n\t" \
  "ds_read_b64_tr_b16 v[216:217], v234 offset:0\n\t" \
  "v_mfma_f32_16x16x32_bf16 v[144:147], v[196:199], v[8:11], v[144:147]\n\t" \
  "ds_read_b64_tr_b16 v[218:219], v234 offset:2048\n\t" \
  "s_add_u32 s78, s78, 4096\n\t" \
  "buffer_load_dwordx4 v[48:51], v244, s[48:51], s78 offen offset:0 sc1\n\t" \
  "v_mfma_f32_16x16x32_bf16 v[144:147], v[200:203], v[16:19], v[144:147]\n\t" \
  "ds_read_b64_tr_b16 v[220:221], v234 offset:4096\n\t" \
  "v_mfma_f32_16x16x32_bf16 v[148:151], v[192:195], v[4:7], 0\n\t" \
  "ds_read_b64_tr_b16 v[222:223], v234 offset:6144\n\t" \
  "v_mfma_f32_16x16x32_bf16 v[148:151], v[196:199], v[12:15], v[148:151]\n\t" \
  "ds_read_b64_tr_b16 v[192:193], v234 offset:8192\n\t" \
  "buffer_load_dwordx4 v[52:55], v244, s[48:51], s78 offen offset:1024 sc1\n\t" \
  "v_mfma_f32_16x16x32_bf16 v[148:151], v[200:203], v[20:23], v[148:151]\n\t" \
  "ds_read_b64_tr_b16 v[194:195], v234 offset:10240\n\t" \
  "s_waitcnt lgkmcnt(6)\n\t" \
  "v_mfma_f32_16x16x32_bf16 v[152:155], v[204:207], v[0:3], 0\n\t" \
  "ds_read_b64_tr_b16 v[196:197], v235 offset:0\n\t" \
  "v_mfma_f32_16x16x32_bf16 v[152:155], v[208:211], v[8:11], v[152:155]\n\t" \
  "ds_read_b64_tr_b16 v[198:199], v235 offset:2048\n\t" \
  "buffer_load_dwordx4 v[56:59], v244, s[48:51], s78 offen offset:2048 sc1\n\t" \
  "v_mfma_f32_16x16x32_bf16 v[152:155], v[212:215], v[16:19], v[152:155]\n\t" \
  "ds_read_b64_tr_b16 v[200:201], v235 offset:4096\n\t" \
  "v_mfma_f32_16x16x32_bf16 v[156:159], v[204:207], v[4:7], 0\n\t" \
  "ds_read_b64_tr_b16 v[202:203], v235 offset:6144\n\t" \
  "v_mfma_f32_16x16x32_bf16 v[156:159], v[208:211], v[12:15], v[156:159]\n\t" \
  "ds_read_b64_tr_b16 v[204:205], v235 offset:8192\n\t" \
  "buffer_load_dwordx4 v[60:63], v244, s[48:51], s78 offen offset:3072 sc1\n\t" \
  "v_mfma_f32_16x16x32_bf16 v[156:159], v[212:215], v[20:23], v[156:159]\n\t" \
  "ds_read_b64_tr_b16 v[206:207], v235 offset:10240\n\t" \
  "s_waitcnt lgkmcnt(6)\n\t" \
  "v_mfma_f32_16x16x32_bf16 v[160:163], v[216:219], v[0:3], 0\n\t" \
  "ds_read_b64_tr_b16 v[208:209], v232 offset:12288\n\t" \
  "v_mfma_f32_16x16x32_bf16 v[160:163], v[220:223], v[8:11], v[160:163]\n\t" \
  "ds_read_b64_tr_b16 v[210:211], v232 offset:14336\n\t" \
  "s_add_u32 s78, s78, 4096\n\t" \
  "buffer_load_dwordx4 v[64:67], v244, s[48:51], s78 offen offset:0 sc1\n\t" \
  "v_mfma_f32_16x16x32_bf16 v[160:163], v[192:195], v[16:19], v[160:163]\n\t" \
  "ds_read_b64_tr_b16 v[212:213], v232 offset:16384\n\t" \
  "v_mfma_f32_16x16x32_bf16 v[164:167], v[216:219], v[4:7], 0\n\t" \
  "ds_read_b64_tr_b16 v[214:215], v232 offset:18432\n\t" \
  "v_mfma_f32_16x16x32_bf16 v[164:167], v[220:223], v[12:15], v[164:167]\n\t" \
  "ds_read_b64_tr_b16 v[216:217], v232 offset:20480\n\t" \
  "buffer_load_dwordx4 v[68:71], v244, s[48:51], s78 offen offset:1024 sc1\n\t" \
  "v_mfma_f32_16x16x32_bf16 v[164:167], v[192:195], v[20:23], v[164:167]\n\t" \
  "ds_read_b64_tr_b16 v[218:219], v232 offset:22528\n\t" \
  "s_waitcnt lgkmcnt(6)\n\t" \
  "v_mfma_f32_16x16x32_bf16 v[168:171], v[196:199], v[0:3], 0\n\t" \
  "ds_read_b64_tr_b16 v[220:221], v233 offset:12288\n\t" \
  "v_mfma_f32_16x16x32_bf16 v[168:171], v[200:203], v[8:11], v[168:171]\n\t" \
  "ds_read_b64_tr_b16 v[222:223], v233 offset:14336\n\t" \
  "buffer_load_dwordx4 v[72:75], v244, s[48:51], s78 offen offset:2048 sc1\n\t" \
  "v_mfma_f32_16x16x32_bf16 v[168:171], v[204:207], v[16:19], v[168:171]\n\t" \
  "ds_read_b64_tr_b16 v[192:193], v233 offset:16384\n\t" \
  "v_mfma_f32_16x16x32_bf16 v[172:175], v[196:199], v[4:7], 0\n\t" \
  "ds_read_b64_tr_b16 v[194:195], v233 offset:18432\n\t" \
  "v_mfma_f32_16x16x32_bf16 v[172:175], v[200:203], v[12:15], v[172:175]\n\t" \
  "ds_read_b64_tr_b16 v[196:197], v233 offset:20480\n\t" \
  "buffer_load_dwordx4 v[76:79], v244, s[48:51], s78 offen offset:3072 sc1\n\t" \
  "v_mfma_f32_16x16x32_bf16 v[172:175], v[204:207], v[20:23], v[172:175]\n\t" \
  "ds_read_b64_tr_b16 v[198:199], v233 offset:22528\n\t" \
  "s_waitcnt lgkmcnt(6)\n\t" \
  "s_waitcnt vmcnt(8)\n\t" \
  "v_mfma_f32_16x16x32_bf16 v[144:147], v[208:211], v[24:27], v[144:147]\n\t" \
  "ds_read_b64_tr_b16 v[200:201], v234 offset:12288\n\t" \
  "v_mfma_f32_16x16x32_bf16 v[144:147], v[212:215], v[32:35], v[144:147]\n\t" \
  "ds_read_b64_tr_b16 v[202:203], v234 offset:14336\n\t" \
  "s_add_u32 s78, s78, 4096\n\t" \
  "buffer_load_dwordx4 v[80:83], v244, s[48:51], s78 offen offset:0 sc1\n\t" \
  "v_mfma_f32_16x16x32_bf16 v[144:147], v[216:219], v[40:43], v[144:147]\n\t" \
  "ds_read_b64_tr_b16 v[204:205], v234 offset:16384\n\t" \
  "v_mfma_f32_16x16x32_bf16 v[148:151], v[208:211], v[28:31], v[148:151]\n\t" \
  "ds_read_b64_tr_b16 v[206:207], v234 offset:18432\n\t" \
  "v_mfma_f32_16x16x32_bf16 v[148:151], v[212:215], v[36:39], v[148:151]\n\t" \
  "ds_read_b64_tr_b16 v[208:209], v234 offset:20480\n\t" \
  "buffer_load_dwordx4 v[84:87], v244, s[48:51], s78 offen offset:1024 sc1\n\t" \
  "v_mfma_f32_16x16x32_bf16 v[148:151], v[216:219], v[44:47], v[148:151]\n\t" \
  "ds_read_b64_tr_b16 v[210:211], v234 offset:22528\n\t" \
  "s_waitcnt lgkmcnt(6)\n\t" \
  "v_mfma_f32_16x16x32_bf16 v[152:155], v[220:223], v[24:27], v[152:155]\n\t" \
  "ds_read_b64_tr_b16 v[212:213], v235 offset:12288\n\t" \
  "v_mfma_f32_16x16x32_bf16 v[152:155], v[192:195], v[32:35], v[152:155]\n\t" \
  "ds_read_b64_tr_b16 v[214:215], v235 offset:14336\n\t" \
  "buffer_load_dwordx4 v[88:91], v244, s[48:51], s78 offen offset:2048 sc1\n\t" \
  "v_mfma_f32_16x16x32_bf16 v[152:155], v[196:199], v[40:43], v[152:155]\n\t" \
  "ds_read_b64_tr_b16 v[216:217], v235 offset:16384\n\t" \
  "v_mfma_f32_16x16x32_bf16 v[156:159], v[220:223], v[28:31], v[156:159]\n\t" \
  "ds_read_b64_tr_b16 v[218:219], v235 offset:18432\n\t" \
  "v_mfma_f32_16x16x32_bf16 v[156:159], v[192:195], v[36:39], v[156:159]\n\t" \
  "ds_read_b64_tr_b16 v[220:221], v235 offset:20480\n\t" \
  "buffer_load_dwordx4 v[92:95], v244, s[48:51], s78 offen offset:3072 sc1\n\t" \
  "v_mfma_f32_16x16x32_bf16 v[156:159], v[196:199], v[44:47], v[156:159]\n\t" \
  "ds_read_b64_tr_b16 v[222:223], v235 offset:22528\n\t" \
  "s_waitcnt lgkmcnt(6)\n\t" \
  "v_mfma_f32_16x16x32_bf16 v[160:163], v[200:203], v[24:27], v[160:163]\n\t" \
  "ds_read_b64_tr_b16 v[192:193], v232 offset:24576\n\t" \
  "v_mfma_f32_16x16x32_bf16 v[160:163], v[204:207], v[32:35], v[160:163]\n\t" \
  "ds_read_b64_tr_b16 v[194:195], v232 offset:26624\n\t" \
  "s_add_u32 s78, s78, 4096\n\t" \
  "buffer_load_dwordx4 v[96:99], v244, s[48:51], s78 offen offset:0 sc1\n\t" \
  "v_mfma_f32_16x16x32_bf16 v[160:163], v[208:211], v[40:43], v[160:163]\n\t" \
  "ds_read_b64_tr_b16 v[196:197], v232 offset:28672\n\t" \
  "v_mfma_f32_16x16x32_bf16 v[164:167], v[200:203], v[28:31], v[164:167]\n\t" \
  "ds_read_b64_tr_b16 v[198:199], v232 offset:30720\n\t" \
  "v_mfma_f32_16x16x32_bf16 v[164:167], v[204:207], v[36:39], v[164:167]\n\t" \
  "ds_read_b64_tr_b16 v[200:201], v232 offset:32768\n\t" \
  "buffer_load_dwordx4 v[100:103], v244, s[48:51], s78 offen offset:1024 sc1\n\t" \
  "v_mfma_f32_16x16x32_bf16 v[164:167], v[208:211], v[44:47], v[164:167]\n\t" \
  "ds_read_b64_tr_b16 v[202:203], v232 offset:34816\n\t" \
  "s_waitcnt lgkmcnt(6)\n\t" \
  "v_mfma_f32_16x16x32_bf16 v[168:171], v[212:215], v[24:27], v[168:171]\n\t" \
  "ds_read_b64_tr_b16 v[204:205], v233 offset:24576\n\t" \
  "v_mfma_f32_16x16x32_bf16 v[168:171], v[216:219], v[32:35], v[168:171]\n\t" \
  "ds_read_b64_tr_b16 v[206:207], v233 offset:26624\n\t" \
  "buffer_load_dwordx4 v[104:107], v244, s[48:51], s78 offen offset:2048 sc1\n\t" \
  "v_mfma_f32_16x16x32_bf16 v[168:171], v[220:223], v[40:43], v[168:171]\n\t" \
  "ds_read_b64_tr_b16 v[208:209], v233 offset:28672\n\t" \
  "v_mfma_f32_16x16x32_bf16 v[172:175], v[212:215], v[28:31], v[172:175]\n\t" \
  "ds_read_b64_tr_b16 v[210:211], v233 offset:30720\n\t" \
  "v_mfma_f32_16x16x32_bf16 v[172:175], v[216:219], v[36:39], v[172:175]\n\t" \
  "ds_read_b64_tr_b16 v[212:213], v233 offset:32768\n\t" \
  "buffer_load_dwordx4 v[108:111], v244, s[48:51], s78 offen offset:3072 sc1\n\t" \
  "v_mfma_f32_16x16x32_bf16 v[172:175], v[220:223], v[44:47], v[172:175]\n\t" \
  "ds_read_b64_tr_b16 v[214:215], v233 offset:34816\n\t" \
  "s_waitcnt lgkmcnt(6)\n\t" \
  "s_waitcnt vmcnt(10)\n\t" \
  "v_mfma_f32_16x16x32_bf16 v[144:147], v[192:195], v[48:51], v[144:147]\n\t" \
  "ds_read_b64_tr_b16 v[216:217], v234 offset:24576\n\t" \
  "v_mfma_f32_16x16x32_bf16 v[144:147], v[196:199], v[56:59], v[144:147]\n\t" \
  "ds_read_b64_tr_b16 v[218:219], v234 offset:26624\n\t" \
  "s_add_u32 s78, s78, 4096\n\t" \
  "buffer_load_dwordx4 v[112:115], v244, s[48:51], s78 offen offset:0 sc1\n\t" \
  "v_mfma_f32_16x16x32_bf16 v[144:147], v[200:203], v[64:67], v[144:147]\n\t" \
  "ds_read_b64_tr_b16 v[220:221], v234 offset:28672\n\t" \
  "v_mfma_f32_16x16x32_bf16 v[148:151], v[192:195], v[52:55], v[148:151]\n\t" \
  "ds_read_b64_tr_b16 v[222:223], v234 offset:30720\n\t" \
  "v_mfma_f32_16x16x32_bf16 v[148:151], v[196:199], v[60:63], v[148:151]\n\t" \
  "ds_read_b64_tr_b16 v[192:193], v234 offset:32768\n\t" \
  "buffer_load_dwordx4 v[116:119], v244, s[48:51], s78 offen offset:1024 sc1\n\t" \
  "v_mfma_f32_16x16x32_bf16 v[148:151], v[200:203], v[68:71], v[148:151]\n\t" \
  "ds_read_b64_tr_b16 v[194:195], v234 offset:34816\n\t" \
  "s_waitcnt lgkmcnt(6)\n\t" \
  "v_mfma_f32_16x16x32_bf16 v[152:155], v[204:207], v[48:51], v[152:155]\n\t" \
  "ds_read_b64_tr_b16 v[196:197], v235 offset:24576\n\t" \
  "v_mfma_f32_16x16x32_bf16 v[152:155], v[208:211], v[56:59], v[152:155]\n\t" \
  "ds_read_b64_tr_b16 v[198:199], v235 offset:26624\n\t" \
  "buffer_load_dwordx4 v[120:123], v244, s[48:51], s78 offen offset:2048 sc1\n\t" \
  "v_mfma_f32_16x16x32_bf16 v[152:155], v[212:215], v[64:67], v[152:155]\n\t" \
  "ds_read_b64_tr_b16 v[200:201], v235 offset:28672\n\t" \
  "v_mfma_f32_16x16x32_bf16 v[156:159], v[204:207], v[52:55], v[156:159]\n\t" \
  "ds_read_b64_tr_b16 v[202:203], v235 offset:30720\n\t" \
  "v_mfma_f32_16x16x32_bf16 v[156:159], v[208:211], v[60:63], v[156:159]\n\t" \
  "ds_read_b64_tr_b16 v[204:205], v235 offset:32768\n\t" \
  "buffer_load_dwordx4 v[124:127], v244, s[48:51], s78 offen offset:3072 sc1\n\t" \
  "v_mfma_f32_16x16x32_bf16 v[156:159], v[212:215], v[68:71], v[156:159]\n\t" \
  "ds_read_b64_tr_b16 v[206:207], v235 offset:34816\n\t" \
  "s_waitcnt lgkmcnt(6)\n\t" \
  "v_mfma_f32_16x16x32_bf16 v[160:163], v[216:219], v[48:51], v[160:163]\n\t" \
  "ds_read_b64_tr_b16 v[208:209], v232 offset:36864\n\t" \
  "v_mfma_f32_16x16x32_bf16 v[160:163], v[220:223], v[56:59], v[160:163]\n\t" \
  "ds_read_b64_tr_b16 v[210:211], v232 offset:38912\n\t" \
  "s_add_u32 s78, s78, 4096\n\t" \
  "buffer_load_dwordx4 v[128:131], v244, s[48:51], s78 offen offset:0 sc1\n\t" \
  "v_mfma_f32_16x16x32_bf16 v[160:163], v[192:195], v[64:67], v[160:163]\n\t" \
  "ds_read_b64_tr_b16 v[212:213], v232 offset:40960\n\t" \
  "v_mfma_f32_16x16x32_bf16 v[164:167], v[216:219], v[52:55], v[164:167]\n\t" \
  "ds_read_b64_tr_b16 v[214:215], v232 offset:43008\n\t" \
  "v_mfma_f32_16x16x32_bf16 v[164:167], v[220:223], v[60:63], v[164:167]\n\t" \
  "ds_read_b64_tr_b16 v[216:217], v232 offset:45056\n\t" \
  "buffer_load_dwordx4 v[132:135], v244, s[48:51], s78 offen offset:1024 sc1\n\t" \
  "v_mfma_f32_16x16x32_bf16 v[164:167], v[192:195], v[68:71], v[164:167]\n\t" \
  "ds_read_b64_tr_b16 v[218:219], v232 offset:47104\n\t" \
  "s_waitcnt lgkmcnt(6)\n\t" \
  "v_mfma_f32_16x16x32_bf16 v[168:171], v[196:199], v[48:51], v[168:171]\n\t" \
  "ds_read_b64_tr_b16 v[220:221], v233 offset:36864\n\t" \
  "v_mfma_f32_16x16x32_bf16 v[168:171], v[200:203], v[56:59], v[168:171]\n\t" \
  "ds_read_b64_tr_b16 v[222:223], v233 offset:38912\n\t" \
  "buffer_load_dwordx4 v[136:139], v244, s[48:51], s78 offen offset:2048 sc1\n\t" \
  "v_mfma_f32_16x16x32_bf16 v[168:171], v[204:207], v[64:67], v[168:171]\n\t" \
  "ds_read_b64_tr_b16 v[192:193], v233 offset:40960\n\t" \
  "v_mfma_f32_16x16x32_bf16 v[172:175], v[196:199], v[52:55], v[172:175]\n\t" \
  "ds_read_b64_tr_b16 v[194:195], v233 offset:43008\n\t" \
  "v_mfma_f32_16x16x32_bf16 v[172:175], v[200:203], v[60:63], v[172:175]\n\t" \
  "ds_read_b64_tr_b16 v[196:197], v233 offset:45056\n\t" \
  "buffer_load_dwordx4 v[140:143], v244, s[48:51], s78 offen offset:3072 sc1\n\t" \
  "v_mfma_f32_16x16x32_bf16 v[172:175], v[204:207], v[68:71], v[172:175]\n\t" \
  "ds_read_b64_tr_b16 v[198:199], v233 offset:47104\n\t" \
  "s_waitcnt lgkmcnt(6)\n\t" \
  "s_waitcnt vmcnt(12)\n\t" \
  "v_mfma_f32_16x16x32_bf16 v[144:147], v[208:211], v[72:75], v[144:147]\n\t" \
  "ds_read_b64_tr_b16 v[200:201], v234 offset:36864\n\t" \
  "v_mfma_f32_16x16x32_bf16 v[144:147], v[212:215], v[80:83], v[144:147]\n\t" \
  "ds_read_b64_tr_b16 v[202:203], v234 offset:38912\n\t" \
  "v_mfma_f32_16x16x32_bf16 v[144:147], v[216:219], v[88:91], v[144:147]\n\t" \
  "ds_read_b64_tr_b16 v[204:205], v234 offset:40960\n\t" \
  "v_mfma_f32_16x16x32_bf16 v[148:151], v[208:211], v[76:79], v[148:151]\n\t" \
  "ds_read_b64_tr_b16 v[206:207], v234 offset:43008\n\t" \
  "v_mfma_f32_16x16x32_bf16 v[148:151], v[212:215], v[84:87], v[148:151]\n\t" \
  "ds_read_b64_tr_b16 v[208:209], v234 offset:45056\n\t" \
  "v_mfma_f32_16x16x32_bf16 v[148:151], v[216:219], v[92:95], v[148:151]\n\t" \
  "ds_read_b64_tr_b16 v[210:211], v234 offset:47104\n\t" \
  "s_waitcnt lgkmcnt(6)\n\t" \
  "v_mfma_f32_16x16x32_bf16 v[152:155], v[220:223], v[72:75], v[152:155]\n\t" \
  "ds_read_b64_tr_b16 v[212:213], v235 offset:36864\n\t" \
  "v_mfma_f32_16x16x32_bf16 v[152:155], v[192:195], v[80:83], v[152:155]\n\t" \
  "ds_read_b64_tr_b16 v[214:215], v235 offset:38912\n\t" \
  "v_mfma_f32_16x16x32_bf16 v[152:155], v[196:199], v[88:91], v[152:155]\n\t" \
  "ds_read_b64_tr_b16 v[216:217], v235 offset:40960\n\t" \
  "v_mfma_f32_16x16x32_bf16 v[156:159], v[220:223], v[76:79], v[156:159]\n\t" \
  "ds_read_b64_tr_b16 v[218:219], v235 offset:43008\n\t" \
  "v_mfma_f32_16x16x32_bf16 v[156:159], v[192:195], v[84:87], v[156:159]\n\t" \
  "ds_read_b64_tr_b16 v[220:221], v235 offset:45056\n\t" \
  "v_mfma_f32_16x16x32_bf16 v[156:159], v[196:199], v[92:95], v[156:159]\n\t" \
  "ds_read_b64_tr_b16 v[222:223], v235 offset:47104\n\t" \
  "s_waitcnt lgkmcnt(6)\n\t" \
  "v_mfma_f32_16x16x32_bf16 v[160:163], v[200:203], v[72:75], v[160:163]\n\t" \
  "ds_read_b64_tr_b16 v[192:193], v232 offset:49152\n\t" \
  "v_mfma_f32_16x16x32_bf16 v[160:163], v[204:207], v[80:83], v[160:163]\n\t" \
  "ds_read_b64_tr_b16 v[194:195], v232 offset:51200\n\t" \
  "v_mfma_f32_16x16x32_bf16 v[160:163], v[208:211], v[88:91], v[160:163]\n\t" \
  "ds_read_b64_tr_b16 v[196:197], v232 offset:53248\n\t" \
  "v_mfma_f32_16x16x32_bf16 v[164:167], v[200:203], v[76:79], v[164:167]\n\t" \
  "ds_read_b64_tr_b16 v[198:199], v232 offset:55296\n\t" \
  "v_mfma_f32_16x16x32_bf16 v[164:167], v[204:207], v[84:87], v[164:167]\n\t" \
  "ds_read_b64_tr_b16 v[200:201], v232 offset:57344\n\t" \
  "v_mfma_f32_16x16x32_bf16 v[164:167], v[208:211], v[92:95], v[164:167]\n\t" \
  "ds_read_b64_tr_b16 v[202:203], v232 offset:59392\n\t" \
  "s_waitcnt lgkmcnt(6)\n\t" \
  "v_mfma_f32_16x16x32_bf16 v[168:171], v[212:215], v[72:75], v[168:171]\n\t" \
  "ds_read_b64_tr_b16 v[204:205], v233 offset:49152\n\t" \
  "v_mfma_f32_16x16x32_bf16 v[168:171], v[216:219], v[80:83], v[168:171]\n\t" \
  "ds_read_b64_tr_b16 v[206:207], v233 offset:51200\n\t" \
  "v_mfma_f32_16x16x32_bf16 v[168:171], v[220:223], v[88:91], v[168:171]\n\t" \
  "ds_read_b64_tr_b16 v[208:209], v233 offset:53248\n\t" \
  "v_mfma_f32_16x16x32_bf16 v[172:175], v[212:215], v[76:79], v[172:175]\n\t" \
  "ds_read_b64_tr_b16 v[210:211], v233 offset:55296\n\t" \
  "v_mfma_f32_16x16x32_bf16 v[172:175], v[216:219], v[84:87], v[172:175]\n\t" \
  "ds_read_b64_tr_b16 v[212:213], v233 offset:57344\n\t" \
  "v_mfma_f32_16x16x32_bf16 v[172:175], v[220:223], v[92:95], v[172:175]\n\t" \
  "ds_read_b64_tr_b16 v[214:215], v233 offset:59392\n\t" \
  "s_waitcnt lgkmcnt(6)\n\t" \
  "s_waitcnt vmcnt(6)\n\t" \
  "v_mfma_f32_16x16x32_bf16 v[144:147], v[192:195], v[96:99], v[144:147]\n\t" \
  "ds_read_b64_tr_b16 v[216:217], v234 offset:49152\n\t" \
  "v_mfma_f32_16x16x32_bf16 v[144:147], v[196:199], v[104:107], v[144:147]\n\t" \
  "ds_read_b64_tr_b16 v[218:219], v234 offset:51200\n\t" \
  "v_mfma_f32_16x16x32_bf16 v[144:147], v[200:203], v[112:115], v[144:147]\n\t" \
  "ds_read_b64_tr_b16 v[220:221], v234 offset:53248\n\t" \
  "v_mfma_f32_16x16x32_bf16 v[148:151], v[192:195], v[100:103], v[148:151]\n\t" \
  "ds_read_b64_tr_b16 v[222:223], v234 offset:55296\n\t" \
  "v_mfma_f32_16x16x32_bf16 v[148:151], v[196:199], v[108:111], v[148:151]\n\t" \
  "ds_read_b64_tr_b16 v[192:193], v234 offset:57344\n\t" \
  "v_mfma_f32_16x16x32_bf16 v[148:151], v[200:203], v[116:119], v[148:151]\n\t" \
  "ds_read_b64_tr_b16 v[194:195], v234 offset:59392\n\t" \
  "s_waitcnt lgkmcnt(6)\n\t" \
  "v_mfma_f32_16x16x32_bf16 v[152:155], v[204:207], v[96:99], v[152:155]\n\t" \
  "ds_read_b64_tr_b16 v[196:197], v235 offset:49152\n\t" \
  "v_mfma_f32_16x16x32_bf16 v[152:155], v[208:211], v[104:107], v[152:155]\n\t" \
  "ds_read_b64_tr_b16 v[198:199], v235 offset:51200\n\t" \
  "v_mfma_f32_16x16x32_bf16 v[152:155], v[212:215], v[112:115], v[152:155]\n\t" \
  "ds_read_b64_tr_b16 v[200:201], v235 offset:53248\n\t" \
  "v_mfma_f32_16x16x32_bf16 v[156:159], v[204:207], v[100:103], v[156:159]\n\t" \
  "ds_read_b64_tr_b16 v[202:203], v235 offset:55296\n\t" \
  "v_mfma_f32_16x16x32_bf16 v[156:159], v[208:211], v[108:111], v[156:159]\n\t" \
  "ds_read_b64_tr_b16 v[204:205], v235 offset:57344\n\t" \
  "v_mfma_f32_16x16x32_bf16 v[156:159], v[212:215], v[116:119], v[156:159]\n\t" \
  "ds_read_b64_tr_b16 v[206:207], v235 offset:59392\n\t" \
  "s_waitcnt lgkmcnt(6)\n\t" \
  "v_mfma_f32_16x16x32_bf16 v[160:163], v[216:219], v[96:99], v[160:163]\n\t" \
  "ds_read_b64_tr_b16 v[208:209], v236 offset:0\n\t" \
  "v_mfma_f32_16x16x32_bf16 v[160:163], v[220:223], v[104:107], v[160:163]\n\t" \
  "ds_read_b64_tr_b16 v[210:211], v236 offset:2048\n\t" \
  "v_mfma_f32_16x16x32_bf16 v[160:163], v[192:195], v[112:115], v[160:163]\n\t" \
  "ds_read_b64_tr_b16 v[212:213], v236 offset:4096\n\t" \
  "v_mfma_f32_16x16x32_bf16 v[164:167], v[216:219], v[100:103], v[164:167]\n\t" \
  "ds_read_b64_tr_b16 v[214:215], v236 offset:6144\n\t" \
  "v_mfma_f32_16x16x32_bf16 v[164:167], v[220:223], v[108:111], v[164:167]\n\t" \
  "ds_read_b64_tr_b16 v[216:217], v236 offset:8192\n\t" \
  "v_mfma_f32_16x16x32_bf16 v[164:167], v[192:195], v[116:119], v[164:167]\n\t" \
  "ds_read_b64_tr_b16 v[218:219], v236 offset:10240\n\t" \
  "s_waitcnt lgkmcnt(6)\n\t" \
  "v_mfma_f32_16x16x32_bf16 v[168:171], v[196:199], v[96:99], v[168:171]\n\t" \
  "ds_read_b64_tr_b16 v[220:221], v237 offset:0\n\t" \
  "v_mfma_f32_16x16x32_bf16 v[168:171], v[200:203], v[104:107], v[168:171]\n\t" \
  "ds_read_b64_tr_b16 v[222:223], v237 offset:2048\n\t" \
  "v_mfma_f32_16x16x32_bf16 v[168:171], v[204:207], v[112:115], v[168:171]\n\t" \
  "ds_read_b64_tr_b16 v[192:193], v237 offset:4096\n\t" \
  "v_mfma_f32_16x16x32_bf16 v[172:175], v[196:199], v[100:103], v[172:175]\n\t" \
  "ds_read_b64_tr_b16 v[194:195], v237 offset:6144\n\t" \
  "v_mfma_f32_16x16x32_bf16 v[172:175], v[200:203], v[108:111], v[172:175]\n\t" \
  "ds_read_b64_tr_b16 v[196:197], v237 offset:8192\n\t" \
  "v_mfma_f32_16x16x32_bf16 v[172:175], v[204:207], v[116:119], v[172:175]\n\t" \
  "ds_read_b64_tr_b16 v[198:199], v237 offset:10240\n\t" \
  "s_waitcnt lgkmcnt(6)\n\t" \
  "s_waitcnt vmcnt(0)\n\t" \
  "v_mfma_f32_16x16x32_bf16 v[144:147], v[208:211], v[120:123], v[144:147]\n\t" \
  "ds_read_b64_tr_b16 v[200:201], v238 offset:0\n\t" \
  "v_mfma_f32_16x16x32_bf16 v[144:147], v[212:215], v[128:131], v[144:147]\n\t" \
  "ds_read_b64_tr_b16 v[202:203], v238 offset:2048\n\t" \
  "v_mfma_f32_16x16x32_bf16 v[144:147], v[216:219], v[136:139], v[144:147]\n\t" \
  "ds_read_b64_tr_b16 v[204:205], v238 offset:4096\n\t" \
  "v_mfma_f32_16x16x32_bf16 v[148:151], v[208:211], v[124:127], v[148:151]\n\t" \
  "ds_read_b64_tr_b16 v[206:207], v238 offset:6144\n\t" \
  "v_mfma_f32_16x16x32_bf16 v[148:151], v[212:215], v[132:135], v[148:151]\n\t" \
  "ds_read_b64_tr_b16 v[208:209], v238 offset:8192\n\t" \
  "v_mfma_f32_16x16x32_bf16 v[148:151], v[216:219], v[140:143], v[148:151]\n\t" \
  "ds_read_b64_tr_b16 v[210:211], v238 offset:10240\n\t" \
  "s_waitcnt lgkmcnt(6)\n\t" \
  "v_mfma_f32_16x16x32_bf16 v[152:155], v[220:223], v[120:123], v[152:155]\n\t" \
  "ds_read_b64_tr_b16 v[212:213], v239 offset:0\n\t" \
  "v_mfma_f32_16x16x32_bf16 v[152:155], v[192:195], v[128:131], v[152:155]\n\t" \
  "ds_read_b64_tr_b16 v[214:215], v239 offset:2048\n\t" \
  "v_mfma_f32_16x16x32_bf16 v[152:155], v[196:199], v[136:139], v[152:155]\n\t" \
  "ds_read_b64_tr_b16 v[216:217], v239 offset:4096\n\t" \
  "s_barrier\n\t" \
  "v_mfma_f32_16x16x32_bf16 v[156:159], v[220:223], v[124:127], v[156:159]\n\t" \
  "ds_read_b64_tr_b16 v[218:219], v239 offset:6144\n\t" \
  "ds_read_b32 v245, v231\n\t" \
  "ds_read_b32 v246, v231 offset:64\n\t" \
  "v_mfma_f32_16x16x32_bf16 v[156:159], v[192:195], v[132:135], v[156:159]\n\t" \
  "ds_read_b64_tr_b16 v[220:221], v239 offset:8192\n\t" \
  "v_mfma_f32_16x16x32_bf16 v[156:159], v[196:199], v[140:143], v[156:159]\n\t" \
  "ds_read_b64_tr_b16 v[222:223], v239 offset:10240\n\t" \
  "s_waitcnt lgkmcnt(8)\n\t" \
  "v_mfma_f32_16x16x32_bf16 v[160:163], v[200:203], v[120:123], v[160:163]\n\t" \
  "ds_read_b64_tr_b16 v[192:193], v236 offset:12288\n\t" \
  "v_mfma_f32_16x16x32_bf16 v[160:163], v[204:207], v[128:131], v[160:163]\n\t" \
  "ds_read_b64_tr_b16 v[194:195], v236 offset:14336\n\t" \
  "v_mfma_f32_16x16x32_bf16 v[160:163], v[208:211], v[136:139], v[160:163]\n\t" \
  "ds_read_b64_tr_b16 v[196:197], v236 offset:16384\n\t" \
  "v_mfma_f32_16x16x32_bf16 v[164:167], v[200:203], v[124:127], v[164:167]\n\t" \
  "ds_read_b64_tr_b16 v[198:199], v236 offset:18432\n\t" \
  "v_mfma_f32_16x16x32_bf16 v[164:167], v[204:207], v[132:135], v[164:167]\n\t" \
  "ds_read_b64_tr_b16 v[200:201], v236 offset:20480\n\t" \
  "v_mfma_f32_16x16x32_bf16 v[164:167], v[208:211], v[140:143], v[164:167]\n\t" \
  "ds_read_b64_tr_b16 v[202:203], v236 offset:22528\n\t" \
  "s_waitcnt lgkmcnt(6)\n\t" \
  "v_mfma_f32_16x16x32_bf16 v[168:171], v[212:215], v[120:123], v[168:171]\n\t" \
  "ds_read_b64_tr_b16 v[204:205], v237 offset:12288\n\t" \
  "v_mul_f32 v224, v245, v144\n\t" \
  "v_mul_f32 v225, v245, v145\n\t" \
  "v_fmaak_f32 v226, v224, v224, 0x409d30fc\n\t" \
  "v_mfma_f32_16x16x32_bf16 v[168:171], v[216:219], v[128:131], v[168:171]\n\t" \
  "ds_read_b64_tr_b16 v[206:207], v237 offset:14336\n\t" \
  "v_fmaak_f32 v227, v225, v225, 0x409d30fc\n\t" \
  "v_mul_f32 v224, v224, v226\n\t" \
  "v_mul_f32 v225, v225, v227\n\t" \
  "v_mfma_f32_16x16x32_bf16 v[168:171], v[220:223], v[136:139], v[168:171]\n\t" \
  "ds_read_b64_tr_b16 v[208:209], v237 offset:16384\n\t" \
  "v_exp_f32 v224, v224\n\t" \
  "v_mfma_f32_16x16x32_bf16 v[172:175], v[212:215], v[124:127], v[172:175]\n\t" \
  "ds_read_b64_tr_b16 v[210:211], v237 offset:18432\n\t" \
  "v_exp_f32 v225, v225\n\t" \
  "v_add_f32 v224, 1.0, v224\n\t" \
  "v_mfma_f32_16x16x32_bf16 v[172:175], v[216:219], v[132:135], v[172:175]\n\t" \
  "ds_read_b64_tr_b16 v[212:213], v237 offset:20480\n\t" \
  "v_add_f32 v225, 1.0, v225\n\t" \
  "v_rcp_f32 v224, v224\n\t" \
  "v_mfma_f32_16x16x32_bf16 v[172:175], v[220:223], v[140:143], v[172:175]\n\t" \
  "ds_read_b64_tr_b16 v[214:215], v237 offset:22528\n\t" \
  "v_rcp_f32 v225, v225\n\t" \
  "s_waitcnt lgkmcnt(6)\n\t" \
  "v_mfma_f32_16x16x32_bf16 v[176:179], v[192:195], v[0:3], 0\n\t" \
  "ds_read_b64_tr_b16 v[216:217], v236 offset:24576\n\t" \
  "v_mul_f32 v224, v224, v144\n\t" \
  "v_mul_f32 v225, v225, v145\n\t" \
  "v_cvt_pk_bf16_f32 v144, v224, v225\n\t" \
  "v_mfma_f32_16x16x32_bf16 v[176:179], v[196:199], v[8:11], v[176:179]\n\t" \
  "ds_read_b64_tr_b16 v[218:219], v236 offset:26624\n\t" \
  "v_mul_f32 v224, v245, v146\n\t" \
  "v_mul_f32 v225, v245, v147\n\t" \
  "v_fmaak_f32 v226, v224, v224, 0x409d30fc\n\t" \
  "v_mfma_f32_16x16x32_bf16 v[176:179], v[200:203], v[16:19], v[176:179]\n\t" \
  "ds_read_b64_tr_b16 v[220:221], v236 offset:28672\n\t" \
  "v_fmaak_f32 v227, v225, v225, 0x409d30fc\n\t" \
  "v_mul_f32 v224, v224, v226\n\t" \
  "v_mfma_f32_16x16x32_bf16 v[180:183], v[192:195], v[4:7], 0\n\t" \
  "ds_read_b64_tr_b16 v[222:223], v236 offset:30720\n\t" \
  "v_mul_f32 v225, v225, v227\n\t" \
  "v_exp_f32 v224, v224\n\t" \
  "v_mfma_f32_16x16x32_bf16 v[180:183], v[196:199], v[12:15], v[180:183]\n\t" \
  "ds_read_b64_tr_b16 v[192:193], v236 offset:32768\n\t" \
  "v_exp_f32 v225, v225\n\t" \
  "v_add_f32 v224, 1.0, v224\n\t" \
  "v_mfma_f32_16x16x32_bf16 v[180:183], v[200:203], v[20:23], v[180:183]\n\t" \
  "ds_read_b64_tr_b16 v[194:195], v236 offset:34816\n\t" \
  "v_add_f32 v225, 1.0, v225\n\t" \
  "v_rcp_f32 v224, v224\n\t" \
  "s_waitcnt lgkmcnt(6)\n\t" \
  "v_mfma_f32_16x16x32_bf16 v[184:187], v[204:207], v[0:3], 0\n\t" \
  "ds_read_b64_tr_b16 v[196:197], v237 offset:24576\n\t" \
  "v_rcp_f32 v225, v225\n\t" \
  "v_mfma_f32_16x16x32_bf16 v[184:187], v[208:211], v[8:11], v[184:187]\n\t" \
  "ds_read_b64_tr_b16 v[198:199], v237 offset:26624\n\t" \
  "v_mul_f32 v224, v224, v146\n\t" \
  "v_mul_f32 v225, v225, v147\n\t" \
  "v_cvt_pk_bf16_f32 v145, v224, v225\n\t" \
  "v_mfma_f32_16x16x32_bf16 v[184:187], v[212:215], v[16:19], v[184:187]\n\t" \
  "ds_read_b64_tr_b16 v[200:201], v237 offset:28672\n\t" \
  "v_mul_f32 v224, v245, v152\n\t" \
  "v_mul_f32 v225, v245, v153\n\t" \
  "v_mfma_f32_16x16x32_bf16 v[188:191], v[204:207], v[4:7], 0\n\t" \
  "ds_read_b64_tr_b16 v[202:203], v237 offset:30720\n\t" \
  "v_fmaak_f32 v226, v224, v224, 0x409d30fc\n\t" \
  "v_fmaak_f32 v227, v225, v225, 0x409d30fc\n\t" \
  "v_mul_f32 v224, v224, v226\n\t" \
  "v_mfma_f32_16x16x32_bf16 v[188:191], v[208:211], v[12:15], v[188:191]\n\t" \
  "ds_read_b64_tr_b16 v[204:205], v237 offset:32768\n\t" \
  "v_mul_f32 v225, v225, v227\n\t" \
  "v_exp_f32 v224, v224\n\t" \
  "v_mfma_f32_16x16x32_bf16 v[188:191], v[212:215], v[20:23], v[188:191]\n\t" \
  "ds_read_b64_tr_b16 v[206:207], v237 offset:34816\n\t" \
  "v_exp_f32 v225, v225\n\t" \
  "s_waitcnt lgkmcnt(6)\n\t" \
  "v_mfma_f32_16x16x32_bf16 v[176:179], v[216:219], v[24:27], v[176:179]\n\t" \
  "ds_read_b64_tr_b16 v[208:209], v236 offset:36864\n\t" \
  "v_add_f32 v224, 1.0, v224\n\t" \
  "v_add_f32 v225, 1.0, v225\n\t" \
  "v_rcp_f32 v224, v224\n\t" \
  "v_mfma_f32_16x16x32_bf16 v[176:179], v[220:223], v[32:35], v[176:179]\n\t" \
  "ds_read_b64_tr_b16 v[210:211], v236 offset:38912\n\t" \
  "v_rcp_f32 v225, v225\n\t" \
  "v_mfma_f32_16x16x32_bf16 v[176:179], v[192:195], v[40:43], v[176:179]\n\t" \
  "ds_read_b64_tr_b16 v[212:213], v236 offset:40960\n\t" \
  "v_mul_f32 v224, v224, v152\n\t" \
  "v_mul_f32 v225, v225, v153\n\t" \
  "v_mfma_f32_16x16x32_bf16 v[180:183], v[216:219], v[28:31], v[180:183]\n\t" \
  "ds_read_b64_tr_b16 v[214:215], v236 offset:43008\n\t" \
  "v_cvt_pk_bf16_f32 v146, v224, v225\n\t" \
  "v_mul_f32 v224, v245, v154\n\t" \
  "v_mul_f32 v225, v245, v155\n\t" \
  "v_mfma_f32_16x16x32_bf16 v[180:183], v[220:223], v[36:39], v[180:183]\n\t" \
  "ds_read_b64_tr_b16 v[216:217], v236 offset:45056\n\t" \
  "v_fmaak_f32 v226, v224, v224, 0x409d30fc\n\t" \
  "v_fmaak_f32 v227, v225, v225, 0x409d30fc\n\t" \
  "v_mul_f32 v224, v224, v226\n\t" \
  "v_mfma_f32_16x16x32_bf16 v[180:183], v[192:195], v[44:47], v[180:183]\n\t" \
  "ds_read_b64_tr_b16 v[218:219], v236 offset:47104\n\t" \
  "v_mul_f32 v225, v225, v227\n\t" \
  "v_exp_f32 v224, v224\n\t" \
  "s_waitcnt lgkmcnt(6)\n\t" \
  "v_mfma_f32_16x16x32_bf16 v[184:187], v[196:199], v[24:27], v[184:187]\n\t" \
  "ds_read_b64_tr_b16 v[220:221], v237 offset:36864\n\t" \
  "v_exp_f32 v225, v225\n\t" \
  "v_mfma_f32_16x16x32_bf16 v[184:187], v[200:203], v[32:35], v[184:187]\n\t" \
  "ds_read_b64_tr_b16 v[222:223], v237 offset:38912\n\t" \
  "v_add_f32 v224, 1.0, v224\n\t" \
  "v_add_f32 v225, 1.0, v225\n\t" \
  "v_rcp_f32 v224, v224\n\t" \
  "v_mfma_f32_16x16x32_bf16 v[184:187], v[204:207], v[40:43], v[184:187]\n\t" \
  "ds_read_b64_tr_b16 v[192:193], v237 offset:40960\n\t" \
  "v_rcp_f32 v225, v225\n\t" \
  "v_mfma_f32_16x16x32_bf16 v[188:191], v[196:199], v[28:31], v[188:191]\n\t" \
  "ds_read_b64_tr_b16 v[194:195], v237 offset:43008\n\t" \
  "v_mul_f32 v224, v224, v154\n\t" \
  "v_mul_f32 v225, v225, v155\n\t" \
  "v_mfma_f32_16x16x32_bf16 v[188:191], v[200:203], v[36:39], v[188:191]\n\t" \
  "ds_read_b64_tr_b16 v[196:197], v237 offset:45056\n\t" \
  "v_cvt_pk_bf16_f32 v147, v224, v225\n\t" \
  "s_add_u32 s75, s71, 0\n\t" \
  "buffer_store_dwordx4 v[144:147], v244, s[48:51], s75 offen offset:0 sc1\n\t" \
  "v_mfma_f32_16x16x32_bf16 v[188:191], v[204:207], v[44:47], v[188:191]\n\t" \
  "ds_read_b64_tr_b16 v[198:199], v237 offset:47104\n\t" \
  "v_mul_f32 v224, v246, v148\n\t" \
  "v_mul_f32 v225, v246, v149\n\t" \
  "v_fmaak_f32 v226, v224, v224, 0x409d30fc\n\t" \
  "s_waitcnt lgkmcnt(6)\n\t" \
  "v_mfma_f32_16x16x32_bf16 v[176:179], v[208:211], v[48:51], v[176:179]\n\t" \
  "ds_read_b64_tr_b16 v[200:201], v236 offset:49152\n\t" \
  "v_fmaak_f32 v227, v225, v225, 0x409d30fc\n\t" \
  "v_mul_f32 v224, v224, v226\n\t" \
  "v_mul_f32 v225, v225, v227\n\t" \
  "v_mfma_f32_16x16x32_bf16 v[176:179], v[212:215], v[56:59], v[176:179]\n\t" \
  "ds_read_b64_tr_b16 v[202:203], v236 offset:51200\n\t" \
  "v_exp_f32 v224, v224\n\t" \
  "v_mfma_f32_16x16x32_bf16 v[176:179], v[216:219], v[64:67], v[176:179]\n\t" \
  "ds_read_b64_tr_b16 v[204:205], v236 offset:53248\n\t" \
  "v_exp_f32 v225, v225\n\t" \
  "v_add_f32 v224, 1.0, v224\n\t" \
  "v_mfma_f32_16x16x32_bf16 v[180:183], v[208:211], v[52:55], v[180:183]\n\t" \
  "ds_read_b64_tr_b16 v[206:207], v236 offset:55296\n\t" \
  "v_add_f32 v225, 1.0, v225\n\t" \
  "v_rcp_f32 v224, v224\n\t" \
  "v_mfma_f32_16x16x32_bf16 v[180:183], v[212:215], v[60:63], v[180:183]\n\t" \
  "ds_read_b64_tr_b16 v[208:209], v236 offset:57344\n\t" \
  "v_rcp_f32 v225, v225\n\t" \
  "v_mfma_f32_16x16x32_bf16 v[180:183], v[216:219], v[68:71], v[180:183]\n\t" \
  "ds_read_b64_tr_b16 v[210:211], v236 offset:59392\n\t" \
  "v_mul_f32 v224, v224, v148\n\t" \
  "v_mul_f32 v225, v225, v149\n\t" \
  "v_cvt_pk_bf16_f32 v148, v224, v225\n\t" \
  "s_waitcnt lgkmcnt(6)\n\t" \
  "v_mfma_f32_16x16x32_bf16 v[184:187], v[220:223], v[48:51], v[184:187]\n\t" \
  "ds_read_b64_tr_b16 v[212:213], v237 offset:49152\n\t" \
  "v_mul_f32 v224, v246, v150\n\t" \
  "v_mul_f32 v225, v246, v151\n\t" \
  "v_fmaak_f32 v226, v224, v224, 0x409d30fc\n\t" \
  "v_mfma_f32_16x16x32_bf16 v[184:187], v[192:195], v[56:59], v[184:187]\n\t" \
  "ds_read_b64_tr_b16 v[214:215], v237 offset:51200\n\t" \
  "v_fmaak_f32 v227, v225, v225, 0x409d30fc\n\t" \
  "v_mul_f32 v224, v224, v226\n\t" \
  "v_mfma_f32_16x16x32_bf16 v[184:187], v[196:199], v[64:67], v[184:187]\n\t" \
  "ds_read_b64_tr_b16 v[216:217], v237 offset:53248\n\t" \
  "v_mul_f32 v225, v225, v227\n\t" \
  "v_exp_f32 v224, v224\n\t" \
  "v_mfma_f32_16x16x32_bf16 v[188:191], v[220:223], v[52:55], v[188:191]\n\t" \
  "ds_read_b64_tr_b16 v[218:219], v237 offset:55296\n\t" \
  "v_exp_f32 v225, v225\n\t" \
  "v_add_f32 v224, 1.0, v224\n\t" \
  "v_mfma_f32_16x16x32_bf16 v[188:191], v[192:195], v[60:63], v[188:191]\n\t" \
  "ds_read_b64_tr_b16 v[220:221], v237 offset:57344\n\t" \
  "v_add_f32 v225, 1.0, v225\n\t" \
  "v_rcp_f32 v224, v224\n\t" \
  "v_mfma_f32_16x16x32_bf16 v[188:191], v[196:199], v[68:71], v[188:191]\n\t" \
  "ds_read_b64_tr_b16 v[222:223], v237 offset:59392\n\t" \
  "v_rcp_f32 v225, v225\n\t" \
  "s_waitcnt lgkmcnt(6)\n\t" \
  "v_mfma_f32_16x16x32_bf16 v[176:179], v[200:203], v[72:75], v[176:179]\n\t" \
  "ds_read_b64_tr_b16 v[192:193], v240 offset:0\n\t" \
  "v_mul_f32 v224, v224, v150\n\t" \
  "v_mul_f32 v225, v225, v151\n\t" \
  "v_cvt_pk_bf16_f32 v149, v224, v225\n\t" \
  "v_mfma_f32_16x16x32_bf16 v[176:179], v[204:207], v[80:83], v[176:179]\n\t" \
  "ds_read_b64_tr_b16 v[194:195], v240 offset:2048\n\t" \
  "v_mul_f32 v224, v246, v156\n\t" \
  "v_mul_f32 v225, v246, v157\n\t" \
  "v_mfma_f32_16x16x32_bf16 v[176:179], v[208:211], v[88:91], v[176:179]\n\t" \
  "ds_read_b64_tr_b16 v[196:197], v240 offset:4096\n\t" \
  "v_fmaak_f32 v226, v224, v224, 0x409d30fc\n\t" \
  "v_fmaak_f32 v227, v225, v225, 0x409d30fc\n\t" \
  "v_mul_f32 v224, v224, v226\n\t" \
  "v_mfma_f32_16x16x32_bf16 v[180:183], v[200:203], v[76:79], v[180:183]\n\t" \
  "ds_read_b64_tr_b16 v[198:199], v240 offset:6144\n\t" \
  "v_mul_f32 v225, v225, v227\n\t" \
  "v_exp_f32 v224, v224\n\t" \
  "v_mfma_f32_16x16x32_bf16 v[180:183], v[204:207], v[84:87], v[180:183]\n\t" \
  "ds_read_b64_tr_b16 v[200:201], v240 offset:8192\n\t" \
  "v_exp_f32 v225, v225\n\t" \
  "v_mfma_f32_16x16x32_bf16 v[180:183], v[208:211], v[92:95], v[180:183]\n\t" \
  "ds_read_b64_tr_b16 v[202:203], v240 offset:10240\n\t" \
  "v_add_f32 v224, 1.0, v224\n\t" \
  "v_add_f32 v225, 1.0, v225\n\t" \
  "v_rcp_f32 v224, v224\n\t" \
  "s_waitcnt lgkmcnt(6)\n\t" \
  "v_mfma_f32_16x16x32_bf16 v[184:187], v[212:215], v[72:75], v[184:187]\n\t" \
  "ds_read_b64_tr_b16 v[204:205], v241 offset:0\n\t" \
  "v_rcp_f32 v225, v225\n\t" \
  "v_mfma_f32_16x16x32_bf16 v[184:187], v[216:219], v[80:83], v[184:187]\n\t" \
  "ds_read_b64_tr_b16 v[206:207], v241 offset:2048\n\t" \
  "v_mul_f32 v224, v224, v156\n\t" \
  "v_mul_f32 v225, v225, v157\n\t" \
  "v_mfma_f32_16x16x32_bf16 v[184:187], v[220:223], v[88:91], v[184:187]\n\t" \
  "ds_read_b64_tr_b16 v[208:209], v241 offset:4096\n\t" \
  "v_cvt_pk_bf16_f32 v150, v224, v225\n\t" \
  "v_mul_f32 v224, v246, v158\n\t" \
  "v_mul_f32 v225, v246, v159\n\t" \
  "v_mfma_f32_16x16x32_bf16 v[188:191], v[212:215], v[76:79], v[188:191]\n\t" \
  "ds_read_b64_tr_b16 v[210:211], v241 offset:6144\n\t" \
  "v_fmaak_f32 v226, v224, v224, 0x409d30fc\n\t" \
  "v_fmaak_f32 v227, v225, v225, 0x409d30fc\n\t" \
  "v_mul_f32 v224, v224, v226\n\t" \
  "v_mfma_f32_16x16x32_bf16 v[188:191], v[216:219], v[84:87], v[188:191]\n\t" \
  "ds_read_b64_tr_b16 v[212:213], v241 offset:8192\n\t" \
  "v_mul_f32 v225, v225, v227\n\t" \
  "v_exp_f32 v224, v224\n\t" \
  "v_mfma_f32_16x16x32_bf16 v[188:191], v[220:223], v[92:95], v[188:191]\n\t" \
  "ds_read_b64_tr_b16 v[214:215], v241 offset:10240\n\t" \
  "v_exp_f32 v225, v225\n\t" \
  "s_waitcnt lgkmcnt(6)\n\t" \
  "v_mfma_f32_16x16x32_bf16 v[176:179], v[192:195], v[96:99], v[176:179]\n\t" \
  "ds_read_b64_tr_b16 v[216:217], v240 offset:12288\n\t" \
  "v_add_f32 v224, 1.0, v224\n\t" \
  "v_add_f32 v225, 1.0, v225\n\t" \
  "v_rcp_f32 v224, v224\n\t" \
  "v_mfma_f32_16x16x32_bf16 v[176:179], v[196:199], v[104:107], v[176:179]\n\t" \
  "ds_read_b64_tr_b16 v[218:219], v240 offset:14336\n\t" \
  "v_rcp_f32 v225, v225\n\t" \
  "v_mfma_f32_16x16x32_bf16 v[176:179], v[200:203], v[112:115], v[176:179]\n\t" \
  "ds_read_b64_tr_b16 v[220:221], v240 offset:16384\n\t" \
  "v_mul_f32 v224, v224, v158\n\t" \
  "v_mul_f32 v225, v225, v159\n\t" \
  "v_mfma_f32_16x16x32_bf16 v[180:183], v[192:195], v[100:103], v[180:183]\n\t" \
  "ds_read_b64_tr_b16 v[222:223], v240 offset:18432\n\t" \
  "v_cvt_pk_bf16_f32 v151, v224, v225\n\t" \
  "s_add_u32 s75, s71, 0\n\t" \
  "buffer_store_dwordx4 v[148:151], v244, s[48:51], s75 offen offset:1024 sc1\n\t" \
  "v_mfma_f32_16x16x32_bf16 v[180:183], v[196:199], v[108:111], v[180:183]\n\t" \
  "ds_read_b64_tr_b16 v[192:193], v240 offset:20480\n\t" \
  "v_mul_f32 v224, v245, v160\n\t" \
  "v_mul_f32 v225, v245, v161\n\t" \
  "v_fmaak_f32 v226, v224, v224, 0x409d30fc\n\t" \
  "v_mfma_f32_16x16x32_bf16 v[180:183], v[200:203], v[116:119], v[180:183]\n\t" \
  "ds_read_b64_tr_b16 v[194:195], v240 offset:22528\n\t" \
  "v_fmaak_f32 v227, v225, v225, 0x409d30fc\n\t" \
  "v_mul_f32 v224, v224, v226\n\t" \
  "s_waitcnt lgkmcnt(6)\n\t" \
  "v_mfma_f32_16x16x32_bf16 v[184:187], v[204:207], v[96:99], v[184:187]\n\t" \
  "ds_read_b64_tr_b16 v[196:197], v241 offset:12288\n\t" \
  "v_mul_f32 v225, v225, v227\n\t" \
  "v_exp_f32 v224, v224\n\t" \
  "v_mfma_f32_16x16x32_bf16 v[184:187], v[208:211], v[104:107], v[184:187]\n\t" \
  "ds_read_b64_tr_b16 v[198:199], v241 offset:14336\n\t" \
  "v_exp_f32 v225, v225\n\t" \
  "v_mfma_f32_16x16x32_bf16 v[184:187], v[212:215], v[112:115], v[184:187]\n\t" \
  "ds_read_b64_tr_b16 v[200:201], v241 offset:16384\n\t" \
  "v_add_f32 v224, 1.0, v224\n\t" \
  "v_mfma_f32_16x16x32_bf16 v[188:191], v[204:207], v[100:103], v[188:191]\n\t" \
  "ds_read_b64_tr_b16 v[202:203], v241 offset:18432\n\t" \
  "v_add_f32 v225, 1.0, v225\n\t" \
  "v_rcp_f32 v224, v224\n\t" \
  "v_mfma_f32_16x16x32_bf16 v[188:191], v[208:211], v[108:111], v[188:191]\n\t" \
  "ds_read_b64_tr_b16 v[204:205], v241 offset:20480\n\t" \
  "v_rcp_f32 v225, v225\n\t" \
  "v_mfma_f32_16x16x32_bf16 v[188:191], v[212:215], v[116:119], v[188:191]\n\t" \
  "ds_read_b64_tr_b16 v[206:207], v241 offset:22528\n\t" \
  "v_mul_f32 v224, v224, v160\n\t" \
  "v_mul_f32 v225, v225, v161\n\t" \
  "s_waitcnt lgkmcnt(6)\n\t" \
  "v_mfma_f32_16x16x32_bf16 v[176:179], v[216:219], v[120:123], v[176:179]\n\t" \
  "ds_read_b64_tr_b16 v[208:209], v238 offset:12288\n\t" \
  "v_cvt_pk_bf16_f32 v160, v224, v225\n\t" \
  "v_mul_f32 v224, v245, v162\n\t" \
  "v_mfma_f32_16x16x32_bf16 v[176:179], v[220:223], v[128:131], v[176:179]\n\t" \
  "ds_read_b64_tr_b16 v[210:211], v238 offset:14336\n\t" \
  "v_mul_f32 v225, v245, v163\n\t" \
  "v_fmaak_f32 v226, v224, v224, 0x409d30fc\n\t" \
  "v_mfma_f32_16x16x32_bf16 v[176:179], v[192:195], v[136:139], v[176:179]\n\t" \
  "ds_read_b64_tr_b16 v[212:213], v238 offset:16384\n\t" \
  "v_fmaak_f32 v227, v225, v225, 0x409d30fc\n\t" \
  "v_mul_f32 v224, v224, v226\n\t" \
  "v_mfma_f32_16x16x32_bf16 v[180:183], v[216:219], v[124:127], v[180:183]\n\t" \
  "ds_read_b64_tr_b16 v[214:215], v238 offset:18432\n\t" \
  "v_mul_f32 v225, v225, v227\n\t" \
  "v_exp_f32 v224, v224\n\t" \
  "v_mfma_f32_16x16x32_bf16 v[180:183], v[220:223], v[132:135], v[180:183]\n\t" \
  "ds_read_b64_tr_b16 v[216:217], v238 offset:20480\n\t" \
  "v_exp_f32 v225, v225\n\t" \
  "v_mfma_f32_16x16x32_bf16 v[180:183], v[192:195], v[140:143], v[180:183]\n\t" \
  "ds_read_b64_tr_b16 v[218:219], v238 offset:22528\n\t" \
  "v_add_f32 v224, 1.0, v224\n\t" \
  "s_waitcnt lgkmcnt(6)\n\t" \
  "v_mfma_f32_16x16x32_bf16 v[184:187], v[196:199], v[120:123], v[184:187]\n\t" \
  "ds_read_b64_tr_b16 v[220:221], v239 offset:12288\n\t" \
  "v_add_f32 v225, 1.0, v225\n\t" \
  "v_rcp_f32 v224, v224\n\t" \
  "v_mfma_f32_16x16x32_bf16 v[184:187], v[200:203], v[128:131], v[184:187]\n\t" \
  "ds_read_b64_tr_b16 v[222:223], v239 offset:14336\n\t" \
  "v_rcp_f32 v225, v225\n\t" \
  "v_mfma_f32_16x16x32_bf16 v[184:187], v[204:207], v[136:139], v[184:187]\n\t" \
  "ds_read_b64_tr_b16 v[192:193], v239 offset:16384\n\t" \
  "v_mul_f32 v224, v224, v162\n\t" \
  "v_mul_f32 v225, v225, v163\n\t" \
  "v_mfma_f32_16x16x32_bf16 v[188:191], v[196:199], v[124:127], v[188:191]\n\t" \
  "ds_read_b64_tr_b16 v[194:195], v239 offset:18432\n\t" \
  "v_cvt_pk_bf16_f32 v161, v224, v225\n\t" \
  "v_mul_f32 v224, v245, v168\n\t" \
  "v_mfma_f32_16x16x32_bf16 v[188:191], v[200:203], v[132:135], v[188:191]\n\t" \
  "ds_read_b64_tr_b16 v[196:197], v239 offset:20480\n\t" \
  "v_mul_f32 v225, v245, v169\n\t" \
  "v_fmaak_f32 v226, v224, v224, 0x409d30fc\n\t" \
  "v_mfma_f32_16x16x32_bf16 v[188:191], v[204:207], v[140:143], v[188:191]\n\t" \
  "ds_read_b64_tr_b16 v[198:199], v239 offset:22528\n\t" \
  "v_fmaak_f32 v227, v225, v225, 0x409d30fc\n\t" \
  "v_mul_f32 v224, v224, v226\n\t" \
  "s_waitcnt lgkmcnt(6)\n\t" \
  "v_mfma_f32_16x16x32_bf16 v[144:147], v[208:211], v[0:3], 0\n\t" \
  "ds_read_b64_tr_b16 v[200:201], v238 offset:24576\n\t" \
  "v_mul_f32 v225, v225, v227\n\t" \
  "v_exp_f32 v224, v224\n\t" \
  "v_mfma_f32_16x16x32_bf16 v[144:147], v[212:215], v[8:11], v[144:147]\n\t" \
  "ds_read_b64_tr_b16 v[202:203], v238 offset:26624\n\t" \
  "v_exp_f32 v225, v225\n\t" \
  "v_mfma_f32_16x16x32_bf16 v[144:147], v[216:219], v[16:19], v[144:147]\n\t" \
  "ds_read_b64_tr_b16 v[204:205], v238 offset:28672\n\t" \
  "v_add_f32 v224, 1.0, v224\n\t" \
  "v_add_f32 v225, 1.0, v225\n\t" \
  "v_mfma_f32_16x16x32_bf16 v[148:151], v[208:211], v[4:7], 0\n\t" \
  "ds_read_b64_tr_b16 v[206:207], v238 offset:30720\n\t" \
  "v_rcp_f32 v224, v224\n\t" \
  "v_mfma_f32_16x16x32_bf16 v[148:151], v[212:215], v[12:15], v[148:151]\n\t" \
  "ds_read_b64_tr_b16 v[208:209], v238 offset:32768\n\t" \
  "v_rcp_f32 v225, v225\n\t" \
  "v_mfma_f32_16x16x32_bf16 v[148:151], v[216:219], v[20:23], v[148:151]\n\t" \
  "ds_read_b64_tr_b16 v[210:211], v238 offset:34816\n\t" \
  "v_mul_f32 v224, v224, v168\n\t" \
  "v_mul_f32 v225, v225, v169\n\t" \
  "s_waitcnt lgkmcnt(6)\n\t" \
  "v_mfma_f32_16x16x32_bf16 v[152:155], v[220:223], v[0:3], 0\n\t" \
  "ds_read_b64_tr_b16 v[212:213], v239 offset:24576\n\t" \
  "v_cvt_pk_bf16_f32 v162, v224, v225\n\t" \
  "v_mul_f32 v224, v245, v170\n\t" \
  "v_mfma_f32_16x16x32_bf16 v[152:155], v[192:195], v[8:11], v[152:155]\n\t" \
  "ds_read_b64_tr_b16 v[214:215], v239 offset:26624\n\t" \
  "v_mul_f32 v225, v245, v171\n\t" \
  "v_fmaak_f32 v226, v224, v224, 0x409d30fc\n\t" \
  "v_mfma_f32_16x16x32_bf16 v[152:155], v[196:199], v[16:19], v[152:155]\n\t" \
  "ds_read_b64_tr_b16 v[216:217], v239 offset:28672\n\t" \
  "v_fmaak_f32 v227, v225, v225, 0x409d30fc\n\t" \
  "v_mul_f32 v224, v224, v226\n\t" \
  "v_mul_f32 v225, v225, v227\n\t" \
  "v_mfma_f32_16x16x32_bf16 v[156:159], v[220:223], v[4:7], 0\n\t" \
  "ds_read_b64_tr_b16 v[218:219], v239 offset:30720\n\t" \
  "v_exp_f32 v224, v224\n\t" \
  "v_mfma_f32_16x16x32_bf16 v[156:159], v[192:195], v[12:15], v[156:159]\n\t" \
  "ds_read_b64_tr_b16 v[220:221], v239 offset:32768\n\t" \
  "v_exp_f32 v225, v225\n\t" \
  "v_mfma_f32_16x16x32_bf16 v[156:159], v[196:199], v[20:23], v[156:159]\n\t" \
  "ds_read_b64_tr_b16 v[222:223], v239 offset:34816\n\t" \
  "v_add_f32 v224, 1.0, v224\n\t" \
  "v_add_f32 v225, 1.0, v225\n\t" \
  "s_waitcnt lgkmcnt(6)\n\t" \
  "v_mfma_f32_16x16x32_bf16 v[144:147], v[200:203], v[24:27], v[144:147]\n\t" \
  "ds_read_b64_tr_b16 v[192:193], v238 offset:36864\n\t" \
  "v_rcp_f32 v224, v224\n\t" \
  "v_mfma_f32_16x16x32_bf16 v[144:147], v[204:207], v[32:35], v[144:147]\n\t" \
  "ds_read_b64_tr_b16 v[194:195], v238 offset:38912\n\t" \
  "v_rcp_f32 v225, v225\n\t" \
  "v_mfma_f32_16x16x32_bf16 v[144:147], v[208:211], v[40:43], v[144:147]\n\t" \
  "ds_read_b64_tr_b16 v[196:197], v238 offset:40960\n\t" \
  "v_mul_f32 v224, v224, v170\n\t" \
  "v_mul_f32 v225, v225, v171\n\t" \
  "v_mfma_f32_16x16x32_bf16 v[148:151], v[200:203], v[28:31], v[148:151]\n\t" \
  "ds_read_b64_tr_b16 v[198:199], v238 offset:43008\n\t" \
  "v_cvt_pk_bf16_f32 v163, v224, v225\n\t" \
  "s_add_u32 s75, s71, 0\n\t" \
  "buffer_store_dwordx4 v[160:163], v244, s[48:51], s75 offen offset:2048 sc1\n\t" \
  "v_mfma_f32_16x16x32_bf16 v[148:151], v[204:207], v[36:39], v[148:151]\n\t" \
  "ds_read_b64_tr_b16 v[200:201], v238 offset:45056\n\t" \
  "v_mul_f32 v224, v246, v164\n\t" \
  "v_mul_f32 v225, v246, v165\n\t" \
  "v_fmaak_f32 v226, v224, v224, 0x409d30fc\n\t" \
  "v_mfma_f32_16x16x32_bf16 v[148:151], v[208:211], v[44:47], v[148:151]\n\t" \
  "ds_read_b64_tr_b16 v[202:203], v238 offset:47104\n\t" \
  "v_fmaak_f32 v227, v225, v225, 0x409d30fc\n\t" \
  "v_mul_f32 v224, v224, v226\n\t" \
  "s_waitcnt lgkmcnt(6)\n\t" \
  "v_mfma_f32_16x16x32_bf16 v[152:155], v[212:215], v[24:27], v[152:155]\n\t" \
  "ds_read_b64_tr_b16 v[204:205], v239 offset:36864\n\t" \
  "v_mul_f32 v225, v225, v227\n\t" \
  "v_exp_f32 v224, v224\n\t" \
  "v_mfma_f32_16x16x32_bf16 v[152:155], v[216:219], v[32:35], v[152:155]\n\t" \
  "ds_read_b64_tr_b16 v[206:207], v239 offset:38912\n\t" \
  "v_exp_f32 v225, v225\n\t" \
  "v_mfma_f32_16x16x32_bf16 v[152:155], v[220:223], v[40:43], v[152:155]\n\t" \
  "ds_read_b64_tr_b16 v[208:209], v239 offset:40960\n\t" \
  "v_add_f32 v224, 1.0, v224\n\t" \
  "v_mfma_f32_16x16x32_bf16 v[156:159], v[212:215], v[28:31], v[156:159]\n\t" \
  "ds_read_b64_tr_b16 v[210:211], v239 offset:43008\n\t" \
  "v_add_f32 v225, 1.0, v225\n\t" \
  "v_rcp_f32 v224, v224\n\t" \
  "v_mfma_f32_16x16x32_bf16 v[156:159], v[216:219], v[36:39], v[156:159]\n\t" \
  "ds_read_b64_tr_b16 v[212:213], v239 offset:45056\n\t" \
  "v_rcp_f32 v225, v225\n\t" \
  "v_mfma_f32_16x16x32_bf16 v[156:159], v[220:223], v[44:47], v[156:159]\n\t" \
  "ds_read_b64_tr_b16 v[214:215], v239 offset:47104\n\t" \
  "v_mul_f32 v224, v224, v164\n\t" \
  "v_mul_f32 v225, v225, v165\n\t" \
  "s_waitcnt lgkmcnt(6)\n\t" \
  "v_mfma_f32_16x16x32_bf16 v[144:147], v[192:195], v[48:51], v[144:147]\n\t" \
  "ds_read_b64_tr_b16 v[216:217], v238 offset:49152\n\t" \
  "v_cvt_pk_bf16_f32 v164, v224, v225\n\t" \
  "v_mul_f32 v224, v246, v166\n\t" \
  "v_mfma_f32_16x16x32_bf16 v[144:147], v[196:199], v[56:59], v[144:147]\n\t" \
  "ds_read_b64_tr_b16 v[218:219], v238 offset:51200\n\t" \
  "v_mul_f32 v225, v246, v167\n\t" \
  "v_fmaak_f32 v226, v224, v224, 0x409d30fc\n\t" \
  "v_mfma_f32_16x16x32_bf16 v[144:147], v[200:203], v[64:67], v[144:147]\n\t" \
  "ds_read_b64_tr_b16 v[220:221], v238 offset:53248\n\t" \
  "v_fmaak_f32 v227, v225, v225, 0x409d30fc\n\t" \
  "v_mul_f32 v224, v224, v226\n\t" \
  "v_mfma_f32_16x16x32_bf16 v[148:151], v[192:195], v[52:55], v[148:151]\n\t" \
  "ds_read_b64_tr_b16 v[222:223], v238 offset:55296\n\t" \
  "v_mul_f32 v225, v225, v227\n\t" \
  "v_exp_f32 v224, v224\n\t" \
  "v_mfma_f32_16x16x32_bf16 v[148:151], v[196:199], v[60:63], v[148:151]\n\t" \
  "ds_read_b64_tr_b16 v[192:193], v238 offset:57344\n\t" \
  "v_exp_f32 v225, v225\n\t" \
  "v_mfma_f32_16x16x32_bf16 v[148:151], v[200:203], v[68:71], v[148:151]\n\t" \
  "ds_read_b64_tr_b16 v[194:195], v238 offset:59392\n\t" \
  "v_add_f32 v224, 1.0, v224\n\t" \
  "s_waitcnt lgkmcnt(6)\n\t" \
  "v_mfma_f32_16x16x32_bf16 v[152:155], v[204:207], v[48:51], v[152:155]\n\t" \
  "ds_read_b64_tr_b16 v[196:197], v239 offset:49152\n\t" \
  "v_add_f32 v225, 1.0, v225\n\t" \
  "v_rcp_f32 v224, v224\n\t" \
  "v_mfma_f32_16x16x32_bf16 v[152:155], v[208:211], v[56:59], v[152:155]\n\t" \
  "ds_read_b64_tr_b16 v[198:199], v239 offset:51200\n\t" \
  "v_rcp_f32 v225, v225\n\t" \
  "v_mfma_f32_16x16x32_bf16 v[152:155], v[212:215], v[64:67], v[152:155]\n\t" \
  "ds_read_b64_tr_b16 v[200:201], v239 offset:53248\n\t" \
  "v_mul_f32 v224, v224, v166\n\t" \
  "v_mul_f32 v225, v225, v167\n\t" \
  "v_mfma_f32_16x16x32_bf16 v[156:159], v[204:207], v[52:55], v[156:159]\n\t" \
  "ds_read_b64_tr_b16 v[202:203], v239 offset:55296\n\t" \
  "v_cvt_pk_bf16_f32 v165, v224, v225\n\t" \
  "v_mul_f32 v224, v246, v172\n\t" \
  "v_mfma_f32_16x16x32_bf16 v[156:159], v[208:211], v[60:63], v[156:159]\n\t" \
  "ds_read_b64_tr_b16 v[204:205], v239 offset:57344\n\t" \
  "v_mul_f32 v225, v246, v173\n\t" \
  "v_fmaak_f32 v226, v224, v224, 0x409d30fc\n\t" \
  "v_mfma_f32_16x16x32_bf16 v[156:159], v[212:215], v[68:71], v[156:159]\n\t" \
  "ds_read_b64_tr_b16 v[206:207], v239 offset:59392\n\t" \
  "v_fmaak_f32 v227, v225, v225, 0x409d30fc\n\t" \
  "v_mul_f32 v224, v224, v226\n\t" \
  "s_waitcnt lgkmcnt(6)\n\t" \
  "v_mfma_f32_16x16x32_bf16 v[144:147], v[216:219], v[72:75], v[144:147]\n\t" \
  "ds_read_b64_tr_b16 v[208:209], v242 offset:0\n\t" \
  "v_mul_f32 v225, v225, v227\n\t" \
  "v_exp_f32 v224, v224\n\t" \
  "v_mfma_f32_16x16x32_bf16 v[144:147], v[220:223], v[80:83], v[144:147]\n\t" \
  "ds_read_b64_tr_b16 v[210:211], v242 offset:2048\n\t" \
  "v_exp_f32 v225, v225\n\t" \
  "v_mfma_f32_16x16x32_bf16 v[144:147], v[192:195], v[88:91], v[144:147]\n\t" \
  "ds_read_b64_tr_b16 v[212:213], v242 offset:4096\n\t" \
  "v_add_f32 v224, 1.0, v224\n\t" \
  "v_add_f32 v225, 1.0, v225\n\t" \
  "v_mfma_f32_16x16x32_bf16 v[148:151], v[216:219], v[76:79], v[148:151]\n\t" \
  "ds_read_b64_tr_b16 v[214:215], v242 offset:6144\n\t" \
  "v_rcp_f32 v224, v224\n\t" \
  "v_mfma_f32_16x16x32_bf16 v[148:151], v[220:223], v[84:87], v[148:151]\n\t" \
  "ds_read_b64_tr_b16 v[216:217], v242 offset:8192\n\t" \
  "v_rcp_f32 v225, v225\n\t" \
  "v_mfma_f32_16x16x32_bf16 v[148:151], v[192:195], v[92:95], v[148:151]\n\t" \
  "ds_read_b64_tr_b16 v[218:219], v242 offset:10240\n\t" \
  "v_mul_f32 v224, v224, v172\n\t" \
  "v_mul_f32 v225, v225, v173\n\t" \
  "s_waitcnt lgkmcnt(6)\n\t" \
  "v_mfma_f32_16x16x32_bf16 v[152:155], v[196:199], v[72:75], v[152:155]\n\t" \
  "ds_read_b64_tr_b16 v[220:221], v243 offset:0\n\t" \
  "v_cvt_pk_bf16_f32 v166, v224, v225\n\t" \
  "v_mul_f32 v224, v246, v174\n\t" \
  "v_mfma_f32_16x16x32_bf16 v[152:155], v[200:203], v[80:83], v[152:155]\n\t" \
  "ds_read_b64_tr_b16 v[222:223], v243 offset:2048\n\t" \
  "v_mul_f32 v225, v246, v175\n\t" \
  "v_fmaak_f32 v226, v224, v224, 0x409d30fc\n\t" \
  "v_mfma_f32_16x16x32_bf16 v[152:155], v[204:207], v[88:91], v[152:155]\n\t" \
  "ds_read_b64_tr_b16 v[192:193], v243 offset:4096\n\t" \
  "v_fmaak_f32 v227, v225, v225, 0x409d30fc\n\t" \
  "v_mul_f32 v224, v224, v226\n\t" \
  "v_mul_f32 v225, v225, v227\n\t" \
  "v_mfma_f32_16x16x32_bf16 v[156:159], v[196:199], v[76:79], v[156:159]\n\t" \
  "ds_read_b64_tr_b16 v[194:195], v243 offset:6144\n\t" \
  "v_exp_f32 v224, v224\n\t" \
  "v_mfma_f32_16x16x32_bf16 v[156:159], v[200:203], v[84:87], v[156:159]\n\t" \
  "ds_read_b64_tr_b16 v[196:197], v243 offset:8192\n\t" \
  "v_exp_f32 v225, v225\n\t" \
  "v_mfma_f32_16x16x32_bf16 v[156:159], v[204:207], v[92:95], v[156:159]\n\t" \
  "ds_read_b64_tr_b16 v[198:199], v243 offset:10240\n\t" \
  "v_add_f32 v224, 1.0, v224\n\t" \
  "v_add_f32 v225, 1.0, v225\n\t" \
  "s_waitcnt lgkmcnt(6)\n\t" \
  "v_mfma_f32_16x16x32_bf16 v[144:147], v[208:211], v[96:99], v[144:147]\n\t" \
  "ds_read_b64_tr_b16 v[200:201], v242 offset:12288\n\t" \
  "v_rcp_f32 v224, v224\n\t" \
  "v_mfma_f32_16x16x32_bf16 v[144:147], v[212:215], v[104:107], v[144:147]\n\t" \
  "ds_read_b64_tr_b16 v[202:203], v242 offset:14336\n\t" \
  "v_rcp_f32 v225, v225\n\t" \
  "v_mfma_f32_16x16x32_bf16 v[144:147], v[216:219], v[112:115], v[144:147]\n\t" \
  "ds_read_b64_tr_b16 v[204:205], v242 offset:16384\n\t" \
  "v_mul_f32 v224, v224, v174\n\t" \
  "v_mul_f32 v225, v225, v175\n\t" \
  "v_mfma_f32_16x16x32_bf16 v[148:151], v[208:211], v[100:103], v[148:151]\n\t" \
  "ds_read_b64_tr_b16 v[206:207], v242 offset:18432\n\t" \
  "v_cvt_pk_bf16_f32 v167, v224, v225\n\t" \
  "s_add_u32 s75, s71, 0\n\t" \
  "buffer_store_dwordx4 v[164:167], v244, s[48:51], s75 offen offset:3072 sc1\n\t" \
  "v_mfma_f32_16x16x32_bf16 v[148:151], v[212:215], v[108:111], v[148:151]\n\t" \
  "ds_read_b64_tr_b16 v[208:209], v242 offset:20480\n\t" \
  "v_mul_f32 v224, v245, v176\n\t" \
  "v_mul_f32 v225, v245, v177\n\t" \
  "v_fmaak_f32 v226, v224, v224, 0x409d30fc\n\t" \
  "v_mfma_f32_16x16x32_bf16 v[148:151], v[216:219], v[116:119], v[148:151]\n\t" \
  "ds_read_b64_tr_b16 v[210:211], v242 offset:22528\n\t" \
  "v_fmaak_f32 v227, v225, v225, 0x409d30fc\n\t" \
  "v_mul_f32 v224, v224, v226\n\t" \
  "s_waitcnt lgkmcnt(6)\n\t" \
  "v_mfma_f32_16x16x32_bf16 v[152:155], v[220:223], v[96:99], v[152:155]\n\t" \
  "ds_read_b64_tr_b16 v[212:213], v243 offset:12288\n\t" \
  "v_mul_f32 v225, v225, v227\n\t" \
  "v_exp_f32 v224, v224\n\t" \
  "v_mfma_f32_16x16x32_bf16 v[152:155], v[192:195], v[104:107], v[152:155]\n\t" \
  "ds_read_b64_tr_b16 v[214:215], v243 offset:14336\n\t" \
  "v_exp_f32 v225, v225\n\t" \
  "v_mfma_f32_16x16x32_bf16 v[152:155], v[196:199], v[112:115], v[152:155]\n\t" \
  "ds_read_b64_tr_b16 v[216:217], v243 offset:16384\n\t" \
  "v_add_f32 v224, 1.0, v224\n\t" \
  "s_barrier\n\t" \
  "v_mfma_f32_16x16x32_bf16 v[156:159], v[220:223], v[100:103], v[156:159]\n\t" \
  "ds_read_b64_tr_b16 v[218:219], v243 offset:18432\n\t" \
  "v_add_f32 v225, 1.0, v225\n\t" \
  "v_rcp_f32 v224, v224\n\t" \
  "v_mfma_f32_16x16x32_bf16 v[156:159], v[192:195], v[108:111], v[156:159]\n\t" \
  "ds_read_b64_tr_b16 v[220:221], v243 offset:20480\n\t" \
  "v_rcp_f32 v225, v225\n\t" \
  "v_mfma_f32_16x16x32_bf16 v[156:159], v[196:199], v[116:119], v[156:159]\n\t" \
  "ds_read_b64_tr_b16 v[222:223], v243 offset:22528\n\t" \
  "v_mul_f32 v224, v224, v176\n\t" \
  "v_mul_f32 v225, v225, v177\n\t" \
  "s_waitcnt lgkmcnt(6)\n\t" \
  "v_mfma_f32_16x16x32_bf16 v[144:147], v[200:203], v[120:123], v[144:147]\n\t" \
  "ds_read_b64_tr_b16 v[192:193], v240 offset:24576\n\t" \
  "v_cvt_pk_bf16_f32 v176, v224, v225\n\t" \
  "v_mul_f32 v224, v245, v178\n\t" \
  "v_mfma_f32_16x16x32_bf16 v[144:147], v[204:207], v[128:131], v[144:147]\n\t" \
  "ds_read_b64_tr_b16 v[194:195], v240 offset:26624\n\t" \
  "v_mul_f32 v225, v245, v179\n\t" \
  "v_fmaak_f32 v226, v224, v224, 0x409d30fc\n\t" \
  "v_mfma_f32_16x16x32_bf16 v[144:147], v[208:211], v[136:139], v[144:147]\n\t" \
  "ds_read_b64_tr_b16 v[196:197], v240 offset:28672\n\t" \
  "v_fmaak_f32 v227, v225, v225, 0x409d30fc\n\t" \
  "v_mul_f32 v224, v224, v226\n\t" \
  "v_mfma_f32_16x16x32_bf16 v[148:151], v[200:203], v[124:127], v[148:151]\n\t" \
  "ds_read_b64_tr_b16 v[198:199], v240 offset:30720\n\t" \
  "v_mul_f32 v225, v225, v227\n\t" \
  "v_exp_f32 v224, v224\n\t" \
  "v_mfma_f32_16x16x32_bf16 v[148:151], v[204:207], v[132:135], v[148:151]\n\t" \
  "ds_read_b64_tr_b16 v[200:201], v240 offset:32768\n\t" \
  "v_exp_f32 v225, v225\n\t" \
  "v_mfma_f32_16x16x32_bf16 v[148:151], v[208:211], v[140:143], v[148:151]\n\t" \
  "ds_read_b64_tr_b16 v[202:203], v240 offset:34816\n\t" \
  "v_add_f32 v224, 1.0, v224\n\t" \
  "s_waitcnt lgkmcnt(6)\n\t" \
  "v_mfma_f32_16x16x32_bf16 v[152:155], v[212:215], v[120:123], v[152:155]\n\t" \
  "ds_read_b64_tr_b16 v[204:205], v241 offset:24576\n\t" \
  "v_add_f32 v225, 1.0, v225\n\t" \
  "v_rcp_f32 v224, v224\n\t" \
  "v_mfma_f32_16x16x32_bf16 v[152:155], v[216:219], v[128:131], v[152:155]\n\t" \
  "ds_read_b64_tr_b16 v[206:207], v241 offset:26624\n\t" \
  "v_rcp_f32 v225, v225\n\t" \
  "v_mfma_f32_16x16x32_bf16 v[152:155], v[220:223], v[136:139], v[152:155]\n\t" \
  "ds_read_b64_tr_b16 v[208:209], v241 offset:28672\n\t" \
  "v_mul_f32 v224, v224, v178\n\t" \
  "v_mul_f32 v225, v225, v179\n\t" \
  "v_mfma_f32_16x16x32_bf16 v[156:159], v[212:215], v[124:127], v[156:159]\n\t" \
  "ds_read_b64_tr_b16 v[210:211], v241 offset:30720\n\t" \
  "v_cvt_pk_bf16_f32 v177, v224, v225\n\t" \
  "v_mul_f32 v224, v245, v184\n\t" \
  "v_mfma_f32_16x16x32_bf16 v[156:159], v[216:219], v[132:135], v[156:159]\n\t" \
  "ds_read_b64_tr_b16 v[212:213], v241 offset:32768\n\t" \
  "v_mul_f32 v225, v245, v185\n\t" \
  "v_fmaak_f32 v226, v224, v224, 0x409d30fc\n\t" \
  "v_mfma_f32_16x16x32_bf16 v[156:159], v[220:223], v[140:143], v[156:159]\n\t" \
  "ds_read_b64_tr_b16 v[214:215], v241 offset:34816\n\t" \
  "v_fmaak_f32 v227, v225, v225, 0x409d30fc\n\t" \
  "v_mul_f32 v224, v224, v226\n\t" \
  "s_waitcnt lgkmcnt(6)\n\t" \
  "v_mfma_f32_16x16x32_bf16 v[160:163], v[192:195], v[0:3], 0\n\t" \
  "ds_read_b64_tr_b16 v[216:217], v232 offset:0\n\t" \
  "v_mul_f32 v225, v225, v227\n\t" \
  "v_exp_f32 v224, v224\n\t" \
  "v_mfma_f32_16x16x32_bf16 v[160:163], v[196:199], v[8:11], v[160:163]\n\t" \
  "ds_read_b64_tr_b16 v[218:219], v232 offset:2048\n\t" \
  "v_exp_f32 v225, v225\n\t" \
  "v_mfma_f32_16x16x32_bf16 v[160:163], v[200:203], v[16:19], v[160:163]\n\t" \
  "ds_read_b64_tr_b16 v[220:221], v232 offset:4096\n\t" \
  "v_add_f32 v224, 1.0, v224\n\t" \
  "v_add_f32 v225, 1.0, v225\n\t" \
  "v_mfma_f32_16x16x32_bf16 v[164:167], v[192:195], v[4:7], 0\n\t" \
  "ds_read_b64_tr_b16 v[222:223], v232 offset:6144\n\t" \
  "v_rcp_f32 v224, v224\n\t" \
  "v_mfma_f32_16x16x32_bf16 v[164:167], v[196:199], v[12:15], v[164:167]\n\t" \
  "ds_read_b64_tr_b16 v[192:193], v232 offset:8192\n\t" \
  "v_rcp_f32 v225, v225\n\t" \
  "v_mfma_f32_16x16x32_bf16 v[164:167], v[200:203], v[20:23], v[164:167]\n\t" \
  "ds_read_b64_tr_b16 v[194:195], v232 offset:10240\n\t" \
  "v_mul_f32 v224, v224, v184\n\t" \
  "v_mul_f32 v225, v225, v185\n\t" \
  "s_waitcnt lgkmcnt(6)\n\t" \
  "v_mfma_f32_16x16x32_bf16 v[168:171], v[204:207], v[0:3], 0\n\t" \
  "ds_read_b64_tr_b16 v[196:197], v233 offset:0\n\t" \
  "v_cvt_pk_bf16_f32 v178, v224, v225\n\t" \
  "v_mul_f32 v224, v245, v186\n\t" \
  "v_mfma_f32_16x16x32_bf16 v[168:171], v[208:211], v[8:11], v[168:171]\n\t" \
  "ds_read_b64_tr_b16 v[198:199], v233 offset:2048\n\t" \
  "v_mul_f32 v225, v245, v187\n\t" \
  "v_fmaak_f32 v226, v224, v224, 0x409d30fc\n\t" \
  "v_mfma_f32_16x16x32_bf16 v[168:171], v[212:215], v[16:19], v[168:171]\n\t" \
  "ds_read_b64_tr_b16 v[200:201], v233 offset:4096\n\t" \
  "v_fmaak_f32 v227, v225, v225, 0x409d30fc\n\t" \
  "v_mul_f32 v224, v224, v226\n\t" \
  "v_mul_f32 v225, v225, v227\n\t" \
  "v_mfma_f32_16x16x32_bf16 v[172:175], v[204:207], v[4:7], 0\n\t" \
  "ds_read_b64_tr_b16 v[202:203], v233 offset:6144\n\t" \
  "v_exp_f32 v224, v224\n\t" \
  "v_mfma_f32_16x16x32_bf16 v[172:175], v[208:211], v[12:15], v[172:175]\n\t" \
  "ds_read_b64_tr_b16 v[204:205], v233 offset:8192\n\t" \
  "v_exp_f32 v225, v225\n\t" \
  "v_mfma_f32_16x16x32_bf16 v[172:175], v[212:215], v[20:23], v[172:175]\n\t" \
  "ds_read_b64_tr_b16 v[206:207], v233 offset:10240\n\t" \
  "v_add_f32 v224, 1.0, v224\n\t" \
  "v_add_f32 v225, 1.0, v225\n\t" \
  "s_waitcnt lgkmcnt(6)\n\t" \
  "v_mfma_f32_16x16x32_bf16 v[160:163], v[216:219], v[24:27], v[160:163]\n\t" \
  "ds_read_b64_tr_b16 v[208:209], v232 offset:12288\n\t" \
  "v_rcp_f32 v224, v224\n\t" \
  "v_mfma_f32_16x16x32_bf16 v[160:163], v[220:223], v[32:35], v[160:163]\n\t" \
  "ds_read_b64_tr_b16 v[210:211], v232 offset:14336\n\t" \
  "v_rcp_f32 v225, v225\n\t" \
  "v_mfma_f32_16x16x32_bf16 v[160:163], v[192:195], v[40:43], v[160:163]\n\t" \
  "ds_read_b64_tr_b16 v[212:213], v232 offset:16384\n\t" \
  "v_mul_f32 v224, v224, v186\n\t" \
  "v_mul_f32 v225, v225, v187\n\t" \
  "v_mfma_f32_16x16x32_bf16 v[164:167], v[216:219], v[28:31], v[164:167]\n\t" \
  "ds_read_b64_tr_b16 v[214:215], v232 offset:18432\n\t" \
  "v_cvt_pk_bf16_f32 v179, v224, v225\n\t" \
  "s_add_u32 s75, s71, 4096\n\t" \
  "buffer_store_dwordx4 v[176:179], v244, s[48:51], s75 offen offset:0 sc1\n\t" \
  "v_mfma_f32_16x16x32_bf16 v[164:167], v[220:223], v[36:39], v[164:167]\n\t" \
  "ds_read_b64_tr_b16 v[216:217], v232 offset:20480\n\t" \
  "v_mul_f32 v224, v246, v180\n\t" \
  "v_mul_f32 v225, v246, v181\n\t" \
  "v_fmaak_f32 v226, v224, v224, 0x409d30fc\n\t" \
  "v_mfma_f32_16x16x32_bf16 v[164:167], v[192:195], v[44:47], v[164:167]\n\t" \
  "ds_read_b64_tr_b16 v[218:219], v232 offset:22528\n\t" \
  "v_fmaak_f32 v227, v225, v225, 0x409d30fc\n\t" \
  "v_mul_f32 v224, v224, v226\n\t" \
  "s_waitcnt lgkmcnt(6)\n\t" \
  "v_mfma_f32_16x16x32_bf16 v[168:171], v[196:199], v[24:27], v[168:171]\n\t" \
  "ds_read_b64_tr_b16 v[220:221], v233 offset:12288\n\t" \
  "v_mul_f32 v225, v225, v227\n\t" \
  "v_exp_f32 v224, v224\n\t" \
  "v_mfma_f32_16x16x32_bf16 v[168:171], v[200:203], v[32:35], v[168:171]\n\t" \
  "ds_read_b64_tr_b16 v[222:223], v233 offset:14336\n\t" \
  "v_exp_f32 v225, v225\n\t" \
  "v_mfma_f32_16x16x32_bf16 v[168:171], v[204:207], v[40:43], v[168:171]\n\t" \
  "ds_read_b64_tr_b16 v[192:193], v233 offset:16384\n\t" \
  "v_add_f32 v224, 1.0, v224\n\t" \
  "v_mfma_f32_16x16x32_bf16 v[172:175], v[196:199], v[28:31], v[172:175]\n\t" \
  "ds_read_b64_tr_b16 v[194:195], v233 offset:18432\n\t" \
  "v_add_f32 v225, 1.0, v225\n\t" \
  "v_rcp_f32 v224, v224\n\t" \
  "v_mfma_f32_16x16x32_bf16 v[172:175], v[200:203], v[36:39], v[172:175]\n\t" \
  "ds_read_b64_tr_b16 v[196:197], v233 offset:20480\n\t" \
  "v_rcp_f32 v225, v225\n\t" \
  "v_mfma_f32_16x16x32_bf16 v[172:175], v[204:207], v[44:47], v[172:175]\n\t" \
  "ds_read_b64_tr_b16 v[198:199], v233 offset:22528\n\t" \
  "v_mul_f32 v224, v224, v180\n\t" \
  "v_mul_f32 v225, v225, v181\n\t" \
  "s_waitcnt lgkmcnt(6)\n\t" \
  "v_mfma_f32_16x16x32_bf16 v[160:163], v[208:211], v[48:51], v[160:163]\n\t" \
  "ds_read_b64_tr_b16 v[200:201], v232 offset:24576\n\t" \
  "v_cvt_pk_bf16_f32 v180, v224, v225\n\t" \
  "v_mul_f32 v224, v246, v182\n\t" \
  "v_mfma_f32_16x16x32_bf16 v[160:163], v[212:215], v[56:59], v[160:163]\n\t" \
  "ds_read_b64_tr_b16 v[202:203], v232 offset:26624\n\t" \
  "v_mul_f32 v225, v246, v183\n\t" \
  "v_fmaak_f32 v226, v224, v224, 0x409d30fc\n\t" \
  "v_mfma_f32_16x16x32_bf16 v[160:163], v[216:219], v[64:67], v[160:163]\n\t" \
  "ds_read_b64_tr_b16 v[204:205], v232 offset:28672\n\t" \
  "v_fmaak_f32 v227, v225, v225, 0x409d30fc\n\t" \
  "v_mul_f32 v224, v224, v226\n\t" \
  "v_mfma_f32_16x16x32_bf16 v[164:167], v[208:211], v[52:55], v[164:167]\n\t" \
  "ds_read_b64_tr_b16 v[206:207], v232 offset:30720\n\t" \
  "v_mul_f32 v225, v225, v227\n\t" \
  "v_exp_f32 v224, v224\n\t" \
  "v_mfma_f32_16x16x32_bf16 v[164:167], v[212:215], v[60:63], v[164:167]\n\t" \
  "ds_read_b64_tr_b16 v[208:209], v232 offset:32768\n\t" \
  "v_exp_f32 v225, v225\n\t" \
  "v_mfma_f32_16x16x32_bf16 v[164:167], v[216:219], v[68:71], v[164:167]\n\t" \
  "ds_read_b64_tr_b16 v[210:211], v232 offset:34816\n\t" \
  "v_add_f32 v224, 1.0, v224\n\t" \
  "s_waitcnt lgkmcnt(6)\n\t" \
  "v_mfma_f32_16x16x32_bf16 v[168:171], v[220:223], v[48:51], v[168:171]\n\t" \
  "ds_read_b64_tr_b16 v[212:213], v233 offset:24576\n\t" \
  "v_add_f32 v225, 1.0, v225\n\t" \
  "v_rcp_f32 v224, v224\n\t" \
  "v_mfma_f32_16x16x32_bf16 v[168:171], v[192:195], v[56:59], v[168:171]\n\t" \
  "ds_read_b64_tr_b16 v[214:215], v233 offset:26624\n\t" \
  "v_rcp_f32 v225, v225\n\t" \
  "v_mfma_f32_16x16x32_bf16 v[168:171], v[196:199], v[64:67], v[168:171]\n\t" \
  "ds_read_b64_tr_b16 v[216:217], v233 offset:28672\n\t" \
  "v_mul_f32 v224, v224, v182\n\t" \
  "v_mul_f32 v225, v225, v183\n\t" \
  "v_mfma_f32_16x16x32_bf16 v[172:175], v[220:223], v[52:55], v[172:175]\n\t" \
  "ds_read_b64_tr_b16 v[218:219], v233 offset:30720\n\t" \
  "v_cvt_pk_bf16_f32 v181, v224, v225\n\t" \
  "v_mul_f32 v224, v246, v188\n\t" \
  "v_mfma_f32_16x16x32_bf16 v[172:175], v[192:195], v[60:63], v[172:175]\n\t" \
  "ds_read_b64_tr_b16 v[220:221], v233 offset:32768\n\t" \
  "v_mul_f32 v225, v246, v189\n\t" \
  "v_fmaak_f32 v226, v224, v224, 0x409d30fc\n\t" \
  "v_mfma_f32_16x16x32_bf16 v[172:175], v[196:199], v[68:71], v[172:175]\n\t" \
  "ds_read_b64_tr_b16 v[222:223], v233 offset:34816\n\t" \
  "v_fmaak_f32 v227, v225, v225, 0x409d30fc\n\t" \
  "v_mul_f32 v224, v224, v226\n\t" \
  "s_waitcnt lgkmcnt(6)\n\t" \
  "v_mfma_f32_16x16x32_bf16 v[160:163], v[200:203], v[72:75], v[160:163]\n\t" \
  "ds_read_b64_tr_b16 v[192:193], v232 offset:36864\n\t" \
  "v_mul_f32 v225, v225, v227\n\t" \
  "v_exp_f32 v224, v224\n\t" \
  "v_mfma_f32_16x16x32_bf16 v[160:163], v[204:207], v[80:83], v[160:163]\n\t" \
  "ds_read_b64_tr_b16 v[194:195], v232 offset:38912\n\t" \
  "v_exp_f32 v225, v225\n\t" \
  "v_mfma_f32_16x16x32_bf16 v[160:163], v[208:211], v[88:91], v[160:163]\n\t" \
  "ds_read_b64_tr_b16 v[196:197], v232 offset:40960\n\t" \
  "v_add_f32 v224, 1.0, v224\n\t" \
  "v_add_f32 v225, 1.0, v225\n\t" \
  "v_mfma_f32_16x16x32_bf16 v[164:167], v[200:203], v[76:79], v[164:167]\n\t" \
  "ds_read_b64_tr_b16 v[198:199], v232 offset:43008\n\t" \
  "v_rcp_f32 v224, v224\n\t" \
  "v_mfma_f32_16x16x32_bf16 v[164:167], v[204:207], v[84:87], v[164:167]\n\t" \
  "ds_read_b64_tr_b16 v[200:201], v232 offset:45056\n\t" \
  "v_rcp_f32 v225, v225\n\t" \
  "v_mfma_f32_16x16x32_bf16 v[164:167], v[208:211], v[92:95], v[164:167]\n\t" \
  "ds_read_b64_tr_b16 v[202:203], v232 offset:47104\n\t" \
  "v_mul_f32 v224, v224, v188\n\t" \
  "v_mul_f32 v225, v225, v189\n\t" \
  "s_waitcnt lgkmcnt(6)\n\t" \
  "v_mfma_f32_16x16x32_bf16 v[168:171], v[212:215], v[72:75], v[168:171]\n\t" \
  "ds_read_b64_tr_b16 v[204:205], v233 offset:36864\n\t" \
  "v_cvt_pk_bf16_f32 v182, v224, v225\n\t" \
  "v_mul_f32 v224, v246, v190\n\t" \
  "v_mfma_f32_16x16x32_bf16 v[168:171], v[216:219], v[80:83], v[168:171]\n\t" \
  "ds_read_b64_tr_b16 v[206:207], v233 offset:38912\n\t" \
  "v_mul_f32 v225, v246, v191\n\t" \
  "v_fmaak_f32 v226, v224, v224, 0x409d30fc\n\t" \
  "v_mfma_f32_16x16x32_bf16 v[168:171], v[220:223], v[88:91], v[168:171]\n\t" \
  "ds_read_b64_tr_b16 v[208:209], v233 offset:40960\n\t" \
  "v_fmaak_f32 v227, v225, v225, 0x409d30fc\n\t" \
  "v_mul_f32 v224, v224, v226\n\t" \
  "v_mul_f32 v225, v225, v227\n\t" \
  "v_mfma_f32_16x16x32_bf16 v[172:175], v[212:215], v[76:79], v[172:175]\n\t" \
  "ds_read_b64_tr_b16 v[210:211], v233 offset:43008\n\t" \
  "v_exp_f32 v224, v224\n\t" \
  "v_mfma_f32_16x16x32_bf16 v[172:175], v[216:219], v[84:87], v[172:175]\n\t" \
  "ds_read_b64_tr_b16 v[212:213], v233 offset:45056\n\t" \
  "v_exp_f32 v225, v225\n\t" \
  "v_mfma_f32_16x16x32_bf16 v[172:175], v[220:223], v[92:95], v[172:175]\n\t" \
  "ds_read_b64_tr_b16 v[214:215], v233 offset:47104\n\t" \
  "v_add_f32 v224, 1.0, v224\n\t" \
  "v_add_f32 v225, 1.0, v225\n\t" \
  "s_waitcnt lgkmcnt(6)\n\t" \
  "v_mfma_f32_16x16x32_bf16 v[160:163], v[192:195], v[96:99], v[160:163]\n\t" \
  "ds_read_b64_tr_b16 v[216:217], v232 offset:49152\n\t" \
  "v_rcp_f32 v224, v224\n\t" \
  "v_mfma_f32_16x16x32_bf16 v[160:163], v[196:199], v[104:107], v[160:163]\n\t" \
  "ds_read_b64_tr_b16 v[218:219], v232 offset:51200\n\t" \
  "v_rcp_f32 v225, v225\n\t" \
  "v_mfma_f32_16x16x32_bf16 v[160:163], v[200:203], v[112:115], v[160:163]\n\t" \
  "ds_read_b64_tr_b16 v[220:221], v232 offset:53248\n\t" \
  "v_mul_f32 v224, v224, v190\n\t" \
  "v_mul_f32 v225, v225, v191\n\t" \
  "v_mfma_f32_16x16x32_bf16 v[164:167], v[192:195], v[100:103], v[164:167]\n\t" \
  "ds_read_b64_tr_b16 v[222:223], v232 offset:55296\n\t" \
  "v_cvt_pk_bf16_f32 v183, v224, v225\n\t" \
  "s_add_u32 s75, s71, 4096\n\t" \
  "buffer_store_dwordx4 v[180:183], v244, s[48:51], s75 offen offset:1024 sc1\n\t" \
  "v_mfma_f32_16x16x32_bf16 v[164:167], v[196:199], v[108:111], v[164:167]\n\t" \
  "ds_read_b64_tr_b16 v[192:193], v232 offset:57344\n\t" \
  "v_mul_f32 v224, v245, v144\n\t" \
  "v_mul_f32 v225, v245, v145\n\t" \
  "v_fmaak_f32 v226, v224, v224, 0x409d30fc\n\t" \
  "v_mfma_f32_16x16x32_bf16 v[164:167], v[200:203], v[116:119], v[164:167]\n\t" \
  "ds_read_b64_tr_b16 v[194:195], v232 offset:59392\n\t" \
  "v_fmaak_f32 v227, v225, v225, 0x409d30fc\n\t" \
  "v_mul_f32 v224, v224, v226\n\t" \
  "s_waitcnt lgkmcnt(6)\n\t" \
  "v_mfma_f32_16x16x32_bf16 v[168:171], v[204:207], v[96:99], v[168:171]\n\t" \
  "ds_read_b64_tr_b16 v[196:197], v233 offset:49152\n\t" \
  "v_mul_f32 v225, v225, v227\n\t" \
  "v_exp_f32 v224, v224\n\t" \
  "v_mfma_f32_16x16x32_bf16 v[168:171], v[208:211], v[104:107], v[168:171]\n\t" \
  "ds_read_b64_tr_b16 v[198:199], v233 offset:51200\n\t" \
  "v_exp_f32 v225, v225\n\t" \
  "v_mfma_f32_16x16x32_bf16 v[168:171], v[212:215], v[112:115], v[168:171]\n\t" \
  "ds_read_b64_tr_b16 v[200:201], v233 offset:53248\n\t" \
  "v_add_f32 v224, 1.0, v224\n\t" \
  "v_mfma_f32_16x16x32_bf16 v[172:175], v[204:207], v[100:103], v[172:175]\n\t" \
  "ds_read_b64_tr_b16 v[202:203], v233 offset:55296\n\t" \
  "v_add_f32 v225, 1.0, v225\n\t" \
  "v_rcp_f32 v224, v224\n\t" \
  "v_mfma_f32_16x16x32_bf16 v[172:175], v[208:211], v[108:111], v[172:175]\n\t" \
  "ds_read_b64_tr_b16 v[204:205], v233 offset:57344\n\t" \
  "v_rcp_f32 v225, v225\n\t" \
  "v_mfma_f32_16x16x32_bf16 v[172:175], v[212:215], v[116:119], v[172:175]\n\t" \
  "ds_read_b64_tr_b16 v[206:207], v233 offset:59392\n\t" \
  "v_mul_f32 v224, v224, v144\n\t" \
  "v_mul_f32 v225, v225, v145\n\t" \
  "s_waitcnt lgkmcnt(6)\n\t" \
  "v_mfma_f32_16x16x32_bf16 v[160:163], v[216:219], v[120:123], v[160:163]\n\t" \
  "ds_read_b64_tr_b16 v[208:209], v242 offset:24576\n\t" \
  "v_cvt_pk_bf16_f32 v144, v224, v225\n\t" \
  "v_mul_f32 v224, v245, v146\n\t" \
  "v_mfma_f32_16x16x32_bf16 v[160:163], v[220:223], v[128:131], v[160:163]\n\t" \
  "ds_read_b64_tr_b16 v[210:211], v242 offset:26624\n\t" \
  "v_mul_f32 v225, v245, v147\n\t" \
  "v_fmaak_f32 v226, v224, v224, 0x409d30fc\n\t" \
  "v_mfma_f32_16x16x32_bf16 v[160:163], v[192:195], v[136:139], v[160:163]\n\t" \
  "ds_read_b64_tr_b16 v[212:213], v242 offset:28672\n\t" \
  "v_fmaak_f32 v227, v225, v225, 0x409d30fc\n\t" \
  "v_mul_f32 v224, v224, v226\n\t" \
  "v_mfma_f32_16x16x32_bf16 v[164:167], v[216:219], v[124:127], v[164:167]\n\t" \
  "ds_read_b64_tr_b16 v[214:215], v242 offset:30720\n\t" \
  "v_mul_f32 v225, v225, v227\n\t" \
  "v_exp_f32 v224, v224\n\t" \
  "v_mfma_f32_16x16x32_bf16 v[164:167], v[220:223], v[132:135], v[164:167]\n\t" \
  "ds_read_b64_tr_b16 v[216:217], v242 offset:32768\n\t" \
  "v_exp_f32 v225, v225\n\t" \
  "v_mfma_f32_16x16x32_bf16 v[164:167], v[192:195], v[140:143], v[164:167]\n\t" \
  "ds_read_b64_tr_b16 v[218:219], v242 offset:34816\n\t" \
  "v_add_f32 v224, 1.0, v224\n\t" \
  "s_waitcnt lgkmcnt(6)\n\t" \
  "v_mfma_f32_16x16x32_bf16 v[168:171], v[196:199], v[120:123], v[168:171]\n\t" \
  "ds_read_b64_tr_b16 v[220:221], v243 offset:24576\n\t" \
  "v_add_f32 v225, 1.0, v225\n\t" \
  "v_rcp_f32 v224, v224\n\t" \
  "v_mfma_f32_16x16x32_bf16 v[168:171], v[200:203], v[128:131], v[168:171]\n\t" \
  "ds_read_b64_tr_b16 v[222:223], v243 offset:26624\n\t" \
  "v_rcp_f32 v225, v225\n\t" \
  "v_mfma_f32_16x16x32_bf16 v[168:171], v[204:207], v[136:139], v[168:171]\n\t" \
  "ds_read_b64_tr_b16 v[192:193], v243 offset:28672\n\t" \
  "v_mul_f32 v224, v224, v146\n\t" \
  "v_mul_f32 v225, v225, v147\n\t" \
  "v_mfma_f32_16x16x32_bf16 v[172:175], v[196:199], v[124:127], v[172:175]\n\t" \
  "ds_read_b64_tr_b16 v[194:195], v243 offset:30720\n\t" \
  "v_cvt_pk_bf16_f32 v145, v224, v225\n\t" \
  "v_mul_f32 v224, v245, v152\n\t" \
  "v_mfma_f32_16x16x32_bf16 v[172:175], v[200:203], v[132:135], v[172:175]\n\t" \
  "ds_read_b64_tr_b16 v[196:197], v243 offset:32768\n\t" \
  "v_mul_f32 v225, v245, v153\n\t" \
  "v_fmaak_f32 v226, v224, v224, 0x409d30fc\n\t" \
  "v_mfma_f32_16x16x32_bf16 v[172:175], v[204:207], v[140:143], v[172:175]\n\t" \
  "ds_read_b64_tr_b16 v[198:199], v243 offset:34816\n\t" \
  "v_fmaak_f32 v227, v225, v225, 0x409d30fc\n\t" \
  "v_mul_f32 v224, v224, v226\n\t" \
  "s_waitcnt lgkmcnt(6)\n\t" \
  "v_mfma_f32_16x16x32_bf16 v[176:179], v[208:211], v[0:3], 0\n\t" \
  "ds_read_b64_tr_b16 v[200:201], v234 offset:0\n\t" \
  "v_mul_f32 v225, v225, v227\n\t" \
  "v_exp_f32 v224, v224\n\t" \
  "v_mfma_f32_16x16x32_bf16 v[176:179], v[212:215], v[8:11], v[176:179]\n\t" \
  "ds_read_b64_tr_b16 v[202:203], v234 offset:2048\n\t" \
  "v_exp_f32 v225, v225\n\t" \
  "v_mfma_f32_16x16x32_bf16 v[176:179], v[216:219], v[16:19], v[176:179]\n\t" \
  "ds_read_b64_tr_b16 v[204:205], v234 offset:4096\n\t" \
  "v_add_f32 v224, 1.0, v224\n\t" \
  "v_add_f32 v225, 1.0, v225\n\t" \
  "v_mfma_f32_16x16x32_bf16 v[180:183], v[208:211], v[4:7], 0\n\t" \
  "ds_read_b64_tr_b16 v[206:207], v234 offset:6144\n\t" \
  "v_rcp_f32 v224, v224\n\t" \
  "v_mfma_f32_16x16x32_bf16 v[180:183], v[212:215], v[12:15], v[180:183]\n\t" \
  "ds_read_b64_tr_b16 v[208:209], v234 offset:8192\n\t" \
  "v_rcp_f32 v225, v225\n\t" \
  "v_mfma_f32_16x16x32_bf16 v[180:183], v[216:219], v[20:23], v[180:183]\n\t" \
  "ds_read_b64_tr_b16 v[210:211], v234 offset:10240\n\t" \
  "v_mul_f32 v224, v224, v152\n\t" \
  "v_mul_f32 v225, v225, v153\n\t" \
  "s_waitcnt lgkmcnt(6)\n\t" \
  "v_mfma_f32_16x16x32_bf16 v[184:187], v[220:223], v[0:3], 0\n\t" \
  "ds_read_b64_tr_b16 v[212:213], v235 offset:0\n\t" \
  "v_cvt_pk_bf16_f32 v146, v224, v225\n\t" \
  "v_mul_f32 v224, v245, v154\n\t" \
  "v_mfma_f32_16x16x32_bf16 v[184:187], v[192:195], v[8:11], v[184:187]\n\t" \
  "ds_read_b64_tr_b16 v[214:215], v235 offset:2048\n\t" \
  "v_mul_f32 v225, v245, v155\n\t" \
  "v_fmaak_f32 v226, v224, v224, 0x409d30fc\n\t" \
  "v_mfma_f32_16x16x32_bf16 v[184:187], v[196:199], v[16:19], v[184:187]\n\t" \
  "ds_read_b64_tr_b16 v[216:217], v235 offset:4096\n\t" \
  "v_fmaak_f32 v227, v225, v225, 0x409d30fc\n\t" \
  "v_mul_f32 v224, v224, v226\n\t" \
  "v_mul_f32 v225, v225, v227\n\t" \
  "v_mfma_f32_16x16x32_bf16 v[188:191], v[220:223], v[4:7], 0\n\t" \
  "ds_read_b64_tr_b16 v[218:219], v235 offset:6144\n\t" \
  "v_exp_f32 v224, v224\n\t" \
  "v_mfma_f32_16x16x32_bf16 v[188:191], v[192:195], v[12:15], v[188:191]\n\t" \
  "ds_read_b64_tr_b16 v[220:221], v235 offset:8192\n\t" \
  "v_exp_f32 v225, v225\n\t" \
  "v_mfma_f32_16x16x32_bf16 v[188:191], v[196:199], v[20:23], v[188:191]\n\t" \
  "ds_read_b64_tr_b16 v[222:223], v235 offset:10240\n\t" \
  "v_add_f32 v224, 1.0, v224\n\t" \
  "v_add_f32 v225, 1.0, v225\n\t" \
  "s_waitcnt lgkmcnt(6)\n\t" \
  "v_mfma_f32_16x16x32_bf16 v[176:179], v[200:203], v[24:27], v[176:179]\n\t" \
  "ds_read_b64_tr_b16 v[192:193], v234 offset:12288\n\t" \
  "v_rcp_f32 v224, v224\n\t" \
  "v_mfma_f32_16x16x32_bf16 v[176:179], v[204:207], v[32:35], v[176:179]\n\t" \
  "ds_read_b64_tr_b16 v[194:195], v234 offset:14336\n\t" \
  "v_rcp_f32 v225, v225\n\t" \
  "v_mfma_f32_16x16x32_bf16 v[176:179], v[208:211], v[40:43], v[176:179]\n\t" \
  "ds_read_b64_tr_b16 v[196:197], v234 offset:16384\n\t" \
  "v_mul_f32 v224, v224, v154\n\t" \
  "v_mul_f32 v225, v225, v155\n\t" \
  "v_mfma_f32_16x16x32_bf16 v[180:183], v[200:203], v[28:31], v[180:183]\n\t" \
  "ds_read_b64_tr_b16 v[198:199], v234 offset:18432\n\t" \
  "v_cvt_pk_bf16_f32 v147, v224, v225\n\t" \
  "s_add_u32 s75, s71, 4096\n\t" \
  "buffer_store_dwordx4 v[144:147], v244, s[48:51], s75 offen offset:2048 sc1\n\t" \
  "v_mfma_f32_16x16x32_bf16 v[180:183], v[204:207], v[36:39], v[180:183]\n\t" \
  "ds_read_b64_tr_b16 v[200:201], v234 offset:20480\n\t" \
  "v_mul_f32 v224, v246, v148\n\t" \
  "v_mul_f32 v225, v246, v149\n\t" \
  "v_fmaak_f32 v226, v224, v224, 0x409d30fc\n\t" \
  "v_mfma_f32_16x16x32_bf16 v[180:183], v[208:211], v[44:47], v[180:183]\n\t" \
  "ds_read_b64_tr_b16 v[202:203], v234 offset:22528\n\t" \
  "v_fmaak_f32 v227, v225, v225, 0x409d30fc\n\t" \
  "v_mul_f32 v224, v224, v226\n\t" \
  "s_waitcnt lgkmcnt(6)\n\t" \
  "v_mfma_f32_16x16x32_bf16 v[184:187], v[212:215], v[24:27], v[184:187]\n\t" \
  "ds_read_b64_tr_b16 v[204:205], v235 offset:12288\n\t" \
  "v_mul_f32 v225, v225, v227\n\t" \
  "v_exp_f32 v224, v224\n\t" \
  "v_mfma_f32_16x16x32_bf16 v[184:187], v[216:219], v[32:35], v[184:187]\n\t" \
  "ds_read_b64_tr_b16 v[206:207], v235 offset:14336\n\t" \
  "v_exp_f32 v225, v225\n\t" \
  "v_mfma_f32_16x16x32_bf16 v[184:187], v[220:223], v[40:43], v[184:187]\n\t" \
  "ds_read_b64_tr_b16 v[208:209], v235 offset:16384\n\t" \
  "v_add_f32 v224, 1.0, v224\n\t" \
  "s_waitcnt vmcnt(3)\n\t" \
  "s_barrier\n\t" \
  "v_mfma_f32_16x16x32_bf16 v[188:191], v[212:215], v[28:31], v[188:191]\n\t" \
  "ds_read_b64_tr_b16 v[210:211], v235 offset:18432\n\t" \
  "v_add_f32 v225, 1.0, v225\n\t" \
  "v_rcp_f32 v224, v224\n\t" \
  "v_mfma_f32_16x16x32_bf16 v[188:191], v[216:219], v[36:39], v[188:191]\n\t" \
  "ds_read_b64_tr_b16 v[212:213], v235 offset:20480\n\t" \
  "v_rcp_f32 v225, v225\n\t" \
  "v_mfma_f32_16x16x32_bf16 v[188:191], v[220:223], v[44:47], v[188:191]\n\t" \
  "ds_read_b64_tr_b16 v[214:215], v235 offset:22528\n\t" \
  "v_mul_f32 v224, v224, v148\n\t" \
  "v_mul_f32 v225, v225, v149\n\t" \
  "s_waitcnt lgkmcnt(6)\n\t" \
  "v_mfma_f32_16x16x32_bf16 v[176:179], v[192:195], v[48:51], v[176:179]\n\t" \
  "ds_read_b64_tr_b16 v[216:217], v234 offset:24576\n\t" \
  "v_cvt_pk_bf16_f32 v148, v224, v225\n\t" \
  "v_mul_f32 v224, v246, v150\n\t" \
  "v_mfma_f32_16x16x32_bf16 v[176:179], v[196:199], v[56:59], v[176:179]\n\t" \
  "ds_read_b64_tr_b16 v[218:219], v234 offset:26624\n\t" \
  "v_mul_f32 v225, v246, v151\n\t" \
  "v_fmaak_f32 v226, v224, v224, 0x409d30fc\n\t" \
  "v_mfma_f32_16x16x32_bf16 v[176:179], v[200:203], v[64:67], v[176:179]\n\t" \
  "ds_read_b64_tr_b16 v[220:221], v234 offset:28672\n\t" \
  "v_fmaak_f32 v227, v225, v225, 0x409d30fc\n\t" \
  "v_mul_f32 v224, v224, v226\n\t" \
  "v_mfma_f32_16x16x32_bf16 v[180:183], v[192:195], v[52:55], v[180:183]\n\t" \
  "ds_read_b64_tr_b16 v[222:223], v234 offset:30720\n\t" \
  "v_mul_f32 v225, v225, v227\n\t" \
  "v_exp_f32 v224, v224\n\t" \
  "v_mfma_f32_16x16x32_bf16 v[180:183], v[196:199], v[60:63], v[180:183]\n\t" \
  "ds_read_b64_tr_b16 v[192:193], v234 offset:32768\n\t" \
  "v_exp_f32 v225, v225\n\t" \
  "v_mfma_f32_16x16x32_bf16 v[180:183], v[200:203], v[68:71], v[180:183]\n\t" \
  "ds_read_b64_tr_b16 v[194:195], v234 offset:34816\n\t" \
  "v_add_f32 v224, 1.0, v224\n\t" \
  "s_waitcnt lgkmcnt(6)\n\t" \
  "v_mfma_f32_16x16x32_bf16 v[184:187], v[204:207], v[48:51], v[184:187]\n\t" \
  "ds_read_b64_tr_b16 v[196:197], v235 offset:24576\n\t" \
  "v_add_f32 v225, 1.0, v225\n\t" \
  "v_rcp_f32 v224, v224\n\t" \
  "v_mfma_f32_16x16x32_bf16 v[184:187], v[208:211], v[56:59], v[184:187]\n\t" \
  "ds_read_b64_tr_b16 v[198:199], v235 offset:26624\n\t" \
  "v_rcp_f32 v225, v225\n\t" \
  "v_mfma_f32_16x16x32_bf16 v[184:187], v[212:215], v[64:67], v[184:187]\n\t" \
  "ds_read_b64_tr_b16 v[200:201], v235 offset:28672\n\t" \
  "v_mul_f32 v224, v224, v150\n\t" \
  "v_mul_f32 v225, v225, v151\n\t" \
  "v_mfma_f32_16x16x32_bf16 v[188:191], v[204:207], v[52:55], v[188:191]\n\t" \
  "ds_read_b64_tr_b16 v[202:203], v235 offset:30720\n\t" \
  "v_cvt_pk_bf16_f32 v149, v224, v225\n\t" \
  "v_mul_f32 v224, v246, v156\n\t" \
  "v_mfma_f32_16x16x32_bf16 v[188:191], v[208:211], v[60:63], v[188:191]\n\t" \
  "ds_read_b64_tr_b16 v[204:205], v235 offset:32768\n\t" \
  "v_mul_f32 v225, v246, v157\n\t" \
  "v_fmaak_f32 v226, v224, v224, 0x409d30fc\n\t" \
  "v_mfma_f32_16x16x32_bf16 v[188:191], v[212:215], v[68:71], v[188:191]\n\t" \
  "ds_read_b64_tr_b16 v[206:207], v235 offset:34816\n\t" \
  "v_fmaak_f32 v227, v225, v225, 0x409d30fc\n\t" \
  "v_mul_f32 v224, v224, v226\n\t" \
  "s_waitcnt lgkmcnt(6)\n\t" \
  "v_mfma_f32_16x16x32_bf16 v[176:179], v[216:219], v[72:75], v[176:179]\n\t" \
  "ds_read_b64_tr_b16 v[208:209], v234 offset:36864\n\t" \
  "v_mul_f32 v225, v225, v227\n\t" \
  "v_exp_f32 v224, v224\n\t" \
  "v_mfma_f32_16x16x32_bf16 v[176:179], v[220:223], v[80:83], v[176:179]\n\t" \
  "ds_read_b64_tr_b16 v[210:211], v234 offset:38912\n\t" \
  "v_exp_f32 v225, v225\n\t" \
  "v_mfma_f32_16x16x32_bf16 v[176:179], v[192:195], v[88:91], v[176:179]\n\t" \
  "ds_read_b64_tr_b16 v[212:213], v234 offset:40960\n\t" \
  "v_add_f32 v224, 1.0, v224\n\t" \
  "v_add_f32 v225, 1.0, v225\n\t" \
  "v_mfma_f32_16x16x32_bf16 v[180:183], v[216:219], v[76:79], v[180:183]\n\t" \
  "ds_read_b64_tr_b16 v[214:215], v234 offset:43008\n\t" \
  "v_rcp_f32 v224, v224\n\t" \
  "v_mfma_f32_16x16x32_bf16 v[180:183], v[220:223], v[84:87], v[180:183]\n\t" \
  "ds_read_b64_tr_b16 v[216:217], v234 offset:45056\n\t" \
  "v_rcp_f32 v225, v225\n\t" \
  "v_mfma_f32_16x16x32_bf16 v[180:183], v[192:195], v[92:95], v[180:183]\n\t" \
  "ds_read_b64_tr_b16 v[218:219], v234 offset:47104\n\t" \
  "v_mul_f32 v224, v224, v156\n\t" \
  "v_mul_f32 v225, v225, v157\n\t" \
  "s_waitcnt lgkmcnt(6)\n\t" \
  "v_mfma_f32_16x16x32_bf16 v[184:187], v[196:199], v[72:75], v[184:187]\n\t" \
  "ds_read_b64_tr_b16 v[220:221], v235 offset:36864\n\t" \
  "v_cvt_pk_bf16_f32 v150, v224, v225\n\t" \
  "v_mul_f32 v224, v246, v158\n\t" \
  "v_mfma_f32_16x16x32_bf16 v[184:187], v[200:203], v[80:83], v[184:187]\n\t" \
  "ds_read_b64_tr_b16 v[222:223], v235 offset:38912\n\t" \
  "v_mul_f32 v225, v246, v159\n\t" \
  "v_fmaak_f32 v226, v224, v224, 0x409d30fc\n\t" \
  "v_mfma_f32_16x16x32_bf16 v[184:187], v[204:207], v[88:91], v[184:187]\n\t" \
  "ds_read_b64_tr_b16 v[192:193], v235 offset:40960\n\t" \
  "v_fmaak_f32 v227, v225, v225, 0x409d30fc\n\t" \
  "v_mul_f32 v224, v224, v226\n\t" \
  "v_mul_f32 v225, v225, v227\n\t" \
  "v_mfma_f32_16x16x32_bf16 v[188:191], v[196:199], v[76:79], v[188:191]\n\t" \
  "ds_read_b64_tr_b16 v[194:195], v235 offset:43008\n\t" \
  "v_exp_f32 v224, v224\n\t" \
  "v_mfma_f32_16x16x32_bf16 v[188:191], v[200:203], v[84:87], v[188:191]\n\t" \
  "ds_read_b64_tr_b16 v[196:197], v235 offset:45056\n\t" \
  "v_exp_f32 v225, v225\n\t" \
  "v_mfma_f32_16x16x32_bf16 v[188:191], v[204:207], v[92:95], v[188:191]\n\t" \
  "ds_read_b64_tr_b16 v[198:199], v235 offset:47104\n\t" \
  "v_add_f32 v224, 1.0, v224\n\t" \
  "v_add_f32 v225, 1.0, v225\n\t" \
  "s_waitcnt lgkmcnt(6)\n\t" \
  "v_mfma_f32_16x16x32_bf16 v[176:179], v[208:211], v[96:99], v[176:179]\n\t" \
  "ds_read_b64_tr_b16 v[200:201], v234 offset:49152\n\t" \
  "v_rcp_f32 v224, v224\n\t" \
  "v_mfma_f32_16x16x32_bf16 v[176:179], v[212:215], v[104:107], v[176:179]\n\t" \
  "ds_read_b64_tr_b16 v[202:203], v234 offset:51200\n\t" \
  "v_rcp_f32 v225, v225\n\t" \
  "v_mfma_f32_16x16x32_bf16 v[176:179], v[216:219], v[112:115], v[176:179]\n\t" \
  "ds_read_b64_tr_b16 v[204:205], v234 offset:53248\n\t" \
  "v_mul_f32 v224, v224, v158\n\t" \
  "v_mul_f32 v225, v225, v159\n\t" \
  "v_mfma_f32_16x16x32_bf16 v[180:183], v[208:211], v[100:103], v[180:183]\n\t" \
  "ds_read_b64_tr_b16 v[206:207], v234 offset:55296\n\t" \
  "v_cvt_pk_bf16_f32 v151, v224, v225\n\t" \
  "s_add_u32 s75, s71, 4096\n\t" \
  "buffer_store_dwordx4 v[148:151], v244, s[48:51], s75 offen offset:3072 sc1\n\t" \
  "v_mfma_f32_16x16x32_bf16 v[180:183], v[212:215], v[108:111], v[180:183]\n\t" \
  "ds_read_b64_tr_b16 v[208:209], v234 offset:57344\n\t" \
  "v_mul_f32 v224, v245, v160\n\t" \
  "v_mul_f32 v225, v245, v161\n\t" \
  "v_fmaak_f32 v226, v224, v224, 0x409d30fc\n\t" \
  "v_mfma_f32_16x16x32_bf16 v[180:183], v[216:219], v[116:119], v[180:183]\n\t" \
  "ds_read_b64_tr_b16 v[210:211], v234 offset:59392\n\t" \
  "v_fmaak_f32 v227, v225, v225, 0x409d30fc\n\t" \
  "v_mul_f32 v224, v224, v226\n\t" \
  "s_waitcnt lgkmcnt(6)\n\t" \
  "v_mfma_f32_16x16x32_bf16 v[184:187], v[220:223], v[96:99], v[184:187]\n\t" \
  "ds_read_b64_tr_b16 v[212:213], v235 offset:49152\n\t" \
  "v_mul_f32 v225, v225, v227\n\t" \
  "v_exp_f32 v224, v224\n\t" \
  "v_mfma_f32_16x16x32_bf16 v[184:187], v[192:195], v[104:107], v[184:187]\n\t" \
  "ds_read_b64_tr_b16 v[214:215], v235 offset:51200\n\t" \
  "v_exp_f32 v225, v225\n\t" \
  "v_mfma_f32_16x16x32_bf16 v[184:187], v[196:199], v[112:115], v[184:187]\n\t" \
  "ds_read_b64_tr_b16 v[216:217], v235 offset:53248\n\t" \
  "v_add_f32 v224, 1.0, v224\n\t" \
  "s_barrier\n\t" \
  "v_mfma_f32_16x16x32_bf16 v[188:191], v[220:223], v[100:103], v[188:191]\n\t" \
  "ds_read_b64_tr_b16 v[218:219], v235 offset:55296\n\t" \
  "v_add_f32 v225, 1.0, v225\n\t" \
  "v_rcp_f32 v224, v224\n\t" \
  "v_mfma_f32_16x16x32_bf16 v[188:191], v[192:195], v[108:111], v[188:191]\n\t" \
  "ds_read_b64_tr_b16 v[220:221], v235 offset:57344\n\t" \
  "v_rcp_f32 v225, v225\n\t" \
  "v_mfma_f32_16x16x32_bf16 v[188:191], v[196:199], v[116:119], v[188:191]\n\t" \
  "ds_read_b64_tr_b16 v[222:223], v235 offset:59392\n\t" \
  "v_mul_f32 v224, v224, v160\n\t" \
  "v_mul_f32 v225, v225, v161\n\t" \
  "s_waitcnt lgkmcnt(6)\n\t" \
  "v_mfma_f32_16x16x32_bf16 v[176:179], v[200:203], v[120:123], v[176:179]\n\t" \
  "ds_read_b64_tr_b16 v[192:193], v236 offset:0\n\t" \
  "v_cvt_pk_bf16_f32 v160, v224, v225\n\t" \
  "v_mul_f32 v224, v245, v162\n\t" \
  "v_mfma_f32_16x16x32_bf16 v[176:179], v[204:207], v[128:131], v[176:179]\n\t" \
  "ds_read_b64_tr_b16 v[194:195], v236 offset:2048\n\t" \
  "v_mul_f32 v225, v245, v163\n\t" \
  "v_fmaak_f32 v226, v224, v224, 0x409d30fc\n\t" \
  "v_mfma_f32_16x16x32_bf16 v[176:179], v[208:211], v[136:139], v[176:179]\n\t" \
  "ds_read_b64_tr_b16 v[196:197], v236 offset:4096\n\t" \
  "v_fmaak_f32 v227, v225, v225, 0x409d30fc\n\t" \
  "v_mul_f32 v224, v224, v226\n\t" \
  "v_mfma_f32_16x16x32_bf16 v[180:183], v[200:203], v[124:127], v[180:183]\n\t" \
  "ds_read_b64_tr_b16 v[198:199], v236 offset:6144\n\t" \
  "v_mul_f32 v225, v225, v227\n\t" \
  "v_exp_f32 v224, v224\n\t" \
  "v_mfma_f32_16x16x32_bf16 v[180:183], v[204:207], v[132:135], v[180:183]\n\t" \
  "ds_read_b64_tr_b16 v[200:201], v236 offset:8192\n\t" \
  "v_exp_f32 v225, v225\n\t" \
  "v_mfma_f32_16x16x32_bf16 v[180:183], v[208:211], v[140:143], v[180:183]\n\t" \
  "ds_read_b64_tr_b16 v[202:203], v236 offset:10240\n\t" \
  "v_add_f32 v224, 1.0, v224\n\t" \
  "s_waitcnt lgkmcnt(6)\n\t" \
  "v_mfma_f32_16x16x32_bf16 v[184:187], v[212:215], v[120:123], v[184:187]\n\t" \
  "ds_read_b64_tr_b16 v[204:205], v237 offset:0\n\t" \
  "v_add_f32 v225, 1.0, v225\n\t" \
  "v_rcp_f32 v224, v224\n\t" \
  "v_mfma_f32_16x16x32_bf16 v[184:187], v[216:219], v[128:131], v[184:187]\n\t" \
  "ds_read_b64_tr_b16 v[206:207], v237 offset:2048\n\t" \
  "v_rcp_f32 v225, v225\n\t" \
  "v_mfma_f32_16x16x32_bf16 v[184:187], v[220:223], v[136:139], v[184:187]\n\t" \
  "ds_read_b64_tr_b16 v[208:209], v237 offset:4096\n\t" \
  "v_mul_f32 v224, v224, v162\n\t" \
  "v_mul_f32 v225, v225, v163\n\t" \
  "v_mfma_f32_16x16x32_bf16 v[188:191], v[212:215], v[124:127], v[188:191]\n\t" \
  "ds_read_b64_tr_b16 v[210:211], v237 offset:6144\n\t" \
  "v_cvt_pk_bf16_f32 v161, v224, v225\n\t" \
  "v_mul_f32 v224, v245, v168\n\t" \
  "v_mfma_f32_16x16x32_bf16 v[188:191], v[216:219], v[132:135], v[188:191]\n\t" \
  "ds_read_b64_tr_b16 v[212:213], v237 offset:8192\n\t" \
  "v_mul_f32 v225, v245, v169\n\t" \
  "v_fmaak_f32 v226, v224, v224, 0x409d30fc\n\t" \
  "v_mfma_f32_16x16x32_bf16 v[188:191], v[220:223], v[140:143], v[188:191]\n\t" \
  "ds_read_b64_tr_b16 v[214:215], v237 offset:10240\n\t" \
  "v_fmaak_f32 v227, v225, v225, 0x409d30fc\n\t" \
  "v_mul_f32 v224, v224, v226\n\t" \
  "s_waitcnt lgkmcnt(6)\n\t" \
  "v_mfma_f32_16x16x32_bf16 v[144:147], v[192:195], v[0:3], 0\n\t" \
  "ds_read_b64_tr_b16 v[216:217], v236 offset:12288\n\t" \
  "v_mul_f32 v225, v225, v227\n\t" \
  "v_exp_f32 v224, v224\n\t" \
  "v_mfma_f32_16x16x32_bf16 v[144:147], v[196:199], v[8:11], v[144:147]\n\t" \
  "ds_read_b64_tr_b16 v[218:219], v236 offset:14336\n\t" \
  "v_exp_f32 v225, v225\n\t" \
  "v_mfma_f32_16x16x32_bf16 v[144:147], v[200:203], v[16:19], v[144:147]\n\t" \
  "ds_read_b64_tr_b16 v[220:221], v236 offset:16384\n\t" \
  "v_add_f32 v224, 1.0, v224\n\t" \
  "v_add_f32 v225, 1.0, v225\n\t" \
  "v_mfma_f32_16x16x32_bf16 v[148:151], v[192:195], v[4:7], 0\n\t" \
  "ds_read_b64_tr_b16 v[222:223], v236 offset:18432\n\t" \
  "v_rcp_f32 v224, v224\n\t" \
  "v_mfma_f32_16x16x32_bf16 v[148:151], v[196:199], v[12:15], v[148:151]\n\t" \
  "ds_read_b64_tr_b16 v[192:193], v236 offset:20480\n\t" \
  "v_rcp_f32 v225, v225\n\t" \
  "v_mfma_f32_16x16x32_bf16 v[148:151], v[200:203], v[20:23], v[148:151]\n\t" \
  "ds_read_b64_tr_b16 v[194:195], v236 offset:22528\n\t" \
  "v_mul_f32 v224, v224, v168\n\t" \
  "v_mul_f32 v225, v225, v169\n\t" \
  "s_waitcnt lgkmcnt(6)\n\t" \
  "v_mfma_f32_16x16x32_bf16 v[152:155], v[204:207], v[0:3], 0\n\t" \
  "ds_read_b64_tr_b16 v[196:197], v237 offset:12288\n\t" \
  "v_cvt_pk_bf16_f32 v162, v224, v225\n\t" \
  "v_mul_f32 v224, v245, v170\n\t" \
  "v_mfma_f32_16x16x32_bf16 v[152:155], v[208:211], v[8:11], v[152:155]\n\t" \
  "ds_read_b64_tr_b16 v[198:199], v237 offset:14336\n\t" \
  "v_mul_f32 v225, v245, v171\n\t" \
  "v_fmaak_f32 v226, v224, v224, 0x409d30fc\n\t" \
  "v_mfma_f32_16x16x32_bf16 v[152:155], v[212:215], v[16:19], v[152:155]\n\t" \
  "ds_read_b64_tr_b16 v[200:201], v237 offset:16384\n\t" \
  "v_fmaak_f32 v227, v225, v225, 0x409d30fc\n\t" \
  "v_mul_f32 v224, v224, v226\n\t" \
  "v_mul_f32 v225, v225, v227\n\t" \
  "v_mfma_f32_16x16x32_bf16 v[156:159], v[204:207], v[4:7], 0\n\t" \
  "ds_read_b64_tr_b16 v[202:203], v237 offset:18432\n\t" \
  "v_exp_f32 v224, v224\n\t" \
  "v_mfma_f32_16x16x32_bf16 v[156:159], v[208:211], v[12:15], v[156:159]\n\t" \
  "ds_read_b64_tr_b16 v[204:205], v237 offset:20480\n\t" \
  "v_exp_f32 v225, v225\n\t" \
  "v_mfma_f32_16x16x32_bf16 v[156:159], v[212:215], v[20:23], v[156:159]\n\t" \
  "ds_read_b64_tr_b16 v[206:207], v237 offset:22528\n\t" \
  "v_add_f32 v224, 1.0, v224\n\t" \
  "v_add_f32 v225, 1.0, v225\n\t" \
  "s_waitcnt lgkmcnt(6)\n\t" \
  "v_mfma_f32_16x16x32_bf16 v[144:147], v[216:219], v[24:27], v[144:147]\n\t" \
  "ds_read_b64_tr_b16 v[208:209], v236 offset:24576\n\t" \
  "v_rcp_f32 v224, v224\n\t" \
  "v_mfma_f32_16x16x32_bf16 v[144:147], v[220:223], v[32:35], v[144:147]\n\t" \
  "ds_read_b64_tr_b16 v[210:211], v236 offset:26624\n\t" \
  "v_rcp_f32 v225, v225\n\t" \
  "v_mfma_f32_16x16x32_bf16 v[144:147], v[192:195], v[40:43], v[144:147]\n\t" \
  "ds_read_b64_tr_b16 v[212:213], v236 offset:28672\n\t" \
  "v_mul_f32 v224, v224, v170\n\t" \
  "v_mul_f32 v225, v225, v171\n\t" \
  "v_mfma_f32_16x16x32_bf16 v[148:151], v[216:219], v[28:31], v[148:151]\n\t" \
  "ds_read_b64_tr_b16 v[214:215], v236 offset:30720\n\t" \
  "v_cvt_pk_bf16_f32 v163, v224, v225\n\t" \
  "s_add_u32 s75, s71, 8192\n\t" \
  "buffer_store_dwordx4 v[160:163], v244, s[48:51], s75 offen offset:0 sc1\n\t" \
  "v_mfma_f32_16x16x32_bf16 v[148:151], v[220:223], v[36:39], v[148:151]\n\t" \
  "ds_read_b64_tr_b16 v[216:217], v236 offset:32768\n\t" \
  "v_mul_f32 v224, v246, v164\n\t" \
  "v_mul_f32 v225, v246, v165\n\t" \
  "v_fmaak_f32 v226, v224, v224, 0x409d30fc\n\t" \
  "v_mfma_f32_16x16x32_bf16 v[148:151], v[192:195], v[44:47], v[148:151]\n\t" \
  "ds_read_b64_tr_b16 v[218:219], v236 offset:34816\n\t" \
  "v_fmaak_f32 v227, v225, v225, 0x409d30fc\n\t" \
  "v_mul_f32 v224, v224, v226\n\t" \
  "s_waitcnt lgkmcnt(6)\n\t" \
  "v_mfma_f32_16x16x32_bf16 v[152:155], v[196:199], v[24:27], v[152:155]\n\t" \
  "ds_read_b64_tr_b16 v[220:221], v237 offset:24576\n\t" \
  "v_mul_f32 v225, v225, v227\n\t" \
  "v_exp_f32 v224, v224\n\t" \
  "v_mfma_f32_16x16x32_bf16 v[152:155], v[200:203], v[32:35], v[152:155]\n\t" \
  "ds_read_b64_tr_b16 v[222:223], v237 offset:26624\n\t" \
  "v_exp_f32 v225, v225\n\t" \
  "v_mfma_f32_16x16x32_bf16 v[152:155], v[204:207], v[40:43], v[152:155]\n\t" \
  "ds_read_b64_tr_b16 v[192:193], v237 offset:28672\n\t" \
  "v_add_f32 v224, 1.0, v224\n\t" \
  "s_waitcnt vmcnt(3)\n\t" \
  "s_barrier\n\t" \
  "v_mfma_f32_16x16x32_bf16 v[156:159], v[196:199], v[28:31], v[156:159]\n\t" \
  "ds_read_b64_tr_b16 v[194:195], v237 offset:30720\n\t" \
  "v_add_f32 v225, 1.0, v225\n\t" \
  "v_rcp_f32 v224, v224\n\t" \
  "v_mfma_f32_16x16x32_bf16 v[156:159], v[200:203], v[36:39], v[156:159]\n\t" \
  "ds_read_b64_tr_b16 v[196:197], v237 offset:32768\n\t" \
  "v_rcp_f32 v225, v225\n\t" \
  "v_mfma_f32_16x16x32_bf16 v[156:159], v[204:207], v[44:47], v[156:159]\n\t" \
  "ds_read_b64_tr_b16 v[198:199], v237 offset:34816\n\t" \
  "v_mul_f32 v224, v224, v164\n\t" \
  "v_mul_f32 v225, v225, v165\n\t" \
  "s_waitcnt lgkmcnt(6)\n\t" \
  "v_mfma_f32_16x16x32_bf16 v[144:147], v[208:211], v[48:51], v[144:147]\n\t" \
  "ds_read_b64_tr_b16 v[200:201], v236 offset:36864\n\t" \
  "v_cvt_pk_bf16_f32 v164, v224, v225\n\t" \
  "v_mul_f32 v224, v246, v166\n\t" \
  "v_mfma_f32_16x16x32_bf16 v[144:147], v[212:215], v[56:59], v[144:147]\n\t" \
  "ds_read_b64_tr_b16 v[202:203], v236 offset:38912\n\t" \
  "v_mul_f32 v225, v246, v167\n\t" \
  "v_fmaak_f32 v226, v224, v224, 0x409d30fc\n\t" \
  "v_mfma_f32_16x16x32_bf16 v[144:147], v[216:219], v[64:67], v[144:147]\n\t" \
  "ds_read_b64_tr_b16 v[204:205], v236 offset:40960\n\t" \
  "v_fmaak_f32 v227, v225, v225, 0x409d30fc\n\t" \
  "v_mul_f32 v224, v224, v226\n\t" \
  "v_mfma_f32_16x16x32_bf16 v[148:151], v[208:211], v[52:55], v[148:151]\n\t" \
  "ds_read_b64_tr_b16 v[206:207], v236 offset:43008\n\t" \
  "v_mul_f32 v225, v225, v227\n\t" \
  "v_exp_f32 v224, v224\n\t" \
  "v_mfma_f32_16x16x32_bf16 v[148:151], v[212:215], v[60:63], v[148:151]\n\t" \
  "ds_read_b64_tr_b16 v[208:209], v236 offset:45056\n\t" \
  "v_exp_f32 v225, v225\n\t" \
  "v_mfma_f32_16x16x32_bf16 v[148:151], v[216:219], v[68:71], v[148:151]\n\t" \
  "ds_read_b64_tr_b16 v[210:211], v236 offset:47104\n\t" \
  "v_add_f32 v224, 1.0, v224\n\t" \
  "s_waitcnt lgkmcnt(6)\n\t" \
  "v_mfma_f32_16x16x32_bf16 v[152:155], v[220:223], v[48:51], v[152:155]\n\t" \
  "ds_read_b64_tr_b16 v[212:213], v237 offset:36864\n\t" \
  "v_add_f32 v225, 1.0, v225\n\t" \
  "v_rcp_f32 v224, v224\n\t" \
  "v_mfma_f32_16x16x32_bf16 v[152:155], v[192:195], v[56:59], v[152:155]\n\t" \
  "ds_read_b64_tr_b16 v[214:215], v237 offset:38912\n\t" \
  "v_rcp_f32 v225, v225\n\t" \
  "v_mfma_f32_16x16x32_bf16 v[152:155], v[196:199], v[64:67], v[152:155]\n\t" \
  "ds_read_b64_tr_b16 v[216:217], v237 offset:40960\n\t" \
  "v_mul_f32 v224, v224, v166\n\t" \
  "v_mul_f32 v225, v225, v167\n\t" \
  "v_mfma_f32_16x16x32_bf16 v[156:159], v[220:223], v[52:55], v[156:159]\n\t" \
  "ds_read_b64_tr_b16 v[218:219], v237 offset:43008\n\t" \
  "v_cvt_pk_bf16_f32 v165, v224, v225\n\t" \
  "v_mul_f32 v224, v246, v172\n\t" \
  "v_mfma_f32_16x16x32_bf16 v[156:159], v[192:195], v[60:63], v[156:159]\n\t" \
  "ds_read_b64_tr_b16 v[220:221], v237 offset:45056\n\t" \
  "v_mul_f32 v225, v246, v173\n\t" \
  "v_fmaak_f32 v226, v224, v224, 0x409d30fc\n\t" \
  "v_mfma_f32_16x16x32_bf16 v[156:159], v[196:199], v[68:71], v[156:159]\n\t" \
  "ds_read_b64_tr_b16 v[222:223], v237 offset:47104\n\t" \
  "v_fmaak_f32 v227, v225, v225, 0x409d30fc\n\t" \
  "v_mul_f32 v224, v224, v226\n\t" \
  "s_waitcnt lgkmcnt(6)\n\t" \
  "v_mfma_f32_16x16x32_bf16 v[144:147], v[200:203], v[72:75], v[144:147]\n\t" \
  "ds_read_b64_tr_b16 v[192:193], v236 offset:49152\n\t" \
  "v_mul_f32 v225, v225, v227\n\t" \
  "v_exp_f32 v224, v224\n\t" \
  "v_mfma_f32_16x16x32_bf16 v[144:147], v[204:207], v[80:83], v[144:147]\n\t" \
  "ds_read_b64_tr_b16 v[194:195], v236 offset:51200\n\t" \
  "v_exp_f32 v225, v225\n\t" \
  "v_mfma_f32_16x16x32_bf16 v[144:147], v[208:211], v[88:91], v[144:147]\n\t" \
  "ds_read_b64_tr_b16 v[196:197], v236 offset:53248\n\t" \
  "v_add_f32 v224, 1.0, v224\n\t" \
  "v_add_f32 v225, 1.0, v225\n\t" \
  "v_mfma_f32_16x16x32_bf16 v[148:151], v[200:203], v[76:79], v[148:151]\n\t" \
  "ds_read_b64_tr_b16 v[198:199], v236 offset:55296\n\t" \
  "v_rcp_f32 v224, v224\n\t" \
  "v_mfma_f32_16x16x32_bf16 v[148:151], v[204:207], v[84:87], v[148:151]\n\t" \
  "ds_read_b64_tr_b16 v[200:201], v236 offset:57344\n\t" \
  "v_rcp_f32 v225, v225\n\t" \
  "v_mfma_f32_16x16x32_bf16 v[148:151], v[208:211], v[92:95], v[148:151]\n\t" \
  "ds_read_b64_tr_b16 v[202:203], v236 offset:59392\n\t" \
  "v_mul_f32 v224, v224, v172\n\t" \
  "v_mul_f32 v225, v225, v173\n\t" \
  "s_waitcnt lgkmcnt(6)\n\t" \
  "v_mfma_f32_16x16x32_bf16 v[152:155], v[212:215], v[72:75], v[152:155]\n\t" \
  "ds_read_b64_tr_b16 v[204:205], v237 offset:49152\n\t" \
  "v_cvt_pk_bf16_f32 v166, v224, v225\n\t" \
  "v_mul_f32 v224, v246, v174\n\t" \
  "v_mfma_f32_16x16x32_bf16 v[152:155], v[216:219], v[80:83], v[152:155]\n\t" \
  "ds_read_b64_tr_b16 v[206:207], v237 offset:51200\n\t" \
  "v_mul_f32 v225, v246, v175\n\t" \
  "v_fmaak_f32 v226, v224, v224, 0x409d30fc\n\t" \
  "v_mfma_f32_16x16x32_bf16 v[152:155], v[220:223], v[88:91], v[152:155]\n\t" \
  "ds_read_b64_tr_b16 v[208:209], v237 offset:53248\n\t" \
  "v_fmaak_f32 v227, v225, v225, 0x409d30fc\n\t" \
  "v_mul_f32 v224, v224, v226\n\t" \
  "v_mul_f32 v225, v225, v227\n\t" \
  "v_mfma_f32_16x16x32_bf16 v[156:159], v[212:215], v[76:79], v[156:159]\n\t" \
  "ds_read_b64_tr_b16 v[210:211], v237 offset:55296\n\t" \
  "v_exp_f32 v224, v224\n\t" \
  "v_mfma_f32_16x16x32_bf16 v[156:159], v[216:219], v[84:87], v[156:159]\n\t" \
  "ds_read_b64_tr_b16 v[212:213], v237 offset:57344\n\t" \
  "v_exp_f32 v225, v225\n\t" \
  "v_mfma_f32_16x16x32_bf16 v[156:159], v[220:223], v[92:95], v[156:159]\n\t" \
  "ds_read_b64_tr_b16 v[214:215], v237 offset:59392\n\t" \
  "v_add_f32 v224, 1.0, v224\n\t" \
  "v_add_f32 v225, 1.0, v225\n\t" \
  "s_waitcnt lgkmcnt(6)\n\t" \
  "v_mfma_f32_16x16x32_bf16 v[144:147], v[192:195], v[96:99], v[144:147]\n\t" \
  "ds_read_b64_tr_b16 v[216:217], v240 offset:0\n\t" \
  "v_rcp_f32 v224, v224\n\t" \
  "v_mfma_f32_16x16x32_bf16 v[144:147], v[196:199], v[104:107], v[144:147]\n\t" \
  "ds_read_b64_tr_b16 v[218:219], v240 offset:2048\n\t" \
  "v_rcp_f32 v225, v225\n\t" \
  "v_mfma_f32_16x16x32_bf16 v[144:147], v[200:203], v[112:115], v[144:147]\n\t" \
  "ds_read_b64_tr_b16 v[220:221], v240 offset:4096\n\t" \
  "v_mul_f32 v224, v224, v174\n\t" \
  "v_mul_f32 v225, v225, v175\n\t" \
  "v_mfma_f32_16x16x32_bf16 v[148:151], v[192:195], v[100:103], v[148:151]\n\t" \
  "ds_read_b64_tr_b16 v[222:223], v240 offset:6144\n\t" \
  "v_cvt_pk_bf16_f32 v167, v224, v225\n\t" \
  "s_add_u32 s75, s71, 8192\n\t" \
  "buffer_store_dwordx4 v[164:167], v244, s[48:51], s75 offen offset:1024 sc1\n\t" \
  "v_mfma_f32_16x16x32_bf16 v[148:151], v[196:199], v[108:111], v[148:151]\n\t" \
  "ds_read_b64_tr_b16 v[192:193], v240 offset:8192\n\t" \
  "v_mul_f32 v224, v245, v176\n\t" \
  "v_mul_f32 v225, v245, v177\n\t" \
  "v_fmaak_f32 v226, v224, v224, 0x409d30fc\n\t" \
  "v_mfma_f32_16x16x32_bf16 v[148:151], v[200:203], v[116:119], v[148:151]\n\t" \
  "ds_read_b64_tr_b16 v[194:195], v240 offset:10240\n\t" \
  "v_fmaak_f32 v227, v225, v225, 0x409d30fc\n\t" \
  "v_mul_f32 v224, v224, v226\n\t" \
  "s_waitcnt lgkmcnt(6)\n\t" \
  "v_mfma_f32_16x16x32_bf16 v[152:155], v[204:207], v[96:99], v[152:155]\n\t" \
  "ds_read_b64_tr_b16 v[196:197], v241 offset:0\n\t" \
  "v_mul_f32 v225, v225, v227\n\t" \
  "v_exp_f32 v224, v224\n\t" \
  "v_mfma_f32_16x16x32_bf16 v[152:155], v[208:211], v[104:107], v[152:155]\n\t" \
  "ds_read_b64_tr_b16 v[198:199], v241 offset:2048\n\t" \
  "v_exp_f32 v225, v225\n\t" \
  "v_mfma_f32_16x16x32_bf16 v[152:155], v[212:215], v[112:115], v[152:155]\n\t" \
  "ds_read_b64_tr_b16 v[200:201], v241 offset:4096\n\t" \
  "v_add_f32 v224, 1.0, v224\n\t" \
  "v_mfma_f32_16x16x32_bf16 v[156:159], v[204:207], v[100:103], v[156:159]\n\t" \
  "ds_read_b64_tr_b16 v[202:203], v241 offset:6144\n\t" \
  "v_add_f32 v225, 1.0, v225\n\t" \
  "v_rcp_f32 v224, v224\n\t" \
  "v_mfma_f32_16x16x32_bf16 v[156:159], v[208:211], v[108:111], v[156:159]\n\t" \
  "ds_read_b64_tr_b16 v[204:205], v241 offset:8192\n\t" \
  "v_rcp_f32 v225, v225\n\t" \
  "v_mfma_f32_16x16x32_bf16 v[156:159], v[212:215], v[116:119], v[156:159]\n\t" \
  "ds_read_b64_tr_b16 v[206:207], v241 offset:10240\n\t" \
  "v_mul_f32 v224, v224, v176\n\t" \
  "v_mul_f32 v225, v225, v177\n\t" \
  "s_waitcnt lgkmcnt(6)\n\t" \
  "v_mfma_f32_16x16x32_bf16 v[144:147], v[216:219], v[120:123], v[144:147]\n\t" \
  "ds_read_b64_tr_b16 v[208:209], v238 offset:0\n\t" \
  "v_cvt_pk_bf16_f32 v176, v224, v225\n\t" \
  "v_mul_f32 v224, v245, v178\n\t" \
  "v_mfma_f32_16x16x32_bf16 v[144:147], v[220:223], v[128:131], v[144:147]\n\t" \
  "ds_read_b64_tr_b16 v[210:211], v238 offset:2048\n\t" \
  "v_mul_f32 v225, v245, v179\n\t" \
  "v_fmaak_f32 v226, v224, v224, 0x409d30fc\n\t" \
  "v_mfma_f32_16x16x32_bf16 v[144:147], v[192:195], v[136:139], v[144:147]\n\t" \
  "ds_read_b64_tr_b16 v[212:213], v238 offset:4096\n\t" \
  "v_fmaak_f32 v227, v225, v225, 0x409d30fc\n\t" \
  "v_mul_f32 v224, v224, v226\n\t" \
  "v_mfma_f32_16x16x32_bf16 v[148:151], v[216:219], v[124:127], v[148:151]\n\t" \
  "ds_read_b64_tr_b16 v[214:215], v238 offset:6144\n\t" \
  "v_mul_f32 v225, v225, v227\n\t" \
  "v_exp_f32 v224, v224\n\t" \
  "v_mfma_f32_16x16x32_bf16 v[148:151], v[220:223], v[132:135], v[148:151]\n\t" \
  "ds_read_b64_tr_b16 v[216:217], v238 offset:8192\n\t" \
  "v_exp_f32 v225, v225\n\t" \
  "v_mfma_f32_16x16x32_bf16 v[148:151], v[192:195], v[140:143], v[148:151]\n\t" \
  "ds_read_b64_tr_b16 v[218:219], v238 offset:10240\n\t" \
  "v_add_f32 v224, 1.0, v224\n\t" \
  "s_waitcnt lgkmcnt(6)\n\t" \
  "v_mfma_f32_16x16x32_bf16 v[152:155], v[196:199], v[120:123], v[152:155]\n\t" \
  "ds_read_b64_tr_b16 v[220:221], v239 offset:0\n\t" \
  "v_add_f32 v225, 1.0, v225\n\t" \
  "v_rcp_f32 v224, v224\n\t" \
  "v_mfma_f32_16x16x32_bf16 v[152:155], v[200:203], v[128:131], v[152:155]\n\t" \
  "ds_read_b64_tr_b16 v[222:223], v239 offset:2048\n\t" \
  "v_rcp_f32 v225, v225\n\t" \
  "v_mfma_f32_16x16x32_bf16 v[152:155], v[204:207], v[136:139], v[152:155]\n\t" \
  "ds_read_b64_tr_b16 v[192:193], v239 offset:4096\n\t" \
  "v_mul_f32 v224, v224, v178\n\t" \
  "v_mul_f32 v225, v225, v179\n\t" \
  "v_mfma_f32_16x16x32_bf16 v[156:159], v[196:199], v[124:127], v[156:159]\n\t" \
  "ds_read_b64_tr_b16 v[194:195], v239 offset:6144\n\t" \
  "v_cvt_pk_bf16_f32 v177, v224, v225\n\t" \
  "v_mul_f32 v224, v245, v184\n\t" \
  "v_mfma_f32_16x16x32_bf16 v[156:159], v[200:203], v[132:135], v[156:159]\n\t" \
  "ds_read_b64_tr_b16 v[196:197], v239 offset:8192\n\t" \
  "v_mul_f32 v225, v245, v185\n\t" \
  "v_fmaak_f32 v226, v224, v224, 0x409d30fc\n\t" \
  "v_mfma_f32_16x16x32_bf16 v[156:159], v[204:207], v[140:143], v[156:159]\n\t" \
  "ds_read_b64_tr_b16 v[198:199], v239 offset:10240\n\t" \
  "v_fmaak_f32 v227, v225, v225, 0x409d30fc\n\t" \
  "v_mul_f32 v224, v224, v226\n\t" \
  "s_waitcnt lgkmcnt(6)\n\t" \
  "v_mfma_f32_16x16x32_bf16 v[160:163], v[208:211], v[0:3], 0\n\t" \
  "ds_read_b64_tr_b16 v[200:201], v238 offset:12288\n\t" \
  "v_mul_f32 v225, v225, v227\n\t" \
  "v_exp_f32 v224, v224\n\t" \
  "v_mfma_f32_16x16x32_bf16 v[160:163], v[212:215], v[8:11], v[160:163]\n\t" \
  "ds_read_b64_tr_b16 v[202:203], v238 offset:14336\n\t" \
  "v_exp_f32 v225, v225\n\t" \
  "v_mfma_f32_16x16x32_bf16 v[160:163], v[216:219], v[16:19], v[160:163]\n\t" \
  "ds_read_b64_tr_b16 v[204:205], v238 offset:16384\n\t" \
  "v_add_f32 v224, 1.0, v224\n\t" \
  "v_add_f32 v225, 1.0, v225\n\t" \
  "v_mfma_f32_16x16x32_bf16 v[164:167], v[208:211], v[4:7], 0\n\t" \
  "ds_read_b64_tr_b16 v[206:207], v238 offset:18432\n\t" \
  "v_rcp_f32 v224, v224\n\t" \
  "v_mfma_f32_16x16x32_bf16 v[164:167], v[212:215], v[12:15], v[164:167]\n\t" \
  "ds_read_b64_tr_b16 v[208:209], v238 offset:20480\n\t" \
  "v_rcp_f32 v225, v225\n\t" \
  "v_mfma_f32_16x16x32_bf16 v[164:167], v[216:219], v[20:23], v[164:167]\n\t" \
  "ds_read_b64_tr_b16 v[210:211], v238 offset:22528\n\t" \
  "v_mul_f32 v224, v224, v184\n\t" \
  "v_mul_f32 v225, v225, v185\n\t" \
  "s_waitcnt lgkmcnt(6)\n\t" \
  "v_mfma_f32_16x16x32_bf16 v[168:171], v[220:223], v[0:3], 0\n\t" \
  "ds_read_b64_tr_b16 v[212:213], v239 offset:12288\n\t" \
  "v_cvt_pk_bf16_f32 v178, v224, v225\n\t" \
  "v_mul_f32 v224, v245, v186\n\t" \
  "v_mfma_f32_16x16x32_bf16 v[168:171], v[192:195], v[8:11], v[168:171]\n\t" \
  "ds_read_b64_tr_b16 v[214:215], v239 offset:14336\n\t" \
  "v_mul_f32 v225, v245, v187\n\t" \
  "v_fmaak_f32 v226, v224, v224, 0x409d30fc\n\t" \
  "v_mfma_f32_16x16x32_bf16 v[168:171], v[196:199], v[16:19], v[168:171]\n\t" \
  "ds_read_b64_tr_b16 v[216:217], v239 offset:16384\n\t" \
  "v_fmaak_f32 v227, v225, v225, 0x409d30fc\n\t" \
  "v_mul_f32 v224, v224, v226\n\t" \
  "v_mul_f32 v225, v225, v227\n\t" \
  "v_mfma_f32_16x16x32_bf16 v[172:175], v[220:223], v[4:7], 0\n\t" \
  "ds_read_b64_tr_b16 v[218:219], v239 offset:18432\n\t" \
  "v_exp_f32 v224, v224\n\t" \
  "v_mfma_f32_16x16x32_bf16 v[172:175], v[192:195], v[12:15], v[172:175]\n\t" \
  "ds_read_b64_tr_b16 v[220:221], v239 offset:20480\n\t" \
  "v_exp_f32 v225, v225\n\t" \
  "v_xor_b32 v232, s77, v232\n\t" \
  "v_mfma_f32_16x16x32_bf16 v[172:175], v[196:199], v[20:23], v[172:175]\n\t" \
  "ds_read_b64_tr_b16 v[222:223], v239 offset:22528\n\t" \
  "v_add_f32 v224, 1.0, v224\n\t" \
  "v_add_f32 v225, 1.0, v225\n\t" \
  "s_waitcnt lgkmcnt(6)\n\t" \
  "v_mfma_f32_16x16x32_bf16 v[160:163], v[200:203], v[24:27], v[160:163]\n\t" \
  "ds_read_b64_tr_b16 v[192:193], v238 offset:24576\n\t" \
  "v_rcp_f32 v224, v224\n\t" \
  "v_mfma_f32_16x16x32_bf16 v[160:163], v[204:207], v[32:35], v[160:163]\n\t" \
  "ds_read_b64_tr_b16 v[194:195], v238 offset:26624\n\t" \
  "v_rcp_f32 v225, v225\n\t" \
  "v_mfma_f32_16x16x32_bf16 v[160:163], v[208:211], v[40:43], v[160:163]\n\t" \
  "ds_read_b64_tr_b16 v[196:197], v238 offset:28672\n\t" \
  "v_mul_f32 v224, v224, v186\n\t" \
  "v_mul_f32 v225, v225, v187\n\t" \
  "v_xor_b32 v233, s77, v233\n\t" \
  "v_mfma_f32_16x16x32_bf16 v[164:167], v[200:203], v[28:31], v[164:167]\n\t" \
  "ds_read_b64_tr_b16 v[198:199], v238 offset:30720\n\t" \
  "v_cvt_pk_bf16_f32 v179, v224, v225\n\t" \
  "s_add_u32 s75, s71, 8192\n\t" \
  "buffer_store_dwordx4 v[176:179], v244, s[48:51], s75 offen offset:2048 sc1\n\t" \
  "v_mfma_f32_16x16x32_bf16 v[164:167], v[204:207], v[36:39], v[164:167]\n\t" \
  "ds_read_b64_tr_b16 v[200:201], v238 offset:32768\n\t" \
  "v_mul_f32 v224, v246, v180\n\t" \
  "v_mul_f32 v225, v246, v181\n\t" \
  "v_fmaak_f32 v226, v224, v224, 0x409d30fc\n\t" \
  "v_mfma_f32_16x16x32_bf16 v[164:167], v[208:211], v[44:47], v[164:167]\n\t" \
  "ds_read_b64_tr_b16 v[202:203], v238 offset:34816\n\t" \
  "v_fmaak_f32 v227, v225, v225, 0x409d30fc\n\t" \
  "v_mul_f32 v224, v224, v226\n\t" \
  "s_waitcnt lgkmcnt(6)\n\t" \
  "v_mfma_f32_16x16x32_bf16 v[168:171], v[212:215], v[24:27], v[168:171]\n\t" \
  "ds_read_b64_tr_b16 v[204:205], v239 offset:24576\n\t" \
  "v_mul_f32 v225, v225, v227\n\t" \
  "v_exp_f32 v224, v224\n\t" \
  "v_xor_b32 v236, s77, v236\n\t" \
  "v_mfma_f32_16x16x32_bf16 v[168:171], v[216:219], v[32:35], v[168:171]\n\t" \
  "ds_read_b64_tr_b16 v[206:207], v239 offset:26624\n\t" \
  "v_exp_f32 v225, v225\n\t" \
  "v_mfma_f32_16x16x32_bf16 v[168:171], v[220:223], v[40:43], v[168:171]\n\t" \
  "ds_read_b64_tr_b16 v[208:209], v239 offset:28672\n\t" \
  "v_add_f32 v224, 1.0, v224\n\t" \
  "s_waitcnt vmcnt(3)\n\t" \
  "s_barrier\n\t" \
  "v_mfma_f32_16x16x32_bf16 v[172:175], v[212:215], v[28:31], v[172:175]\n\t" \
  "ds_read_b64_tr_b16 v[210:211], v239 offset:30720\n\t" \
  "v_add_f32 v225, 1.0, v225\n\t" \
  "v_rcp_f32 v224, v224\n\t" \
  "v_mfma_f32_16x16x32_bf16 v[172:175], v[216:219], v[36:39], v[172:175]\n\t" \
  "ds_read_b64_tr_b16 v[212:213], v239 offset:32768\n\t" \
  "v_rcp_f32 v225, v225\n\t" \
  "v_xor_b32 v237, s77, v237\n\t" \
  "v_mfma_f32_16x16x32_bf16 v[172:175], v[220:223], v[44:47], v[172:175]\n\t" \
  "ds_read_b64_tr_b16 v[214:215], v239 offset:34816\n\t" \
  "v_mul_f32 v224, v224, v180\n\t" \
  "v_mul_f32 v225, v225, v181\n\t" \
  "s_waitcnt lgkmcnt(6)\n\t" \
  "v_mfma_f32_16x16x32_bf16 v[160:163], v[192:195], v[48:51], v[160:163]\n\t" \
  "ds_read_b64_tr_b16 v[216:217], v238 offset:36864\n\t" \
  "v_cvt_pk_bf16_f32 v180, v224, v225\n\t" \
  "v_mul_f32 v224, v246, v182\n\t" \
  "v_mfma_f32_16x16x32_bf16 v[160:163], v[196:199], v[56:59], v[160:163]\n\t" \
  "ds_read_b64_tr_b16 v[218:219], v238 offset:38912\n\t" \
  "v_mul_f32 v225, v246, v183\n\t" \
  "v_fmaak_f32 v226, v224, v224, 0x409d30fc\n\t" \
  "v_mfma_f32_16x16x32_bf16 v[160:163], v[200:203], v[64:67], v[160:163]\n\t" \
  "ds_read_b64_tr_b16 v[220:221], v238 offset:40960\n\t" \
  "v_fmaak_f32 v227, v225, v225, 0x409d30fc\n\t" \
  "v_mul_f32 v224, v224, v226\n\t" \
  "v_xor_b32 v240, s77, v240\n\t" \
  "v_mfma_f32_16x16x32_bf16 v[164:167], v[192:195], v[52:55], v[164:167]\n\t" \
  "ds_read_b64_tr_b16 v[222:223], v238 offset:43008\n\t" \
  "v_mul_f32 v225, v225, v227\n\t" \
  "v_exp_f32 v224, v224\n\t" \
  "v_mfma_f32_16x16x32_bf16 v[164:167], v[196:199], v[60:63], v[164:167]\n\t" \
  "ds_read_b64_tr_b16 v[192:193], v238 offset:45056\n\t" \
  "v_exp_f32 v225, v225\n\t" \
  "v_mfma_f32_16x16x32_bf16 v[164:167], v[200:203], v[68:71], v[164:167]\n\t" \
  "ds_read_b64_tr_b16 v[194:195], v238 offset:47104\n\t" \
  "v_add_f32 v224, 1.0, v224\n\t" \
  "s_waitcnt lgkmcnt(6)\n\t" \
  "v_mfma_f32_16x16x32_bf16 v[168:171], v[204:207], v[48:51], v[168:171]\n\t" \
  "ds_read_b64_tr_b16 v[196:197], v239 offset:36864\n\t" \
  "v_add_f32 v225, 1.0, v225\n\t" \
  "v_rcp_f32 v224, v224\n\t" \
  "v_xor_b32 v241, s77, v241\n\t" \
  "v_mfma_f32_16x16x32_bf16 v[168:171], v[208:211], v[56:59], v[168:171]\n\t" \
  "ds_read_b64_tr_b16 v[198:199], v239 offset:38912\n\t" \
  "v_rcp_f32 v225, v225\n\t" \
  "v_mfma_f32_16x16x32_bf16 v[168:171], v[212:215], v[64:67], v[168:171]\n\t" \
  "ds_read_b64_tr_b16 v[200:201], v239 offset:40960\n\t" \
  "v_mul_f32 v224, v224, v182\n\t" \
  "v_mul_f32 v225, v225, v183\n\t" \
  "v_mfma_f32_16x16x32_bf16 v[172:175], v[204:207], v[52:55], v[172:175]\n\t" \
  "ds_read_b64_tr_b16 v[202:203], v239 offset:43008\n\t" \
  "v_cvt_pk_bf16_f32 v181, v224, v225\n\t" \
  "v_mul_f32 v224, v246, v188\n\t" \
  "v_mfma_f32_16x16x32_bf16 v[172:175], v[208:211], v[60:63], v[172:175]\n\t" \
  "ds_read_b64_tr_b16 v[204:205], v239 offset:45056\n\t" \
  "v_mul_f32 v225, v246, v189\n\t" \
  "v_fmaak_f32 v226, v224, v224, 0x409d30fc\n\t" \
  "v_mfma_f32_16x16x32_bf16 v[172:175], v[212:215], v[68:71], v[172:175]\n\t" \
  "ds_read_b64_tr_b16 v[206:207], v239 offset:47104\n\t" \
  "v_fmaak_f32 v227, v225, v225, 0x409d30fc\n\t" \
  "v_mul_f32 v224, v224, v226\n\t" \
  "s_waitcnt lgkmcnt(6)\n\t" \
  "v_mfma_f32_16x16x32_bf16 v[160:163], v[216:219], v[72:75], v[160:163]\n\t" \
  "ds_read_b64_tr_b16 v[208:209], v238 offset:49152\n\t" \
  "v_mul_f32 v225, v225, v227\n\t" \
  "v_exp_f32 v224, v224\n\t" \
  "v_mfma_f32_16x16x32_bf16 v[160:163], v[220:223], v[80:83], v[160:163]\n\t" \
  "ds_read_b64_tr_b16 v[210:211], v238 offset:51200\n\t" \
  "v_exp_f32 v225, v225\n\t" \
  "v_mfma_f32_16x16x32_bf16 v[160:163], v[192:195], v[88:91], v[160:163]\n\t" \
  "ds_read_b64_tr_b16 v[212:213], v238 offset:53248\n\t" \
  "v_add_f32 v224, 1.0, v224\n\t" \
  "v_add_f32 v225, 1.0, v225\n\t" \
  "v_mfma_f32_16x16x32_bf16 v[164:167], v[216:219], v[76:79], v[164:167]\n\t" \
  "ds_read_b64_tr_b16 v[214:215], v238 offset:55296\n\t" \
  "v_rcp_f32 v224, v224\n\t" \
  "v_mfma_f32_16x16x32_bf16 v[164:167], v[220:223], v[84:87], v[164:167]\n\t" \
  "ds_read_b64_tr_b16 v[216:217], v238 offset:57344\n\t" \
  "v_rcp_f32 v225, v225\n\t" \
  "v_mfma_f32_16x16x32_bf16 v[164:167], v[192:195], v[92:95], v[164:167]\n\t" \
  "ds_read_b64_tr_b16 v[218:219], v238 offset:59392\n\t" \
  "v_mul_f32 v224, v224, v188\n\t" \
  "v_mul_f32 v225, v225, v189\n\t" \
  "s_waitcnt lgkmcnt(6)\n\t" \
  "v_mfma_f32_16x16x32_bf16 v[168:171], v[196:199], v[72:75], v[168:171]\n\t" \
  "ds_read_b64_tr_b16 v[220:221], v239 offset:49152\n\t" \
  "v_cvt_pk_bf16_f32 v182, v224, v225\n\t" \
  "v_mul_f32 v224, v246, v190\n\t" \
  "v_mfma_f32_16x16x32_bf16 v[168:171], v[200:203], v[80:83], v[168:171]\n\t" \
  "ds_read_b64_tr_b16 v[222:223], v239 offset:51200\n\t" \
  "v_mul_f32 v225, v246, v191\n\t" \
  "v_fmaak_f32 v226, v224, v224, 0x409d30fc\n\t" \
  "v_mfma_f32_16x16x32_bf16 v[168:171], v[204:207], v[88:91], v[168:171]\n\t" \
  "ds_read_b64_tr_b16 v[192:193], v239 offset:53248\n\t" \
  "v_fmaak_f32 v227, v225, v225, 0x409d30fc\n\t" \
  "v_mul_f32 v224, v224, v226\n\t" \
  "v_mul_f32 v225, v225, v227\n\t" \
  "v_mfma_f32_16x16x32_bf16 v[172:175], v[196:199], v[76:79], v[172:175]\n\t" \
  "ds_read_b64_tr_b16 v[194:195], v239 offset:55296\n\t" \
  "v_exp_f32 v224, v224\n\t" \
  "v_mfma_f32_16x16x32_bf16 v[172:175], v[200:203], v[84:87], v[172:175]\n\t" \
  "ds_read_b64_tr_b16 v[196:197], v239 offset:57344\n\t" \
  "v_exp_f32 v225, v225\n\t" \
  "v_mfma_f32_16x16x32_bf16 v[172:175], v[204:207], v[92:95], v[172:175]\n\t" \
  "ds_read_b64_tr_b16 v[198:199], v239 offset:59392\n\t" \
  "v_add_f32 v224, 1.0, v224\n\t" \
  "v_add_f32 v225, 1.0, v225\n\t" \
  "s_waitcnt lgkmcnt(6)\n\t" \
  "v_mfma_f32_16x16x32_bf16 v[160:163], v[208:211], v[96:99], v[160:163]\n\t" \
  "ds_read_b64_tr_b16 v[200:201], v242 offset:0\n\t" \
  "v_rcp_f32 v224, v224\n\t" \
  "v_mfma_f32_16x16x32_bf16 v[160:163], v[212:215], v[104:107], v[160:163]\n\t" \
  "ds_read_b64_tr_b16 v[202:203], v242 offset:2048\n\t" \
  "v_rcp_f32 v225, v225\n\t" \
  "v_mfma_f32_16x16x32_bf16 v[160:163], v[216:219], v[112:115], v[160:163]\n\t" \
  "ds_read_b64_tr_b16 v[204:205], v242 offset:4096\n\t" \
  "v_mul_f32 v224, v224, v190\n\t" \
  "v_mul_f32 v225, v225, v191\n\t" \
  "v_mfma_f32_16x16x32_bf16 v[164:167], v[208:211], v[100:103], v[164:167]\n\t" \
  "ds_read_b64_tr_b16 v[206:207], v242 offset:6144\n\t" \
  "v_cvt_pk_bf16_f32 v183, v224, v225\n\t" \
  "s_add_u32 s75, s71, 8192\n\t" \
  "buffer_store_dwordx4 v[180:183], v244, s[48:51], s75 offen offset:3072 sc1\n\t" \
  "v_mfma_f32_16x16x32_bf16 v[164:167], v[212:215], v[108:111], v[164:167]\n\t" \
  "ds_read_b64_tr_b16 v[208:209], v242 offset:8192\n\t" \
  "v_mul_f32 v224, v245, v144\n\t" \
  "v_mul_f32 v225, v245, v145\n\t" \
  "v_fmaak_f32 v226, v224, v224, 0x409d30fc\n\t" \
  "v_mfma_f32_16x16x32_bf16 v[164:167], v[216:219], v[116:119], v[164:167]\n\t" \
  "ds_read_b64_tr_b16 v[210:211], v242 offset:10240\n\t" \
  "v_fmaak_f32 v227, v225, v225, 0x409d30fc\n\t" \
  "v_mul_f32 v224, v224, v226\n\t" \
  "s_waitcnt lgkmcnt(6)\n\t" \
  "v_mfma_f32_16x16x32_bf16 v[168:171], v[220:223], v[96:99], v[168:171]\n\t" \
  "ds_read_b64_tr_b16 v[212:213], v243 offset:0\n\t" \
  "v_mul_f32 v225, v225, v227\n\t" \
  "v_exp_f32 v224, v224\n\t" \
  "v_mfma_f32_16x16x32_bf16 v[168:171], v[192:195], v[104:107], v[168:171]\n\t" \
  "ds_read_b64_tr_b16 v[214:215], v243 offset:2048\n\t" \
  "v_exp_f32 v225, v225\n\t" \
  "v_mfma_f32_16x16x32_bf16 v[168:171], v[196:199], v[112:115], v[168:171]\n\t" \
  "ds_read_b64_tr_b16 v[216:217], v243 offset:4096\n\t" \
  "v_add_f32 v224, 1.0, v224\n\t" \
  "s_barrier\n\t" \
  "v_mfma_f32_16x16x32_bf16 v[172:175], v[220:223], v[100:103], v[172:175]\n\t" \
  "ds_read_b64_tr_b16 v[218:219], v243 offset:6144\n\t" \
  "v_add_f32 v225, 1.0, v225\n\t" \
  "v_rcp_f32 v224, v224\n\t" \
  "v_mfma_f32_16x16x32_bf16 v[172:175], v[192:195], v[108:111], v[172:175]\n\t" \
  "ds_read_b64_tr_b16 v[220:221], v243 offset:8192\n\t" \
  "v_rcp_f32 v225, v225\n\t" \
  "v_mfma_f32_16x16x32_bf16 v[172:175], v[196:199], v[116:119], v[172:175]\n\t" \
  "ds_read_b64_tr_b16 v[222:223], v243 offset:10240\n\t" \
  "v_mul_f32 v224, v224, v144\n\t" \
  "v_mul_f32 v225, v225, v145\n\t" \
  "s_waitcnt lgkmcnt(6)\n\t" \
  "v_mfma_f32_16x16x32_bf16 v[160:163], v[200:203], v[120:123], v[160:163]\n\t" \
  "ds_read_b64_tr_b16 v[192:193], v232 offset:0\n\t" \
  "v_cvt_pk_bf16_f32 v144, v224, v225\n\t" \
  "v_mul_f32 v224, v245, v146\n\t" \
  "v_mfma_f32_16x16x32_bf16 v[160:163], v[204:207], v[128:131], v[160:163]\n\t" \
  "ds_read_b64_tr_b16 v[194:195], v232 offset:2048\n\t" \
  "v_mul_f32 v225, v245, v147\n\t" \
  "v_fmaak_f32 v226, v224, v224, 0x409d30fc\n\t" \
  "v_mfma_f32_16x16x32_bf16 v[160:163], v[208:211], v[136:139], v[160:163]\n\t" \
  "ds_read_b64_tr_b16 v[196:197], v232 offset:4096\n\t" \
  "v_fmaak_f32 v227, v225, v225, 0x409d30fc\n\t" \
  "v_mul_f32 v224, v224, v226\n\t" \
  "v_mfma_f32_16x16x32_bf16 v[164:167], v[200:203], v[124:127], v[164:167]\n\t" \
  "ds_read_b64_tr_b16 v[198:199], v232 offset:6144\n\t" \
  "v_mul_f32 v225, v225, v227\n\t" \
  "v_exp_f32 v224, v224\n\t" \
  "v_mfma_f32_16x16x32_bf16 v[164:167], v[204:207], v[132:135], v[164:167]\n\t" \
  "ds_read_b64_tr_b16 v[200:201], v232 offset:8192\n\t" \
  "v_exp_f32 v225, v225\n\t" \
  "v_mfma_f32_16x16x32_bf16 v[164:167], v[208:211], v[140:143], v[164:167]\n\t" \
  "ds_read_b64_tr_b16 v[202:203], v232 offset:10240\n\t" \
  "v_add_f32 v224, 1.0, v224\n\t" \
  "s_waitcnt lgkmcnt(6)\n\t" \
  "v_mfma_f32_16x16x32_bf16 v[168:171], v[212:215], v[120:123], v[168:171]\n\t" \
  "ds_read_b64_tr_b16 v[204:205], v233 offset:0\n\t" \
  "v_add_f32 v225, 1.0, v225\n\t" \
  "v_rcp_f32 v224, v224\n\t" \
  "v_mfma_f32_16x16x32_bf16 v[168:171], v[216:219], v[128:131], v[168:171]\n\t" \
  "ds_read_b64_tr_b16 v[206:207], v233 offset:2048\n\t" \
  "v_rcp_f32 v225, v225\n\t" \
  "v_mfma_f32_16x16x32_bf16 v[168:171], v[220:223], v[136:139], v[168:171]\n\t" \
  "ds_read_b64_tr_b16 v[208:209], v233 offset:4096\n\t" \
  "v_mul_f32 v224, v224, v146\n\t" \
  "v_mul_f32 v225, v225, v147\n\t" \
  "v_mfma_f32_16x16x32_bf16 v[172:175], v[212:215], v[124:127], v[172:175]\n\t" \
  "ds_read_b64_tr_b16 v[210:211], v233 offset:6144\n\t" \
  "v_cvt_pk_bf16_f32 v145, v224, v225\n\t" \
  "v_mul_f32 v224, v245, v152\n\t" \
  "v_mfma_f32_16x16x32_bf16 v[172:175], v[216:219], v[132:135], v[172:175]\n\t" \
  "ds_read_b64_tr_b16 v[212:213], v233 offset:8192\n\t" \
  "v_mul_f32 v225, v245, v153\n\t" \
  "v_fmaak_f32 v226, v224, v224, 0x409d30fc\n\t" \
  "v_mfma_f32_16x16x32_bf16 v[172:175], v[220:223], v[140:143], v[172:175]\n\t" \
  "ds_read_b64_tr_b16 v[214:215], v233 offset:10240\n\t" \
  "v_fmaak_f32 v227, v225, v225, 0x409d30fc\n\t" \
  "v_mul_f32 v224, v224, v226\n\t" \
  "s_waitcnt lgkmcnt(6)\n\t" \
  "v_mfma_f32_16x16x32_bf16 v[176:179], v[192:195], v[0:3], 0\n\t" \
  "ds_read_b64_tr_b16 v[216:217], v240 offset:12288\n\t" \
  "v_mul_f32 v225, v225, v227\n\t" \
  "v_exp_f32 v224, v224\n\t" \
  "v_mfma_f32_16x16x32_bf16 v[176:179], v[196:199], v[8:11], v[176:179]\n\t" \
  "ds_read_b64_tr_b16 v[218:219], v240 offset:14336\n\t" \
  "v_exp_f32 v225, v225\n\t" \
  "v_mfma_f32_16x16x32_bf16 v[176:179], v[200:203], v[16:19], v[176:179]\n\t" \
  "ds_read_b64_tr_b16 v[220:221], v240 offset:16384\n\t" \
  "v_add_f32 v224, 1.0, v224\n\t" \
  "v_add_f32 v225, 1.0, v225\n\t" \
  "v_mfma_f32_16x16x32_bf16 v[180:183], v[192:195], v[4:7], 0\n\t" \
  "ds_read_b64_tr_b16 v[222:223], v240 offset:18432\n\t" \
  "v_rcp_f32 v224, v224\n\t" \
  "v_mfma_f32_16x16x32_bf16 v[180:183], v[196:199], v[12:15], v[180:183]\n\t" \
  "ds_read_b64_tr_b16 v[192:193], v240 offset:20480\n\t" \
  "v_rcp_f32 v225, v225\n\t" \
  "v_mfma_f32_16x16x32_bf16 v[180:183], v[200:203], v[20:23], v[180:183]\n\t" \
  "ds_read_b64_tr_b16 v[194:195], v240 offset:22528\n\t" \
  "v_mul_f32 v224, v224, v152\n\t" \
  "v_mul_f32 v225, v225, v153\n\t" \
  "s_waitcnt lgkmcnt(6)\n\t" \
  "v_mfma_f32_16x16x32_bf16 v[184:187], v[204:207], v[0:3], 0\n\t" \
  "ds_read_b64_tr_b16 v[196:197], v241 offset:12288\n\t" \
  "v_cvt_pk_bf16_f32 v146, v224, v225\n\t" \
  "v_mul_f32 v224, v245, v154\n\t" \
  "v_mfma_f32_16x16x32_bf16 v[184:187], v[208:211], v[8:11], v[184:187]\n\t" \
  "ds_read_b64_tr_b16 v[198:199], v241 offset:14336\n\t" \
  "v_mul_f32 v225, v245, v155\n\t" \
  "v_fmaak_f32 v226, v224, v224, 0x409d30fc\n\t" \
  "v_mfma_f32_16x16x32_bf16 v[184:187], v[212:215], v[16:19], v[184:187]\n\t" \
  "ds_read_b64_tr_b16 v[200:201], v241 offset:16384\n\t" \
  "v_fmaak_f32 v227, v225, v225, 0x409d30fc\n\t" \
  "v_mul_f32 v224, v224, v226\n\t" \
  "v_mul_f32 v225, v225, v227\n\t" \
  "v_mfma_f32_16x16x32_bf16 v[188:191], v[204:207], v[4:7], 0\n\t" \
  "ds_read_b64_tr_b16 v[202:203], v241 offset:18432\n\t" \
  "v_exp_f32 v224, v224\n\t" \
  "v_mfma_f32_16x16x32_bf16 v[188:191], v[208:211], v[12:15], v[188:191]\n\t" \
  "ds_read_b64_tr_b16 v[204:205], v241 offset:20480\n\t" \
  "v_exp_f32 v225, v225\n\t" \
  "v_mfma_f32_16x16x32_bf16 v[188:191], v[212:215], v[20:23], v[188:191]\n\t" \
  "ds_read_b64_tr_b16 v[206:207], v241 offset:22528\n\t" \
  "v_add_f32 v224, 1.0, v224\n\t" \
  "v_add_f32 v225, 1.0, v225\n\t" \
  "s_waitcnt lgkmcnt(6)\n\t" \
  "v_mfma_f32_16x16x32_bf16 v[176:179], v[216:219], v[24:27], v[176:179]\n\t" \
  "ds_read_b64_tr_b16 v[208:209], v240 offset:24576\n\t" \
  "v_rcp_f32 v224, v224\n\t" \
  "v_mfma_f32_16x16x32_bf16 v[176:179], v[220:223], v[32:35], v[176:179]\n\t" \
  "ds_read_b64_tr_b16 v[210:211], v240 offset:26624\n\t" \
  "v_rcp_f32 v225, v225\n\t" \
  "v_mfma_f32_16x16x32_bf16 v[176:179], v[192:195], v[40:43], v[176:179]\n\t" \
  "ds_read_b64_tr_b16 v[212:213], v240 offset:28672\n\t" \
  "v_mul_f32 v224, v224, v154\n\t" \
  "v_mul_f32 v225, v225, v155\n\t" \
  "v_mfma_f32_16x16x32_bf16 v[180:183], v[216:219], v[28:31], v[180:183]\n\t" \
  "ds_read_b64_tr_b16 v[214:215], v240 offset:30720\n\t" \
  "v_cvt_pk_bf16_f32 v147, v224, v225\n\t" \
  "s_add_u32 s75, s71, 12288\n\t" \
  "buffer_store_dwordx4 v[144:147], v244, s[48:51], s75 offen offset:0 sc1\n\t" \
  "v_mfma_f32_16x16x32_bf16 v[180:183], v[220:223], v[36:39], v[180:183]\n\t" \
  "ds_read_b64_tr_b16 v[216:217], v240 offset:32768\n\t" \
  "v_mul_f32 v224, v246, v148\n\t" \
  "v_mul_f32 v225, v246, v149\n\t" \
  "v_fmaak_f32 v226, v224, v224, 0x409d30fc\n\t" \
  "v_mfma_f32_16x16x32_bf16 v[180:183], v[192:195], v[44:47], v[180:183]\n\t" \
  "ds_read_b64_tr_b16 v[218:219], v240 offset:34816\n\t" \
  "v_fmaak_f32 v227, v225, v225, 0x409d30fc\n\t" \
  "v_mul_f32 v224, v224, v226\n\t" \
  "s_waitcnt lgkmcnt(6)\n\t" \
  "v_mfma_f32_16x16x32_bf16 v[184:187], v[196:199], v[24:27], v[184:187]\n\t" \
  "ds_read_b64_tr_b16 v[220:221], v241 offset:24576\n\t" \
  "v_mul_f32 v225, v225, v227\n\t" \
  "v_exp_f32 v224, v224\n\t" \
  "v_mfma_f32_16x16x32_bf16 v[184:187], v[200:203], v[32:35], v[184:187]\n\t" \
  "ds_read_b64_tr_b16 v[222:223], v241 offset:26624\n\t" \
  "v_exp_f32 v225, v225\n\t" \
  "v_mfma_f32_16x16x32_bf16 v[184:187], v[204:207], v[40:43], v[184:187]\n\t" \
  "ds_read_b64_tr_b16 v[192:193], v241 offset:28672\n\t" \
  "v_add_f32 v224, 1.0, v224\n\t" \
  "s_waitcnt vmcnt(3)\n\t" \
  "s_barrier\n\t" \
  "v_mfma_f32_16x16x32_bf16 v[188:191], v[196:199], v[28:31], v[188:191]\n\t" \
  "ds_read_b64_tr_b16 v[194:195], v241 offset:30720\n\t" \
  "v_add_f32 v225, 1.0, v225\n\t" \
  "v_rcp_f32 v224, v224\n\t" \
  "v_mfma_f32_16x16x32_bf16 v[188:191], v[200:203], v[36:39], v[188:191]\n\t" \
  "ds_read_b64_tr_b16 v[196:197], v241 offset:32768\n\t" \
  "v_rcp_f32 v225, v225\n\t" \
  "v_mfma_f32_16x16x32_bf16 v[188:191], v[204:207], v[44:47], v[188:191]\n\t" \
  "ds_read_b64_tr_b16 v[198:199], v241 offset:34816\n\t" \
  "v_mul_f32 v224, v224, v148\n\t" \
  "v_mul_f32 v225, v225, v149\n\t" \
  "s_waitcnt lgkmcnt(6)\n\t" \
  "v_mfma_f32_16x16x32_bf16 v[176:179], v[208:211], v[48:51], v[176:179]\n\t" \
  "ds_read_b64_tr_b16 v[200:201], v232 offset:12288\n\t" \
  "v_cvt_pk_bf16_f32 v148, v224, v225\n\t" \
  "v_mul_f32 v224, v246, v150\n\t" \
  "v_mfma_f32_16x16x32_bf16 v[176:179], v[212:215], v[56:59], v[176:179]\n\t" \
  "ds_read_b64_tr_b16 v[202:203], v232 offset:14336\n\t" \
  "v_mul_f32 v225, v246, v151\n\t" \
  "v_fmaak_f32 v226, v224, v224, 0x409d30fc\n\t" \
  "v_mfma_f32_16x16x32_bf16 v[176:179], v[216:219], v[64:67], v[176:179]\n\t" \
  "ds_read_b64_tr_b16 v[204:205], v232 offset:16384\n\t" \
  "v_fmaak_f32 v227, v225, v225, 0x409d30fc\n\t" \
  "v_mul_f32 v224, v224, v226\n\t" \
  "v_mfma_f32_16x16x32_bf16 v[180:183], v[208:211], v[52:55], v[180:183]\n\t" \
  "ds_read_b64_tr_b16 v[206:207], v232 offset:18432\n\t" \
  "v_mul_f32 v225, v225, v227\n\t" \
  "v_exp_f32 v224, v224\n\t" \
  "v_mfma_f32_16x16x32_bf16 v[180:183], v[212:215], v[60:63], v[180:183]\n\t" \
  "ds_read_b64_tr_b16 v[208:209], v232 offset:20480\n\t" \
  "v_exp_f32 v225, v225\n\t" \
  "v_mfma_f32_16x16x32_bf16 v[180:183], v[216:219], v[68:71], v[180:183]\n\t" \
  "ds_read_b64_tr_b16 v[210:211], v232 offset:22528\n\t" \
  "v_add_f32 v224, 1.0, v224\n\t" \
  "s_waitcnt lgkmcnt(6)\n\t" \
  "v_mfma_f32_16x16x32_bf16 v[184:187], v[220:223], v[48:51], v[184:187]\n\t" \
  "ds_read_b64_tr_b16 v[212:213], v233 offset:12288\n\t" \
  "v_add_f32 v225, 1.0, v225\n\t" \
  "v_rcp_f32 v224, v224\n\t" \
  "v_mfma_f32_16x16x32_bf16 v[184:187], v[192:195], v[56:59], v[184:187]\n\t" \
  "ds_read_b64_tr_b16 v[214:215], v233 offset:14336\n\t" \
  "v_rcp_f32 v225, v225\n\t" \
  "v_mfma_f32_16x16x32_bf16 v[184:187], v[196:199], v[64:67], v[184:187]\n\t" \
  "ds_read_b64_tr_b16 v[216:217], v233 offset:16384\n\t" \
  "v_mul_f32 v224, v224, v150\n\t" \
  "v_mul_f32 v225, v225, v151\n\t" \
  "v_mfma_f32_16x16x32_bf16 v[188:191], v[220:223], v[52:55], v[188:191]\n\t" \
  "ds_read_b64_tr_b16 v[218:219], v233 offset:18432\n\t" \
  "ds_read_b128 v[0:3], v228 offset:24576\n\t" \
  "v_cvt_pk_bf16_f32 v149, v224, v225\n\t" \
  "v_mul_f32 v224, v246, v156\n\t" \
  "v_mfma_f32_16x16x32_bf16 v[188:191], v[192:195], v[60:63], v[188:191]\n\t" \
  "ds_read_b64_tr_b16 v[220:221], v233 offset:20480\n\t" \
  "v_mul_f32 v225, v246, v157\n\t" \
  "v_fmaak_f32 v226, v224, v224, 0x409d30fc\n\t" \
  "v_mfma_f32_16x16x32_bf16 v[188:191], v[196:199], v[68:71], v[188:191]\n\t" \
  "ds_read_b64_tr_b16 v[222:223], v233 offset:22528\n\t" \
  "v_fmaak_f32 v227, v225, v225, 0x409d30fc\n\t" \
  "v_mul_f32 v224, v224, v226\n\t" \
  "s_waitcnt lgkmcnt(7)\n\t" \
  "v_mfma_f32_16x16x32_bf16 v[176:179], v[200:203], v[72:75], v[176:179]\n\t" \
  "ds_read_b64_tr_b16 v[192:193], v232 offset:36864\n\t" \
  "v_mul_f32 v225, v225, v227\n\t" \
  "v_exp_f32 v224, v224\n\t" \
  "v_mfma_f32_16x16x32_bf16 v[176:179], v[204:207], v[80:83], v[176:179]\n\t" \
  "ds_read_b64_tr_b16 v[194:195], v232 offset:38912\n\t" \
  "v_exp_f32 v225, v225\n\t" \
  "v_mfma_f32_16x16x32_bf16 v[176:179], v[208:211], v[88:91], v[176:179]\n\t" \
  "ds_read_b64_tr_b16 v[196:197], v232 offset:40960\n\t" \
  "ds_read_b128 v[4:7], v228 offset:25600\n\t" \
  "v_add_f32 v224, 1.0, v224\n\t" \
  "v_add_f32 v225, 1.0, v225\n\t" \
  "v_mfma_f32_16x16x32_bf16 v[180:183], v[200:203], v[76:79], v[180:183]\n\t" \
  "ds_read_b64_tr_b16 v[198:199], v232 offset:43008\n\t" \
  "v_rcp_f32 v224, v224\n\t" \
  "v_mfma_f32_16x16x32_bf16 v[180:183], v[204:207], v[84:87], v[180:183]\n\t" \
  "ds_read_b64_tr_b16 v[200:201], v232 offset:45056\n\t" \
  "v_rcp_f32 v225, v225\n\t" \
  "v_mfma_f32_16x16x32_bf16 v[180:183], v[208:211], v[92:95], v[180:183]\n\t" \
  "ds_read_b64_tr_b16 v[202:203], v232 offset:47104\n\t" \
  "v_mul_f32 v224, v224, v156\n\t" \
  "v_mul_f32 v225, v225, v157\n\t" \
  "s_waitcnt lgkmcnt(7)\n\t" \
  "v_mfma_f32_16x16x32_bf16 v[184:187], v[212:215], v[72:75], v[184:187]\n\t" \
  "ds_read_b64_tr_b16 v[204:205], v233 offset:36864\n\t" \
  "v_cvt_pk_bf16_f32 v150, v224, v225\n\t" \
  "v_mul_f32 v224, v246, v158\n\t" \
  "v_mfma_f32_16x16x32_bf16 v[184:187], v[216:219], v[80:83], v[184:187]\n\t" \
  "ds_read_b64_tr_b16 v[206:207], v233 offset:38912\n\t" \
  "ds_read_b128 v[8:11], v228 offset:26624\n\t" \
  "v_mul_f32 v225, v246, v159\n\t" \
  "v_fmaak_f32 v226, v224, v224, 0x409d30fc\n\t" \
  "v_mfma_f32_16x16x32_bf16 v[184:187], v[220:223], v[88:91], v[184:187]\n\t" \
  "ds_read_b64_tr_b16 v[208:209], v233 offset:40960\n\t" \
  "v_fmaak_f32 v227, v225, v225, 0x409d30fc\n\t" \
  "v_mul_f32 v224, v224, v226\n\t" \
  "v_mul_f32 v225, v225, v227\n\t" \
  "v_mfma_f32_16x16x32_bf16 v[188:191], v[212:215], v[76:79], v[188:191]\n\t" \
  "ds_read_b64_tr_b16 v[210:211], v233 offset:43008\n\t" \
  "v_exp_f32 v224, v224\n\t" \
  "v_mfma_f32_16x16x32_bf16 v[188:191], v[216:219], v[84:87], v[188:191]\n\t" \
  "ds_read_b64_tr_b16 v[212:213], v233 offset:45056\n\t" \
  "v_exp_f32 v225, v225\n\t" \
  "v_mfma_f32_16x16x32_bf16 v[188:191], v[220:223], v[92:95], v[188:191]\n\t" \
  "ds_read_b64_tr_b16 v[214:215], v233 offset:47104\n\t" \
  "v_add_f32 v224, 1.0, v224\n\t" \
  "v_add_f32 v225, 1.0, v225\n\t" \
  "s_waitcnt lgkmcnt(7)\n\t" \
  "v_mfma_f32_16x16x32_bf16 v[176:179], v[192:195], v[96:99], v[176:179]\n\t" \
  "ds_read_b64_tr_b16 v[216:217], v232 offset:49152\n\t" \
  "ds_read_b128 v[12:15], v229 offset:0\n\t" \
  "v_rcp_f32 v224, v224\n\t" \
  "v_mfma_f32_16x16x32_bf16 v[176:179], v[196:199], v[104:107], v[176:179]\n\t" \
  "ds_read_b64_tr_b16 v[218:219], v232 offset:51200\n\t" \
  "v_rcp_f32 v225, v225\n\t" \
  "v_mfma_f32_16x16x32_bf16 v[176:179], v[200:203], v[112:115], v[176:179]\n\t" \
  "ds_read_b64_tr_b16 v[220:221], v232 offset:53248\n\t" \
  "v_mul_f32 v224, v224, v158\n\t" \
  "v_mul_f32 v225, v225, v159\n\t" \
  "v_mfma_f32_16x16x32_bf16 v[180:183], v[192:195], v[100:103], v[180:183]\n\t" \
  "ds_read_b64_tr_b16 v[222:223], v232 offset:55296\n\t" \
  "v_cvt_pk_bf16_f32 v151, v224, v225\n\t" \
  "s_add_u32 s75, s71, 12288\n\t" \
  "buffer_store_dwordx4 v[148:151], v244, s[48:51], s75 offen offset:1024 sc1\n\t" \
  "v_mfma_f32_16x16x32_bf16 v[180:183], v[196:199], v[108:111], v[180:183]\n\t" \
  "ds_read_b64_tr_b16 v[192:193], v232 offset:57344\n\t" \
  "v_mul_f32 v224, v245, v160\n\t" \
  "v_mul_f32 v225, v245, v161\n\t" \
  "v_fmaak_f32 v226, v224, v224, 0x409d30fc\n\t" \
  "v_mfma_f32_16x16x32_bf16 v[180:183], v[200:203], v[116:119], v[180:183]\n\t" \
  "ds_read_b64_tr_b16 v[194:195], v232 offset:59392\n\t" \
  "ds_read_b128 v[16:19], v229 offset:1024\n\t" \
  "v_fmaak_f32 v227, v225, v225, 0x409d30fc\n\t" \
  "v_mul_f32 v224, v224, v226\n\t" \
  "s_waitcnt lgkmcnt(8)\n\t" \
  "v_mfma_f32_16x16x32_bf16 v[184:187], v[204:207], v[96:99], v[184:187]\n\t" \
  "ds_read_b64_tr_b16 v[196:197], v233 offset:49152\n\t" \
  "v_mul_f32 v225, v225, v227\n\t" \
  "v_exp_f32 v224, v224\n\t" \
  "v_mfma_f32_16x16x32_bf16 v[184:187], v[208:211], v[104:107], v[184:187]\n\t" \
  "ds_read_b64_tr_b16 v[198:199], v233 offset:51200\n\t" \
  "v_exp_f32 v225, v225\n\t" \
  "v_mfma_f32_16x16x32_bf16 v[184:187], v[212:215], v[112:115], v[184:187]\n\t" \
  "ds_read_b64_tr_b16 v[200:201], v233 offset:53248\n\t" \
  "v_add_f32 v224, 1.0, v224\n\t" \
  "s_barrier\n\t" \
  "v_mfma_f32_16x16x32_bf16 v[188:191], v[204:207], v[100:103], v[188:191]\n\t" \
  "ds_read_b64_tr_b16 v[202:203], v233 offset:55296\n\t" \
  "v_add_f32 v225, 1.0, v225\n\t" \
  "v_rcp_f32 v224, v224\n\t" \
  "v_mfma_f32_16x16x32_bf16 v[188:191], v[208:211], v[108:111], v[188:191]\n\t" \
  "ds_read_b64_tr_b16 v[204:205], v233 offset:57344\n\t" \
  "ds_read_b128 v[20:23], v229 offset:2048\n\t" \
  "v_rcp_f32 v225, v225\n\t" \
  "v_mfma_f32_16x16x32_bf16 v[188:191], v[212:215], v[116:119], v[188:191]\n\t" \
  "ds_read_b64_tr_b16 v[206:207], v233 offset:59392\n\t" \
  "v_mul_f32 v224, v224, v160\n\t" \
  "v_mul_f32 v225, v225, v161\n\t" \
  "s_waitcnt lgkmcnt(8)\n\t" \
  "v_mfma_f32_16x16x32_bf16 v[176:179], v[216:219], v[120:123], v[176:179]\n\t" \
  "v_xor_b32 v232, s77, v232\n\t" \
  "v_xor_b32 v233, s77, v233\n\t" \
  "v_xor_b32 v236, s77, v236\n\t" \
  "v_xor_b32 v237, s77, v237\n\t" \
  "v_xor_b32 v240, s77, v240\n\t" \
  "v_xor_b32 v241, s77, v241\n\t" \
  "ds_read_b64_tr_b16 v[208:209], v236 offset:12288\n\t" \
  "v_cvt_pk_bf16_f32 v160, v224, v225\n\t" \
  "v_mul_f32 v224, v245, v162\n\t" \
  "v_mfma_f32_16x16x32_bf16 v[176:179], v[220:223], v[128:131], v[176:179]\n\t" \
  "ds_read_b64_tr_b16 v[210:211], v236 offset:14336\n\t" \
  "v_mul_f32 v225, v245, v163\n\t" \
  "v_fmaak_f32 v226, v224, v224, 0x409d30fc\n\t" \
  "v_mfma_f32_16x16x32_bf16 v[176:179], v[192:195], v[136:139], v[176:179]\n\t" \
  "ds_read_b64_tr_b16 v[212:213], v236 offset:16384\n\t" \
  "v_fmaak_f32 v227, v225, v225, 0x409d30fc\n\t" \
  "v_mul_f32 v224, v224, v226\n\t" \
  "v_mfma_f32_16x16x32_bf16 v[180:183], v[216:219], v[124:127], v[180:183]\n\t" \
  "ds_read_b64_tr_b16 v[214:215], v236 offset:18432\n\t" \
  "v_mul_f32 v225, v225, v227\n\t" \
  "v_exp_f32 v224, v224\n\t" \
  "v_mfma_f32_16x16x32_bf16 v[180:183], v[220:223], v[132:135], v[180:183]\n\t" \
  "ds_read_b64_tr_b16 v[216:217], v236 offset:20480\n\t" \
  "v_exp_f32 v225, v225\n\t" \
  "v_mfma_f32_16x16x32_bf16 v[180:183], v[192:195], v[140:143], v[180:183]\n\t" \
  "ds_read_b64_tr_b16 v[218:219], v236 offset:22528\n\t" \
  "v_add_f32 v224, 1.0, v224\n\t" \
  "s_waitcnt lgkmcnt(6)\n\t" \
  "v_mfma_f32_16x16x32_bf16 v[184:187], v[196:199], v[120:123], v[184:187]\n\t" \
  "ds_read_b64_tr_b16 v[220:221], v237 offset:12288\n\t" \
  "v_add_f32 v225, 1.0, v225\n\t" \
  "v_rcp_f32 v224, v224\n\t" \
  "v_mfma_f32_16x16x32_bf16 v[184:187], v[200:203], v[128:131], v[184:187]\n\t" \
  "ds_read_b64_tr_b16 v[222:223], v237 offset:14336\n\t" \
  "v_rcp_f32 v225, v225\n\t" \
  "v_mfma_f32_16x16x32_bf16 v[184:187], v[204:207], v[136:139], v[184:187]\n\t" \
  "ds_read_b64_tr_b16 v[192:193], v237 offset:16384\n\t" \
  "v_mul_f32 v224, v224, v162\n\t" \
  "v_mul_f32 v225, v225, v163\n\t" \
  "v_mfma_f32_16x16x32_bf16 v[188:191], v[196:199], v[124:127], v[188:191]\n\t" \
  "ds_read_b64_tr_b16 v[194:195], v237 offset:18432\n\t" \
  "v_cvt_pk_bf16_f32 v161, v224, v225\n\t" \
  "v_mul_f32 v224, v245, v168\n\t" \
  "v_mfma_f32_16x16x32_bf16 v[188:191], v[200:203], v[132:135], v[188:191]\n\t" \
  "ds_read_b64_tr_b16 v[196:197], v237 offset:20480\n\t" \
  "v_mul_f32 v225, v245, v169\n\t" \
  "v_fmaak_f32 v226, v224, v224, 0x409d30fc\n\t" \
  "v_mfma_f32_16x16x32_bf16 v[188:191], v[204:207], v[140:143], v[188:191]\n\t" \
  "ds_read_b64_tr_b16 v[198:199], v237 offset:22528\n\t" \
  "v_fmaak_f32 v227, v225, v225, 0x409d30fc\n\t" \
  "v_mul_f32 v224, v224, v226\n\t" \
  "s_waitcnt lgkmcnt(6)\n\t" \
  "v_mfma_f32_16x16x32_bf16 v[80:83], v[208:211], v[0:3], 0\n\t" \
  "ds_read_b64_tr_b16 v[200:201], v238 offset:12288\n\t" \
  "v_mul_f32 v225, v225, v227\n\t" \
  "v_exp_f32 v224, v224\n\t" \
  "v_mfma_f32_16x16x32_bf16 v[80:83], v[212:215], v[8:11], v[80:83]\n\t" \
  "ds_read_b64_tr_b16 v[202:203], v238 offset:14336\n\t" \
  "v_exp_f32 v225, v225\n\t" \
  "v_mfma_f32_16x16x32_bf16 v[80:83], v[216:219], v[16:19], v[80:83]\n\t" \
  "ds_read_b64_tr_b16 v[204:205], v238 offset:16384\n\t" \
  "v_add_f32 v224, 1.0, v224\n\t" \
  "v_add_f32 v225, 1.0, v225\n\t" \
  "v_mfma_f32_16x16x32_bf16 v[84:87], v[208:211], v[4:7], 0\n\t" \
  "ds_read_b64_tr_b16 v[206:207], v238 offset:18432\n\t" \
  "v_rcp_f32 v224, v224\n\t" \
  "v_mfma_f32_16x16x32_bf16 v[84:87], v[212:215], v[12:15], v[84:87]\n\t" \
  "ds_read_b64_tr_b16 v[208:209], v238 offset:20480\n\t" \
  "v_rcp_f32 v225, v225\n\t" \
  "v_mfma_f32_16x16x32_bf16 v[84:87], v[216:219], v[20:23], v[84:87]\n\t" \
  "ds_read_b64_tr_b16 v[210:211], v238 offset:22528\n\t" \
  "v_mul_f32 v224, v224, v168\n\t" \
  "v_mul_f32 v225, v225, v169\n\t" \
  "s_waitcnt lgkmcnt(6)\n\t" \
  "v_mfma_f32_16x16x32_bf16 v[88:91], v[220:223], v[0:3], 0\n\t" \
  "ds_read_b64_tr_b16 v[212:213], v239 offset:12288\n\t" \
  "v_cvt_pk_bf16_f32 v162, v224, v225\n\t" \
  "v_mul_f32 v224, v245, v170\n\t" \
  "v_mfma_f32_16x16x32_bf16 v[88:91], v[192:195], v[8:11], v[88:91]\n\t" \
  "ds_read_b64_tr_b16 v[214:215], v239 offset:14336\n\t" \
  "v_mul_f32 v225, v245, v171\n\t" \
  "v_fmaak_f32 v226, v224, v224, 0x409d30fc\n\t" \
  "v_mfma_f32_16x16x32_bf16 v[88:91], v[196:199], v[16:19], v[88:91]\n\t" \
  "ds_read_b64_tr_b16 v[216:217], v239 offset:16384\n\t" \
  "v_fmaak_f32 v227, v225, v225, 0x409d30fc\n\t" \
  "v_mul_f32 v224, v224, v226\n\t" \
  "v_mul_f32 v225, v225, v227\n\t" \
  "v_mfma_f32_16x16x32_bf16 v[92:95], v[220:223], v[4:7], 0\n\t" \
  "ds_read_b64_tr_b16 v[218:219], v239 offset:18432\n\t" \
  "ds_read_b128 v[24:27], v229 offset:24576\n\t" \
  "v_exp_f32 v224, v224\n\t" \
  "v_mfma_f32_16x16x32_bf16 v[92:95], v[192:195], v[12:15], v[92:95]\n\t" \
  "ds_read_b64_tr_b16 v[220:221], v239 offset:20480\n\t" \
  "v_exp_f32 v225, v225\n\t" \
  "v_mfma_f32_16x16x32_bf16 v[92:95], v[196:199], v[20:23], v[92:95]\n\t" \
  "ds_read_b64_tr_b16 v[222:223], v239 offset:22528\n\t" \
  "v_add_f32 v224, 1.0, v224\n\t" \
  "v_add_f32 v225, 1.0, v225\n\t" \
  "s_waitcnt lgkmcnt(7)\n\t" \
  "v_mfma_f32_16x16x32_bf16 v[96:99], v[200:203], v[0:3], 0\n\t" \
  "ds_read_b64_tr_b16 v[192:193], v236 offset:36864\n\t" \
  "v_rcp_f32 v224, v224\n\t" \
  "v_mfma_f32_16x16x32_bf16 v[96:99], v[204:207], v[8:11], v[96:99]\n\t" \
  "ds_read_b64_tr_b16 v[194:195], v236 offset:38912\n\t" \
  "v_rcp_f32 v225, v225\n\t" \
  "v_mfma_f32_16x16x32_bf16 v[96:99], v[208:211], v[16:19], v[96:99]\n\t" \
  "ds_read_b64_tr_b16 v[196:197], v236 offset:40960\n\t" \
  "ds_read_b128 v[28:31], v229 offset:25600\n\t" \
  "v_mul_f32 v224, v224, v170\n\t" \
  "v_mul_f32 v225, v225, v171\n\t" \
  "v_mfma_f32_16x16x32_bf16 v[100:103], v[200:203], v[4:7], 0\n\t" \
  "ds_read_b64_tr_b16 v[198:199], v236 offset:43008\n\t" \
  "v_cvt_pk_bf16_f32 v163, v224, v225\n\t" \
  "s_add_u32 s75, s71, 12288\n\t" \
  "buffer_store_dwordx4 v[160:163], v244, s[48:51], s75 offen offset:2048 sc1\n\t" \
  "v_mfma_f32_16x16x32_bf16 v[100:103], v[204:207], v[12:15], v[100:103]\n\t" \
  "ds_read_b64_tr_b16 v[200:201], v236 offset:45056\n\t" \
  "v_mul_f32 v224, v246, v164\n\t" \
  "v_mul_f32 v225, v246, v165\n\t" \
  "v_fmaak_f32 v226, v224, v224, 0x409d30fc\n\t" \
  "v_mfma_f32_16x16x32_bf16 v[100:103], v[208:211], v[20:23], v[100:103]\n\t" \
  "ds_read_b64_tr_b16 v[202:203], v236 offset:47104\n\t" \
  "v_fmaak_f32 v227, v225, v225, 0x409d30fc\n\t" \
  "v_mul_f32 v224, v224, v226\n\t" \
  "s_waitcnt lgkmcnt(7)\n\t" \
  "v_mfma_f32_16x16x32_bf16 v[104:107], v[212:215], v[0:3], 0\n\t" \
  "ds_read_b64_tr_b16 v[204:205], v237 offset:36864\n\t" \
  "v_mul_f32 v225, v225, v227\n\t" \
  "v_exp_f32 v224, v224\n\t" \
  "v_mfma_f32_16x16x32_bf16 v[104:107], v[216:219], v[8:11], v[104:107]\n\t" \
  "ds_read_b64_tr_b16 v[206:207], v237 offset:38912\n\t" \
  "ds_read_b128 v[32:35], v229 offset:26624\n\t" \
  "v_exp_f32 v225, v225\n\t" \
  "v_mfma_f32_16x16x32_bf16 v[104:107], v[220:223], v[16:19], v[104:107]\n\t" \
  "ds_read_b64_tr_b16 v[208:209], v237 offset:40960\n\t" \
  "v_add_f32 v224, 1.0, v224\n\t" \
  "s_waitcnt vmcnt(3)\n\t" \
  "s_barrier\n\t" \
  "v_mfma_f32_16x16x32_bf16 v[108:111], v[212:215], v[4:7], 0\n\t" \
  "ds_read_b64_tr_b16 v[210:211], v237 offset:43008\n\t" \
  "v_add_f32 v225, 1.0, v225\n\t" \
  "v_rcp_f32 v224, v224\n\t" \
  "v_mfma_f32_16x16x32_bf16 v[108:111], v[216:219], v[12:15], v[108:111]\n\t" \
  "ds_read_b64_tr_b16 v[212:213], v237 offset:45056\n\t" \
  "v_rcp_f32 v225, v225\n\t" \
  "v_mfma_f32_16x16x32_bf16 v[108:111], v[220:223], v[20:23], v[108:111]\n\t" \
  "ds_read_b64_tr_b16 v[214:215], v237 offset:47104\n\t" \
  "v_mul_f32 v224, v224, v164\n\t" \
  "v_mul_f32 v225, v225, v165\n\t" \
  "s_waitcnt lgkmcnt(7)\n\t" \
  "v_mfma_f32_16x16x32_bf16 v[112:115], v[192:195], v[0:3], 0\n\t" \
  "ds_read_b64_tr_b16 v[216:217], v238 offset:36864\n\t" \
  "ds_read_b128 v[36:39], v229 offset:49152\n\t" \
  "v_cvt_pk_bf16_f32 v164, v224, v225\n\t" \
  "v_mul_f32 v224, v246, v166\n\t" \
  "v_mfma_f32_16x16x32_bf16 v[112:115], v[196:199], v[8:11], v[112:115]\n\t" \
  "ds_read_b64_tr_b16 v[218:219], v238 offset:38912\n\t" \
  "v_mul_f32 v225, v246, v167\n\t" \
  "v_fmaak_f32 v226, v224, v224, 0x409d30fc\n\t" \
  "v_mfma_f32_16x16x32_bf16 v[112:115], v[200:203], v[16:19], v[112:115]\n\t" \
  "ds_read_b64_tr_b16 v[220:221], v238 offset:40960\n\t" \
  "v_fmaak_f32 v227, v225, v225, 0x409d30fc\n\t" \
  "v_mul_f32 v224, v224, v226\n\t" \
  "v_mfma_f32_16x16x32_bf16 v[116:119], v[192:195], v[4:7], 0\n\t" \
  "ds_read_b64_tr_b16 v[222:223], v238 offset:43008\n\t" \
  "v_mul_f32 v225, v225, v227\n\t" \
  "v_exp_f32 v224, v224\n\t" \
  "v_mfma_f32_16x16x32_bf16 v[116:119], v[196:199], v[12:15], v[116:119]\n\t" \
  "ds_read_b64_tr_b16 v[192:193], v238 offset:45056\n\t" \
  "v_exp_f32 v225, v225\n\t" \
  "v_mfma_f32_16x16x32_bf16 v[116:119], v[200:203], v[20:23], v[116:119]\n\t" \
  "ds_read_b64_tr_b16 v[194:195], v238 offset:47104\n\t" \
  "ds_read_b128 v[40:43], v229 offset:50176\n\t" \
  "v_add_f32 v224, 1.0, v224\n\t" \
  "s_waitcnt lgkmcnt(8)\n\t" \
  "v_mfma_f32_16x16x32_bf16 v[120:123], v[204:207], v[0:3], 0\n\t" \
  "ds_read_b64_tr_b16 v[196:197], v239 offset:36864\n\t" \
  "v_add_f32 v225, 1.0, v225\n\t" \
  "v_rcp_f32 v224, v224\n\t" \
  "v_mfma_f32_16x16x32_bf16 v[120:123], v[208:211], v[8:11], v[120:123]\n\t" \
  "ds_read_b64_tr_b16 v[198:199], v239 offset:38912\n\t" \
  "v_rcp_f32 v225, v225\n\t" \
  "v_mfma_f32_16x16x32_bf16 v[120:123], v[212:215], v[16:19], v[120:123]\n\t" \
  "ds_read_b64_tr_b16 v[200:201], v239 offset:40960\n\t" \
  "v_mul_f32 v224, v224, v166\n\t" \
  "v_mul_f32 v225, v225, v167\n\t" \
  "v_mfma_f32_16x16x32_bf16 v[124:127], v[204:207], v[4:7], 0\n\t" \
  "ds_read_b64_tr_b16 v[202:203], v239 offset:43008\n\t" \
  "v_cvt_pk_bf16_f32 v165, v224, v225\n\t" \
  "v_mul_f32 v224, v246, v172\n\t" \
  "v_mfma_f32_16x16x32_bf16 v[124:127], v[208:211], v[12:15], v[124:127]\n\t" \
  "ds_read_b64_tr_b16 v[204:205], v239 offset:45056\n\t" \
  "ds_read_b128 v[44:47], v229 offset:51200\n\t" \
  "v_mul_f32 v225, v246, v173\n\t" \
  "v_fmaak_f32 v226, v224, v224, 0x409d30fc\n\t" \
  "v_mfma_f32_16x16x32_bf16 v[124:127], v[212:215], v[20:23], v[124:127]\n\t" \
  "ds_read_b64_tr_b16 v[206:207], v239 offset:47104\n\t" \
  "v_fmaak_f32 v227, v225, v225, 0x409d30fc\n\t" \
  "v_mul_f32 v224, v224, v226\n\t" \
  "s_waitcnt lgkmcnt(8)\n\t" \
  "v_mfma_f32_16x16x32_bf16 v[128:131], v[216:219], v[0:3], 0\n\t" \
  "ds_read_b64_tr_b16 v[208:209], v232 offset:0\n\t" \
  "v_mul_f32 v225, v225, v227\n\t" \
  "v_exp_f32 v224, v224\n\t" \
  "v_mfma_f32_16x16x32_bf16 v[128:131], v[220:223], v[8:11], v[128:131]\n\t" \
  "ds_read_b64_tr_b16 v[210:211], v232 offset:2048\n\t" \
  "v_exp_f32 v225, v225\n\t" \
  "v_mfma_f32_16x16x32_bf16 v[128:131], v[192:195], v[16:19], v[128:131]\n\t" \
  "ds_read_b64_tr_b16 v[212:213], v232 offset:4096\n\t" \
  "v_add_f32 v224, 1.0, v224\n\t" \
  "v_add_f32 v225, 1.0, v225\n\t" \
  "v_mfma_f32_16x16x32_bf16 v[132:135], v[216:219], v[4:7], 0\n\t" \
  "ds_read_b64_tr_b16 v[214:215], v232 offset:6144\n\t" \
  "v_rcp_f32 v224, v224\n\t" \
  "v_mfma_f32_16x16x32_bf16 v[132:135], v[220:223], v[12:15], v[132:135]\n\t" \
  "ds_read_b64_tr_b16 v[216:217], v232 offset:8192\n\t" \
  "v_rcp_f32 v225, v225\n\t" \
  "v_mfma_f32_16x16x32_bf16 v[132:135], v[192:195], v[20:23], v[132:135]\n\t" \
  "ds_read_b64_tr_b16 v[218:219], v232 offset:10240\n\t" \
  "v_mul_f32 v224, v224, v172\n\t" \
  "v_mul_f32 v225, v225, v173\n\t" \
  "s_waitcnt lgkmcnt(6)\n\t" \
  "v_mfma_f32_16x16x32_bf16 v[136:139], v[196:199], v[0:3], 0\n\t" \
  "ds_read_b64_tr_b16 v[220:221], v233 offset:0\n\t" \
  "v_cvt_pk_bf16_f32 v166, v224, v225\n\t" \
  "v_mul_f32 v224, v246, v174\n\t" \
  "v_mfma_f32_16x16x32_bf16 v[136:139], v[200:203], v[8:11], v[136:139]\n\t" \
  "ds_read_b64_tr_b16 v[222:223], v233 offset:2048\n\t" \
  "v_mul_f32 v225, v246, v175\n\t" \
  "v_fmaak_f32 v226, v224, v224, 0x409d30fc\n\t" \
  "v_mfma_f32_16x16x32_bf16 v[136:139], v[204:207], v[16:19], v[136:139]\n\t" \
  "ds_read_b64_tr_b16 v[192:193], v233 offset:4096\n\t" \
  "v_fmaak_f32 v227, v225, v225, 0x409d30fc\n\t" \
  "v_mul_f32 v224, v224, v226\n\t" \
  "v_mul_f32 v225, v225, v227\n\t" \
  "v_mfma_f32_16x16x32_bf16 v[140:143], v[196:199], v[4:7], 0\n\t" \
  "ds_read_b64_tr_b16 v[194:195], v233 offset:6144\n\t" \
  "v_exp_f32 v224, v224\n\t" \
  "v_mfma_f32_16x16x32_bf16 v[140:143], v[200:203], v[12:15], v[140:143]\n\t" \
  "ds_read_b64_tr_b16 v[196:197], v233 offset:8192\n\t" \
  "v_exp_f32 v225, v225\n\t" \
  "v_mfma_f32_16x16x32_bf16 v[140:143], v[204:207], v[20:23], v[140:143]\n\t" \
  "ds_read_b64_tr_b16 v[198:199], v233 offset:10240\n\t" \
  "v_add_f32 v224, 1.0, v224\n\t" \
  "v_add_f32 v225, 1.0, v225\n\t" \
  "s_waitcnt lgkmcnt(6)\n\t" \
  "v_mfma_f32_16x16x32_bf16 v[80:83], v[208:211], v[24:27], v[80:83]\n\t" \
  "ds_read_b64_tr_b16 v[200:201], v234 offset:0\n\t" \
  "v_rcp_f32 v224, v224\n\t" \
  "v_mfma_f32_16x16x32_bf16 v[80:83], v[212:215], v[32:35], v[80:83]\n\t" \
  "ds_read_b64_tr_b16 v[202:203], v234 offset:2048\n\t" \
  "v_rcp_f32 v225, v225\n\t" \
  "v_mfma_f32_16x16x32_bf16 v[80:83], v[216:219], v[40:43], v[80:83]\n\t" \
  "ds_read_b64_tr_b16 v[204:205], v234 offset:4096\n\t" \
  "v_mul_f32 v224, v224, v174\n\t" \
  "v_mul_f32 v225, v225, v175\n\t" \
  "v_mfma_f32_16x16x32_bf16 v[84:87], v[208:211], v[28:31], v[84:87]\n\t" \
  "ds_read_b64_tr_b16 v[206:207], v234 offset:6144\n\t" \
  "v_cvt_pk_bf16_f32 v167, v224, v225\n\t" \
  "s_add_u32 s75, s71, 12288\n\t" \
  "buffer_store_dwordx4 v[164:167], v244, s[48:51], s75 offen offset:3072 sc1\n\t" \
  "v_mfma_f32_16x16x32_bf16 v[84:87], v[212:215], v[36:39], v[84:87]\n\t" \
  "ds_read_b64_tr_b16 v[208:209], v234 offset:8192\n\t" \
  "v_mul_f32 v224, v245, v176\n\t" \
  "v_mul_f32 v225, v245, v177\n\t" \
  "v_fmaak_f32 v226, v224, v224, 0x409d30fc\n\t" \
  "v_mfma_f32_16x16x32_bf16 v[84:87], v[216:219], v[44:47], v[84:87]\n\t" \
  "ds_read_b64_tr_b16 v[210:211], v234 offset:10240\n\t" \
  "v_fmaak_f32 v227, v225, v225, 0x409d30fc\n\t" \
  "v_mul_f32 v224, v224, v226\n\t" \
  "s_waitcnt lgkmcnt(6)\n\t" \
  "v_mfma_f32_16x16x32_bf16 v[88:91], v[220:223], v[24:27], v[88:91]\n\t" \
  "ds_read_b64_tr_b16 v[212:213], v235 offset:0\n\t" \
  "v_mul_f32 v225, v225, v227\n\t" \
  "v_exp_f32 v224, v224\n\t" \
  "v_mfma_f32_16x16x32_bf16 v[88:91], v[192:195], v[32:35], v[88:91]\n\t" \
  "ds_read_b64_tr_b16 v[214:215], v235 offset:2048\n\t" \
  "v_exp_f32 v225, v225\n\t" \
  "v_mfma_f32_16x16x32_bf16 v[88:91], v[196:199], v[40:43], v[88:91]\n\t" \
  "ds_read_b64_tr_b16 v[216:217], v235 offset:4096\n\t" \
  "v_add_f32 v224, 1.0, v224\n\t" \
  "s_barrier\n\t" \
  "v_mfma_f32_16x16x32_bf16 v[92:95], v[220:223], v[28:31], v[92:95]\n\t" \
  "ds_read_b64_tr_b16 v[218:219], v235 offset:6144\n\t" \
  "ds_read_b128 v[0:3], v230 offset:0\n\t" \
  "v_add_f32 v225, 1.0, v225\n\t" \
  "v_rcp_f32 v224, v224\n\t" \
  "v_mfma_f32_16x16x32_bf16 v[92:95], v[192:195], v[36:39], v[92:95]\n\t" \
  "ds_read_b64_tr_b16 v[220:221], v235 offset:8192\n\t" \
  "v_rcp_f32 v225, v225\n\t" \
  "v_mfma_f32_16x16x32_bf16 v[92:95], v[196:199], v[44:47], v[92:95]\n\t" \
  "ds_read_b64_tr_b16 v[222:223], v235 offset:10240\n\t" \
  "v_mul_f32 v224, v224, v176\n\t" \
  "v_mul_f32 v225, v225, v177\n\t" \
  "s_waitcnt lgkmcnt(7)\n\t" \
  "v_mfma_f32_16x16x32_bf16 v[96:99], v[200:203], v[24:27], v[96:99]\n\t" \
  "ds_read_b64_tr_b16 v[192:193], v240 offset:12288\n\t" \
  "v_cvt_pk_bf16_f32 v176, v224, v225\n\t" \
  "v_mul_f32 v224, v245, v178\n\t" \
  "v_mfma_f32_16x16x32_bf16 v[96:99], v[204:207], v[32:35], v[96:99]\n\t" \
  "ds_read_b64_tr_b16 v[194:195], v240 offset:14336\n\t" \
  "v_mul_f32 v225, v245, v179\n\t" \
  "v_fmaak_f32 v226, v224, v224, 0x409d30fc\n\t" \
  "v_mfma_f32_16x16x32_bf16 v[96:99], v[208:211], v[40:43], v[96:99]\n\t" \
  "ds_read_b64_tr_b16 v[196:197], v240 offset:16384\n\t" \
  "ds_read_b128 v[4:7], v230 offset:1024\n\t" \
  "v_fmaak_f32 v227, v225, v225, 0x409d30fc\n\t" \
  "v_mul_f32 v224, v224, v226\n\t" \
  "v_mfma_f32_16x16x32_bf16 v[100:103], v[200:203], v[28:31], v[100:103]\n\t" \
  "ds_read_b64_tr_b16 v[198:199], v240 offset:18432\n\t" \
  "v_mul_f32 v225, v225, v227\n\t" \
  "v_exp_f32 v224, v224\n\t" \
  "v_mfma_f32_16x16x32_bf16 v[100:103], v[204:207], v[36:39], v[100:103]\n\t" \
  "ds_read_b64_tr_b16 v[200:201], v240 offset:20480\n\t" \
  "v_exp_f32 v225, v225\n\t" \
  "v_mfma_f32_16x16x32_bf16 v[100:103], v[208:211], v[44:47], v[100:103]\n\t" \
  "ds_read_b64_tr_b16 v[202:203], v240 offset:22528\n\t" \
  "v_add_f32 v224, 1.0, v224\n\t" \
  "s_waitcnt lgkmcnt(7)\n\t" \
  "v_mfma_f32_16x16x32_bf16 v[104:107], v[212:215], v[24:27], v[104:107]\n\t" \
  "ds_read_b64_tr_b16 v[204:205], v241 offset:12288\n\t" \
  "v_add_f32 v225, 1.0, v225\n\t" \
  "v_rcp_f32 v224, v224\n\t" \
  "v_mfma_f32_16x16x32_bf16 v[104:107], v[216:219], v[32:35], v[104:107]\n\t" \
  "ds_read_b64_tr_b16 v[206:207], v241 offset:14336\n\t" \
  "ds_read_b128 v[8:11], v230 offset:2048\n\t" \
  "v_rcp_f32 v225, v225\n\t" \
  "v_mfma_f32_16x16x32_bf16 v[104:107], v[220:223], v[40:43], v[104:107]\n\t" \
  "ds_read_b64_tr_b16 v[208:209], v241 offset:16384\n\t" \
  "v_mul_f32 v224, v224, v178\n\t" \
  "v_mul_f32 v225, v225, v179\n\t" \
  "v_mfma_f32_16x16x32_bf16 v[108:111], v[212:215], v[28:31], v[108:111]\n\t" \
  "ds_read_b64_tr_b16 v[210:211], v241 offset:18432\n\t" \
  "v_cvt_pk_bf16_f32 v177, v224, v225\n\t" \
  "v_mul_f32 v224, v245, v184\n\t" \
  "v_mfma_f32_16x16x32_bf16 v[108:111], v[216:219], v[36:39], v[108:111]\n\t" \
  "ds_read_b64_tr_b16 v[212:213], v241 offset:20480\n\t" \
  "v_mul_f32 v225, v245, v185\n\t" \
  "v_fmaak_f32 v226, v224, v224, 0x409d30fc\n\t" \
  "v_mfma_f32_16x16x32_bf16 v[108:111], v[220:223], v[44:47], v[108:111]\n\t" \
  "ds_read_b64_tr_b16 v[214:215], v241 offset:22528\n\t" \
  "v_fmaak_f32 v227, v225, v225, 0x409d30fc\n\t" \
  "v_mul_f32 v224, v224, v226\n\t" \
  "s_waitcnt lgkmcnt(7)\n\t" \
  "v_mfma_f32_16x16x32_bf16 v[112:115], v[192:195], v[24:27], v[112:115]\n\t" \
  "ds_read_b64_tr_b16 v[216:217], v242 offset:12288\n\t" \
  "ds_read_b128 v[12:15], v228 offset:12288\n\t" \
  "v_mul_f32 v225, v225, v227\n\t" \
  "v_exp_f32 v224, v224\n\t" \
  "v_mfma_f32_16x16x32_bf16 v[112:115], v[196:199], v[32:35], v[112:115]\n\t" \
  "ds_read_b64_tr_b16 v[218:219], v242 offset:14336\n\t" \
  "v_exp_f32 v225, v225\n\t" \
  "v_mfma_f32_16x16x32_bf16 v[112:115], v[200:203], v[40:43], v[112:115]\n\t" \
  "ds_read_b64_tr_b16 v[220:221], v242 offset:16384\n\t" \
  "v_add_f32 v224, 1.0, v224\n\t" \
  "v_add_f32 v225, 1.0, v225\n\t" \
  "v_mfma_f32_16x16x32_bf16 v[116:119], v[192:195], v[28:31], v[116:119]\n\t" \
  "ds_read_b64_tr_b16 v[222:223], v242 offset:18432\n\t" \
  "v_rcp_f32 v224, v224\n\t" \
  "v_mfma_f32_16x16x32_bf16 v[116:119], v[196:199], v[36:39], v[116:119]\n\t" \
  "ds_read_b64_tr_b16 v[192:193], v242 offset:20480\n\t" \
  "v_rcp_f32 v225, v225\n\t" \
  "v_mfma_f32_16x16x32_bf16 v[116:119], v[200:203], v[44:47], v[116:119]\n\t" \
  "ds_read_b64_tr_b16 v[194:195], v242 offset:22528\n\t" \
  "ds_read_b128 v[16:19], v228 offset:13312\n\t" \
  "v_mul_f32 v224, v224, v184\n\t" \
  "v_mul_f32 v225, v225, v185\n\t" \
  "s_waitcnt lgkmcnt(8)\n\t" \
  "v_mfma_f32_16x16x32_bf16 v[120:123], v[204:207], v[24:27], v[120:123]\n\t" \
  "ds_read_b64_tr_b16 v[196:197], v243 offset:12288\n\t" \
  "v_cvt_pk_bf16_f32 v178, v224, v225\n\t" \
  "v_mul_f32 v224, v245, v186\n\t" \
  "v_mfma_f32_16x16x32_bf16 v[120:123], v[208:211], v[32:35], v[120:123]\n\t" \
  "ds_read_b64_tr_b16 v[198:199], v243 offset:14336\n\t" \
  "v_mul_f32 v225, v245, v187\n\t" \
  "v_fmaak_f32 v226, v224, v224, 0x409d30fc\n\t" \
  "v_mfma_f32_16x16x32_bf16 v[120:123], v[212:215], v[40:43], v[120:123]\n\t" \
  "ds_read_b64_tr_b16 v[200:201], v243 offset:16384\n\t" \
  "v_fmaak_f32 v227, v225, v225, 0x409d30fc\n\t" \
  "v_mul_f32 v224, v224, v226\n\t" \
  "v_mul_f32 v225, v225, v227\n\t" \
  "v_mfma_f32_16x16x32_bf16 v[124:127], v[204:207], v[28:31], v[124:127]\n\t" \
  "ds_read_b64_tr_b16 v[202:203], v243 offset:18432\n\t" \
  "v_exp_f32 v224, v224\n\t" \
  "v_mfma_f32_16x16x32_bf16 v[124:127], v[208:211], v[36:39], v[124:127]\n\t" \
  "ds_read_b64_tr_b16 v[204:205], v243 offset:20480\n\t" \
  "ds_read_b128 v[20:23], v228 offset:14336\n\t" \
  "v_exp_f32 v225, v225\n\t" \
  "v_mfma_f32_16x16x32_bf16 v[124:127], v[212:215], v[44:47], v[124:127]\n\t" \
  "ds_read_b64_tr_b16 v[206:207], v243 offset:22528\n\t" \
  "v_add_f32 v224, 1.0, v224\n\t" \
  "v_add_f32 v225, 1.0, v225\n\t" \
  "s_waitcnt lgkmcnt(8)\n\t" \
  "v_mfma_f32_16x16x32_bf16 v[128:131], v[216:219], v[24:27], v[128:131]\n\t" \
  "ds_read_b64_tr_b16 v[208:209], v232 offset:36864\n\t" \
  "v_rcp_f32 v224, v224\n\t" \
  "v_mfma_f32_16x16x32_bf16 v[128:131], v[220:223], v[32:35], v[128:131]\n\t" \
  "ds_read_b64_tr_b16 v[210:211], v232 offset:38912\n\t" \
  "v_rcp_f32 v225, v225\n\t" \
  "v_mfma_f32_16x16x32_bf16 v[128:131], v[192:195], v[40:43], v[128:131]\n\t" \
  "ds_read_b64_tr_b16 v[212:213], v232 offset:40960\n\t" \
  "v_mul_f32 v224, v224, v186\n\t" \
  "v_mul_f32 v225, v225, v187\n\t" \
  "v_mfma_f32_16x16x32_bf16 v[132:135], v[216:219], v[28:31], v[132:135]\n\t" \
  "ds_read_b64_tr_b16 v[214:215], v232 offset:43008\n\t" \
  "v_cvt_pk_bf16_f32 v179, v224, v225\n\t" \
  "s_add_u32 s75, s71, 16384\n\t" \
  "buffer_store_dwordx4 v[176:179], v244, s[48:51], s75 offen offset:0 sc1\n\t" \
  "v_mfma_f32_16x16x32_bf16 v[132:135], v[220:223], v[36:39], v[132:135]\n\t" \
  "ds_read_b64_tr_b16 v[216:217], v232 offset:45056\n\t" \
  "v_mul_f32 v224, v246, v180\n\t" \
  "v_mul_f32 v225, v246, v181\n\t" \
  "v_fmaak_f32 v226, v224, v224, 0x409d30fc\n\t" \
  "v_mfma_f32_16x16x32_bf16 v[132:135], v[192:195], v[44:47], v[132:135]\n\t" \
  "ds_read_b64_tr_b16 v[218:219], v232 offset:47104\n\t" \
  "v_fmaak_f32 v227, v225, v225, 0x409d30fc\n\t" \
  "v_mul_f32 v224, v224, v226\n\t" \
  "s_waitcnt lgkmcnt(6)\n\t" \
  "v_mfma_f32_16x16x32_bf16 v[136:139], v[196:199], v[24:27], v[136:139]\n\t" \
  "ds_read_b64_tr_b16 v[220:221], v233 offset:36864\n\t" \
  "v_mul_f32 v225, v225, v227\n\t" \
  "v_exp_f32 v224, v224\n\t" \
  "v_mfma_f32_16x16x32_bf16 v[136:139], v[200:203], v[32:35], v[136:139]\n\t" \
  "ds_read_b64_tr_b16 v[222:223], v233 offset:38912\n\t" \
  "v_exp_f32 v225, v225\n\t" \
  "v_mfma_f32_16x16x32_bf16 v[136:139], v[204:207], v[40:43], v[136:139]\n\t" \
  "ds_read_b64_tr_b16 v[192:193], v233 offset:40960\n\t" \
  "v_add_f32 v224, 1.0, v224\n\t" \
  "s_waitcnt vmcnt(3)\n\t" \
  "s_barrier\n\t" \
  "v_mfma_f32_16x16x32_bf16 v[140:143], v[196:199], v[28:31], v[140:143]\n\t" \
  "ds_read_b64_tr_b16 v[194:195], v233 offset:43008\n\t" \
  "v_add_f32 v225, 1.0, v225\n\t" \
  "v_rcp_f32 v224, v224\n\t" \
  "v_mfma_f32_16x16x32_bf16 v[140:143], v[200:203], v[36:39], v[140:143]\n\t" \
  "ds_read_b64_tr_b16 v[196:197], v233 offset:45056\n\t" \
  "v_rcp_f32 v225, v225\n\t" \
  "v_mfma_f32_16x16x32_bf16 v[140:143], v[204:207], v[44:47], v[140:143]\n\t" \
  "ds_read_b64_tr_b16 v[198:199], v233 offset:47104\n\t" \
  "v_mul_f32 v224, v224, v180\n\t" \
  "v_mul_f32 v225, v225, v181\n\t" \
  "s_waitcnt lgkmcnt(6)\n\t" \
  "v_mfma_f32_16x16x32_bf16 v[80:83], v[208:211], v[0:3], v[80:83]\n\t" \
  "ds_read_b64_tr_b16 v[200:201], v234 offset:36864\n\t" \
  "v_cvt_pk_bf16_f32 v180, v224, v225\n\t" \
  "v_mul_f32 v224, v246, v182\n\t" \
  "v_mfma_f32_16x16x32_bf16 v[80:83], v[212:215], v[8:11], v[80:83]\n\t" \
  "ds_read_b64_tr_b16 v[202:203], v234 offset:38912\n\t" \
  "v_mul_f32 v225, v246, v183\n\t" \
  "v_fmaak_f32 v226, v224, v224, 0x409d30fc\n\t" \
  "v_mfma_f32_16x16x32_bf16 v[80:83], v[216:219], v[16:19], v[80:83]\n\t" \
  "ds_read_b64_tr_b16 v[204:205], v234 offset:40960\n\t" \
  "v_fmaak_f32 v227, v225, v225, 0x409d30fc\n\t" \
  "v_mul_f32 v224, v224, v226\n\t" \
  "v_mfma_f32_16x16x32_bf16 v[84:87], v[208:211], v[4:7], v[84:87]\n\t" \
  "ds_read_b64_tr_b16 v[206:207], v234 offset:43008\n\t" \
  "v_mul_f32 v225, v225, v227\n\t" \
  "v_exp_f32 v224, v224\n\t" \
  "v_mfma_f32_16x16x32_bf16 v[84:87], v[212:215], v[12:15], v[84:87]\n\t" \
  "ds_read_b64_tr_b16 v[208:209], v234 offset:45056\n\t" \
  "v_exp_f32 v225, v225\n\t" \
  "v_mfma_f32_16x16x32_bf16 v[84:87], v[216:219], v[20:23], v[84:87]\n\t" \
  "ds_read_b64_tr_b16 v[210:211], v234 offset:47104\n\t" \
  "v_add_f32 v224, 1.0, v224\n\t" \
  "s_waitcnt lgkmcnt(6)\n\t" \
  "v_mfma_f32_16x16x32_bf16 v[88:91], v[220:223], v[0:3], v[88:91]\n\t" \
  "ds_read_b64_tr_b16 v[212:213], v235 offset:36864\n\t" \
  "v_add_f32 v225, 1.0, v225\n\t" \
  "v_rcp_f32 v224, v224\n\t" \
  "v_mfma_f32_16x16x32_bf16 v[88:91], v[192:195], v[8:11], v[88:91]\n\t" \
  "ds_read_b64_tr_b16 v[214:215], v235 offset:38912\n\t" \
  "v_rcp_f32 v225, v225\n\t" \
  "v_mfma_f32_16x16x32_bf16 v[88:91], v[196:199], v[16:19], v[88:91]\n\t" \
  "ds_read_b64_tr_b16 v[216:217], v235 offset:40960\n\t" \
  "v_mul_f32 v224, v224, v182\n\t" \
  "v_mul_f32 v225, v225, v183\n\t" \
  "v_mfma_f32_16x16x32_bf16 v[92:95], v[220:223], v[4:7], v[92:95]\n\t" \
  "ds_read_b64_tr_b16 v[218:219], v235 offset:43008\n\t" \
  "ds_read_b128 v[24:27], v230 offset:24576\n\t" \
  "v_cvt_pk_bf16_f32 v181, v224, v225\n\t" \
  "v_mul_f32 v224, v246, v188\n\t" \
  "v_mfma_f32_16x16x32_bf16 v[92:95], v[192:195], v[12:15], v[92:95]\n\t" \
  "ds_read_b64_tr_b16 v[220:221], v235 offset:45056\n\t" \
  "v_mul_f32 v225, v246, v189\n\t" \
  "v_fmaak_f32 v226, v224, v224, 0x409d30fc\n\t" \
  "v_mfma_f32_16x16x32_bf16 v[92:95], v[196:199], v[20:23], v[92:95]\n\t" \
  "ds_read_b64_tr_b16 v[222:223], v235 offset:47104\n\t" \
  "v_fmaak_f32 v227, v225, v225, 0x409d30fc\n\t" \
  "v_mul_f32 v224, v224, v226\n\t" \
  "s_waitcnt lgkmcnt(7)\n\t" \
  "v_mfma_f32_16x16x32_bf16 v[96:99], v[200:203], v[0:3], v[96:99]\n\t" \
  "ds_read_b64_tr_b16 v[192:193], v232 offset:24576\n\t" \
  "v_mul_f32 v225, v225, v227\n\t" \
  "v_exp_f32 v224, v224\n\t" \
  "v_mfma_f32_16x16x32_bf16 v[96:99], v[204:207], v[8:11], v[96:99]\n\t" \
  "ds_read_b64_tr_b16 v[194:195], v232 offset:26624\n\t" \
  "v_exp_f32 v225, v225\n\t" \
  "v_mfma_f32_16x16x32_bf16 v[96:99], v[208:211], v[16:19], v[96:99]\n\t" \
  "ds_read_b64_tr_b16 v[196:197], v232 offset:28672\n\t" \
  "ds_read_b128 v[28:31], v230 offset:25600\n\t" \
  "v_add_f32 v224, 1.0, v224\n\t" \
  "v_add_f32 v225, 1.0, v225\n\t" \
  "v_mfma_f32_16x16x32_bf16 v[100:103], v[200:203], v[4:7], v[100:103]\n\t" \
  "ds_read_b64_tr_b16 v[198:199], v232 offset:30720\n\t" \
  "v_rcp_f32 v224, v224\n\t" \
  "v_mfma_f32_16x16x32_bf16 v[100:103], v[204:207], v[12:15], v[100:103]\n\t" \
  "ds_read_b64_tr_b16 v[200:201], v232 offset:32768\n\t" \
  "v_rcp_f32 v225, v225\n\t" \
  "v_mfma_f32_16x16x32_bf16 v[100:103], v[208:211], v[20:23], v[100:103]\n\t" \
  "ds_read_b64_tr_b16 v[202:203], v232 offset:34816\n\t" \
  "v_mul_f32 v224, v224, v188\n\t" \
  "v_mul_f32 v225, v225, v189\n\t" \
  "s_waitcnt lgkmcnt(7)\n\t" \
  "v_mfma_f32_16x16x32_bf16 v[104:107], v[212:215], v[0:3], v[104:107]\n\t" \
  "ds_read_b64_tr_b16 v[204:205], v233 offset:24576\n\t" \
  "v_cvt_pk_bf16_f32 v182, v224, v225\n\t" \
  "v_mul_f32 v224, v246, v190\n\t" \
  "v_mfma_f32_16x16x32_bf16 v[104:107], v[216:219], v[8:11], v[104:107]\n\t" \
  "ds_read_b64_tr_b16 v[206:207], v233 offset:26624\n\t" \
  "ds_read_b128 v[32:35], v230 offset:26624\n\t" \
  "v_mul_f32 v225, v246, v191\n\t" \
  "v_fmaak_f32 v226, v224, v224, 0x409d30fc\n\t" \
  "v_mfma_f32_16x16x32_bf16 v[104:107], v[220:223], v[16:19], v[104:107]\n\t" \
  "ds_read_b64_tr_b16 v[208:209], v233 offset:28672\n\t" \
  "v_fmaak_f32 v227, v225, v225, 0x409d30fc\n\t" \
  "v_mul_f32 v224, v224, v226\n\t" \
  "v_mul_f32 v225, v225, v227\n\t" \
  "v_mfma_f32_16x16x32_bf16 v[108:111], v[212:215], v[4:7], v[108:111]\n\t" \
  "ds_read_b64_tr_b16 v[210:211], v233 offset:30720\n\t" \
  "v_exp_f32 v224, v224\n\t" \
  "v_mfma_f32_16x16x32_bf16 v[108:111], v[216:219], v[12:15], v[108:111]\n\t" \
  "ds_read_b64_tr_b16 v[212:213], v233 offset:32768\n\t" \
  "v_exp_f32 v225, v225\n\t" \
  "v_mfma_f32_16x16x32_bf16 v[108:111], v[220:223], v[20:23], v[108:111]\n\t" \
  "ds_read_b64_tr_b16 v[214:215], v233 offset:34816\n\t" \
  "v_add_f32 v224, 1.0, v224\n\t" \
  "v_add_f32 v225, 1.0, v225\n\t" \
  "s_waitcnt lgkmcnt(7)\n\t" \
  "v_mfma_f32_16x16x32_bf16 v[112:115], v[192:195], v[0:3], v[112:115]\n\t" \
  "ds_read_b64_tr_b16 v[216:217], v234 offset:24576\n\t" \
  "ds_read_b128 v[36:39], v228 offset:49152\n\t" \
  "v_rcp_f32 v224, v224\n\t" \
  "v_mfma_f32_16x16x32_bf16 v[112:115], v[196:199], v[8:11], v[112:115]\n\t" \
  "ds_read_b64_tr_b16 v[218:219], v234 offset:26624\n\t" \
  "v_rcp_f32 v225, v225\n\t" \
  "v_mfma_f32_16x16x32_bf16 v[112:115], v[200:203], v[16:19], v[112:115]\n\t" \
  "ds_read_b64_tr_b16 v[220:221], v234 offset:28672\n\t" \
  "v_mul_f32 v224, v224, v190\n\t" \
  "v_mul_f32 v225, v225, v191\n\t" \
  "v_mfma_f32_16x16x32_bf16 v[116:119], v[192:195], v[4:7], v[116:119]\n\t" \
  "ds_read_b64_tr_b16 v[222:223], v234 offset:30720\n\t" \
  "v_cvt_pk_bf16_f32 v183, v224, v225\n\t" \
  "s_add_u32 s75, s71, 16384\n\t" \
  "buffer_store_dwordx4 v[180:183], v244, s[48:51], s75 offen offset:1024 sc1\n\t" \
  "v_mfma_f32_16x16x32_bf16 v[116:119], v[196:199], v[12:15], v[116:119]\n\t" \
  "ds_read_b64_tr_b16 v[192:193], v234 offset:32768\n\t" \
  "v_mfma_f32_16x16x32_bf16 v[116:119], v[200:203], v[20:23], v[116:119]\n\t" \
  "ds_read_b64_tr_b16 v[194:195], v234 offset:34816\n\t" \
  "ds_read_b128 v[40:43], v228 offset:50176\n\t" \
  "s_waitcnt lgkmcnt(8)\n\t" \
  "v_mfma_f32_16x16x32_bf16 v[120:123], v[204:207], v[0:3], v[120:123]\n\t" \
  "ds_read_b64_tr_b16 v[196:197], v235 offset:24576\n\t" \
  "v_mfma_f32_16x16x32_bf16 v[120:123], v[208:211], v[8:11], v[120:123]\n\t" \
  "ds_read_b64_tr_b16 v[198:199], v235 offset:26624\n\t" \
  "v_mfma_f32_16x16x32_bf16 v[120:123], v[212:215], v[16:19], v[120:123]\n\t" \
  "ds_read_b64_tr_b16 v[200:201], v235 offset:28672\n\t" \
  "s_barrier\n\t" \
  "v_mfma_f32_16x16x32_bf16 v[124:127], v[204:207], v[4:7], v[124:127]\n\t" \
  "ds_read_b64_tr_b16 v[202:203], v235 offset:30720\n\t" \
  "v_mfma_f32_16x16x32_bf16 v[124:127], v[208:211], v[12:15], v[124:127]\n\t" \
  "ds_read_b64_tr_b16 v[204:205], v235 offset:32768\n\t" \
  "ds_read_b128 v[44:47], v228 offset:51200\n\t" \
  "v_mfma_f32_16x16x32_bf16 v[124:127], v[212:215], v[20:23], v[124:127]\n\t" \
  "ds_read_b64_tr_b16 v[206:207], v235 offset:34816\n\t" \
  "s_waitcnt lgkmcnt(8)\n\t" \
  "v_mfma_f32_16x16x32_bf16 v[128:131], v[216:219], v[0:3], v[128:131]\n\t" \
  "ds_read_b64_tr_b16 v[208:209], v236 offset:0\n\t" \
  "v_mfma_f32_16x16x32_bf16 v[128:131], v[220:223], v[8:11], v[128:131]\n\t" \
  "ds_read_b64_tr_b16 v[210:211], v236 offset:2048\n\t" \
  "v_mfma_f32_16x16x32_bf16 v[128:131], v[192:195], v[16:19], v[128:131]\n\t" \
  "ds_read_b64_tr_b16 v[212:213], v236 offset:4096\n\t" \
  "v_mfma_f32_16x16x32_bf16 v[132:135], v[216:219], v[4:7], v[132:135]\n\t" \
  "ds_read_b64_tr_b16 v[214:215], v236 offset:6144\n\t" \
  "v_mfma_f32_16x16x32_bf16 v[132:135], v[220:223], v[12:15], v[132:135]\n\t" \
  "ds_read_b64_tr_b16 v[216:217], v236 offset:8192\n\t" \
  "v_mfma_f32_16x16x32_bf16 v[132:135], v[192:195], v[20:23], v[132:135]\n\t" \
  "ds_read_b64_tr_b16 v[218:219], v236 offset:10240\n\t" \
  "s_waitcnt lgkmcnt(6)\n\t" \
  "v_mfma_f32_16x16x32_bf16 v[136:139], v[196:199], v[0:3], v[136:139]\n\t" \
  "ds_read_b64_tr_b16 v[220:221], v237 offset:0\n\t" \
  "v_mfma_f32_16x16x32_bf16 v[136:139], v[200:203], v[8:11], v[136:139]\n\t" \
  "ds_read_b64_tr_b16 v[222:223], v237 offset:2048\n\t" \
  "v_mfma_f32_16x16x32_bf16 v[136:139], v[204:207], v[16:19], v[136:139]\n\t" \
  "ds_read_b64_tr_b16 v[192:193], v237 offset:4096\n\t" \
  "v_mfma_f32_16x16x32_bf16 v[140:143], v[196:199], v[4:7], v[140:143]\n\t" \
  "ds_read_b64_tr_b16 v[194:195], v237 offset:6144\n\t" \
  "v_mfma_f32_16x16x32_bf16 v[140:143], v[200:203], v[12:15], v[140:143]\n\t" \
  "ds_read_b64_tr_b16 v[196:197], v237 offset:8192\n\t" \
  "v_mfma_f32_16x16x32_bf16 v[140:143], v[204:207], v[20:23], v[140:143]\n\t" \
  "ds_read_b64_tr_b16 v[198:199], v237 offset:10240\n\t" \
  "s_waitcnt lgkmcnt(6)\n\t" \
  "v_mfma_f32_16x16x32_bf16 v[48:51], v[208:211], v[24:27], 0\n\t" \
  "ds_read_b64_tr_b16 v[200:201], v238 offset:0\n\t" \
  "v_mfma_f32_16x16x32_bf16 v[48:51], v[212:215], v[32:35], v[48:51]\n\t" \
  "ds_read_b64_tr_b16 v[202:203], v238 offset:2048\n\t" \
  "v_mfma_f32_16x16x32_bf16 v[48:51], v[216:219], v[40:43], v[48:51]\n\t" \
  "ds_read_b64_tr_b16 v[204:205], v238 offset:4096\n\t" \
  "v_mfma_f32_16x16x32_bf16 v[52:55], v[208:211], v[28:31], 0\n\t" \
  "ds_read_b64_tr_b16 v[206:207], v238 offset:6144\n\t" \
  "v_mfma_f32_16x16x32_bf16 v[52:55], v[212:215], v[36:39], v[52:55]\n\t" \
  "ds_read_b64_tr_b16 v[208:209], v238 offset:8192\n\t" \
  "v_mfma_f32_16x16x32_bf16 v[52:55], v[216:219], v[44:47], v[52:55]\n\t" \
  "ds_read_b64_tr_b16 v[210:211], v238 offset:10240\n\t" \
  "s_waitcnt lgkmcnt(6)\n\t" \
  "v_mfma_f32_16x16x32_bf16 v[56:59], v[220:223], v[24:27], 0\n\t" \
  "ds_read_b64_tr_b16 v[212:213], v239 offset:0\n\t" \
  "v_mfma_f32_16x16x32_bf16 v[56:59], v[192:195], v[32:35], v[56:59]\n\t" \
  "ds_read_b64_tr_b16 v[214:215], v239 offset:2048\n\t" \
  "v_mfma_f32_16x16x32_bf16 v[56:59], v[196:199], v[40:43], v[56:59]\n\t" \
  "ds_read_b64_tr_b16 v[216:217], v239 offset:4096\n\t" \
  "v_mfma_f32_16x16x32_bf16 v[60:63], v[220:223], v[28:31], 0\n\t" \
  "ds_read_b64_tr_b16 v[218:219], v239 offset:6144\n\t" \
  "v_mfma_f32_16x16x32_bf16 v[60:63], v[192:195], v[36:39], v[60:63]\n\t" \
  "ds_read_b64_tr_b16 v[220:221], v239 offset:8192\n\t" \
  "v_mfma_f32_16x16x32_bf16 v[60:63], v[196:199], v[44:47], v[60:63]\n\t" \
  "ds_read_b64_tr_b16 v[222:223], v239 offset:10240\n\t" \
  "s_waitcnt lgkmcnt(6)\n\t" \
  "v_mfma_f32_16x16x32_bf16 v[64:67], v[200:203], v[24:27], 0\n\t" \
  "ds_read_b64_tr_b16 v[192:193], v236 offset:12288\n\t" \
  "v_mfma_f32_16x16x32_bf16 v[64:67], v[204:207], v[32:35], v[64:67]\n\t" \
  "ds_read_b64_tr_b16 v[194:195], v236 offset:14336\n\t" \
  "v_mfma_f32_16x16x32_bf16 v[64:67], v[208:211], v[40:43], v[64:67]\n\t" \
  "ds_read_b64_tr_b16 v[196:197], v236 offset:16384\n\t" \
  "v_mfma_f32_16x16x32_bf16 v[68:71], v[200:203], v[28:31], 0\n\t" \
  "ds_read_b64_tr_b16 v[198:199], v236 offset:18432\n\t" \
  "v_mfma_f32_16x16x32_bf16 v[68:71], v[204:207], v[36:39], v[68:71]\n\t" \
  "ds_read_b64_tr_b16 v[200:201], v236 offset:20480\n\t" \
  "v_mfma_f32_16x16x32_bf16 v[68:71], v[208:211], v[44:47], v[68:71]\n\t" \
  "ds_read_b64_tr_b16 v[202:203], v236 offset:22528\n\t" \
  "s_waitcnt lgkmcnt(6)\n\t" \
  "v_mfma_f32_16x16x32_bf16 v[72:75], v[212:215], v[24:27], 0\n\t" \
  "ds_read_b64_tr_b16 v[204:205], v237 offset:12288\n\t" \
  "v_mfma_f32_16x16x32_bf16 v[72:75], v[216:219], v[32:35], v[72:75]\n\t" \
  "ds_read_b64_tr_b16 v[206:207], v237 offset:14336\n\t" \
  "v_mfma_f32_16x16x32_bf16 v[72:75], v[220:223], v[40:43], v[72:75]\n\t" \
  "ds_read_b64_tr_b16 v[208:209], v237 offset:16384\n\t" \
  "s_waitcnt vmcnt(2)\n\t" \
  "s_barrier\n\t" \
  "v_mfma_f32_16x16x32_bf16 v[76:79], v[212:215], v[28:31], 0\n\t" \
  "ds_read_b64_tr_b16 v[210:211], v237 offset:18432\n\t" \
  "v_mfma_f32_16x16x32_bf16 v[76:79], v[216:219], v[36:39], v[76:79]\n\t" \
  "ds_read_b64_tr_b16 v[212:213], v237 offset:20480\n\t" \
  "v_mfma_f32_16x16x32_bf16 v[76:79], v[220:223], v[44:47], v[76:79]\n\t" \
  "ds_read_b64_tr_b16 v[214:215], v237 offset:22528\n\t" \
  "s_waitcnt lgkmcnt(6)\n\t" \
  "v_mfma_f32_16x16x32_bf16 v[80:83], v[192:195], v[24:27], v[80:83]\n\t" \
  "ds_read_b64_tr_b16 v[216:217], v238 offset:12288\n\t" \
  "v_mfma_f32_16x16x32_bf16 v[80:83], v[196:199], v[32:35], v[80:83]\n\t" \
  "ds_read_b64_tr_b16 v[218:219], v238 offset:14336\n\t" \
  "v_mfma_f32_16x16x32_bf16 v[80:83], v[200:203], v[40:43], v[80:83]\n\t" \
  "ds_read_b64_tr_b16 v[220:221], v238 offset:16384\n\t" \
  "v_mfma_f32_16x16x32_bf16 v[84:87], v[192:195], v[28:31], v[84:87]\n\t" \
  "ds_read_b64_tr_b16 v[222:223], v238 offset:18432\n\t" \
  "v_mfma_f32_16x16x32_bf16 v[84:87], v[196:199], v[36:39], v[84:87]\n\t" \
  "ds_read_b64_tr_b16 v[192:193], v238 offset:20480\n\t" \
  "v_mfma_f32_16x16x32_bf16 v[84:87], v[200:203], v[44:47], v[84:87]\n\t" \
  "ds_read_b64_tr_b16 v[194:195], v238 offset:22528\n\t" \
  "s_waitcnt lgkmcnt(6)\n\t" \
  "v_mfma_f32_16x16x32_bf16 v[88:91], v[204:207], v[24:27], v[88:91]\n\t" \
  "ds_read_b64_tr_b16 v[196:197], v239 offset:12288\n\t" \
  "v_mfma_f32_16x16x32_bf16 v[88:91], v[208:211], v[32:35], v[88:91]\n\t" \
  "ds_read_b64_tr_b16 v[198:199], v239 offset:14336\n\t" \
  "v_mfma_f32_16x16x32_bf16 v[88:91], v[212:215], v[40:43], v[88:91]\n\t" \
  "ds_read_b64_tr_b16 v[200:201], v239 offset:16384\n\t" \
  "v_mfma_f32_16x16x32_bf16 v[92:95], v[204:207], v[28:31], v[92:95]\n\t" \
  "ds_read_b64_tr_b16 v[202:203], v239 offset:18432\n\t" \
  "ds_read_b128 v[0:3], v229 offset:24576\n\t" \
  "v_mfma_f32_16x16x32_bf16 v[92:95], v[208:211], v[36:39], v[92:95]\n\t" \
  "ds_read_b64_tr_b16 v[204:205], v239 offset:20480\n\t" \
  "v_mfma_f32_16x16x32_bf16 v[92:95], v[212:215], v[44:47], v[92:95]\n\t" \
  "ds_read_b64_tr_b16 v[206:207], v239 offset:22528\n\t" \
  "s_waitcnt lgkmcnt(7)\n\t" \
  "v_mfma_f32_16x16x32_bf16 v[96:99], v[216:219], v[24:27], v[96:99]\n\t" \
  "ds_read_b64_tr_b16 v[208:209], v236 offset:36864\n\t" \
  "v_mfma_f32_16x16x32_bf16 v[96:99], v[220:223], v[32:35], v[96:99]\n\t" \
  "ds_read_b64_tr_b16 v[210:211], v236 offset:38912\n\t" \
  "v_mfma_f32_16x16x32_bf16 v[96:99], v[192:195], v[40:43], v[96:99]\n\t" \
  "ds_read_b64_tr_b16 v[212:213], v236 offset:40960\n\t" \
  "ds_read_b128 v[4:7], v229 offset:25600\n\t" \
  "v_mfma_f32_16x16x32_bf16 v[100:103], v[216:219], v[28:31], v[100:103]\n\t" \
  "ds_read_b64_tr_b16 v[214:215], v236 offset:43008\n\t" \
  "v_mfma_f32_16x16x32_bf16 v[100:103], v[220:223], v[36:39], v[100:103]\n\t" \
  "ds_read_b64_tr_b16 v[216:217], v236 offset:45056\n\t" \
  "v_mfma_f32_16x16x32_bf16 v[100:103], v[192:195], v[44:47], v[100:103]\n\t" \
  "ds_read_b64_tr_b16 v[218:219], v236 offset:47104\n\t" \
  "s_waitcnt lgkmcnt(7)\n\t" \
  "v_mfma_f32_16x16x32_bf16 v[104:107], v[196:199], v[24:27], v[104:107]\n\t" \
  "ds_read_b64_tr_b16 v[220:221], v237 offset:36864\n\t" \
  "v_mfma_f32_16x16x32_bf16 v[104:107], v[200:203], v[32:35], v[104:107]\n\t" \
  "ds_read_b64_tr_b16 v[222:223], v237 offset:38912\n\t" \
  "ds_read_b128 v[8:11], v229 offset:26624\n\t" \
  "v_mfma_f32_16x16x32_bf16 v[104:107], v[204:207], v[40:43], v[104:107]\n\t" \
  "ds_read_b64_tr_b16 v[192:193], v237 offset:40960\n\t" \
  "v_mfma_f32_16x16x32_bf16 v[108:111], v[196:199], v[28:31], v[108:111]\n\t" \
  "ds_read_b64_tr_b16 v[194:195], v237 offset:43008\n\t" \
  "v_mfma_f32_16x16x32_bf16 v[108:111], v[200:203], v[36:39], v[108:111]\n\t" \
  "ds_read_b64_tr_b16 v[196:197], v237 offset:45056\n\t" \
  "v_mfma_f32_16x16x32_bf16 v[108:111], v[204:207], v[44:47], v[108:111]\n\t" \
  "ds_read_b64_tr_b16 v[198:199], v237 offset:47104\n\t" \
  "s_waitcnt lgkmcnt(7)\n\t" \
  "v_mfma_f32_16x16x32_bf16 v[112:115], v[208:211], v[24:27], v[112:115]\n\t" \
  "ds_read_b64_tr_b16 v[200:201], v238 offset:36864\n\t" \
  "ds_read_b128 v[12:15], v229 offset:49152\n\t" \
  "v_mfma_f32_16x16x32_bf16 v[112:115], v[212:215], v[32:35], v[112:115]\n\t" \
  "ds_read_b64_tr_b16 v[202:203], v238 offset:38912\n\t" \
  "v_mfma_f32_16x16x32_bf16 v[112:115], v[216:219], v[40:43], v[112:115]\n\t" \
  "ds_read_b64_tr_b16 v[204:205], v238 offset:40960\n\t" \
  "v_mfma_f32_16x16x32_bf16 v[116:119], v[208:211], v[28:31], v[116:119]\n\t" \
  "ds_read_b64_tr_b16 v[206:207], v238 offset:43008\n\t" \
  "v_mfma_f32_16x16x32_bf16 v[116:119], v[212:215], v[36:39], v[116:119]\n\t" \
  "ds_read_b64_tr_b16 v[208:209], v238 offset:45056\n\t" \
  "v_mfma_f32_16x16x32_bf16 v[116:119], v[216:219], v[44:47], v[116:119]\n\t" \
  "ds_read_b64_tr_b16 v[210:211], v238 offset:47104\n\t" \
  "ds_read_b128 v[16:19], v229 offset:50176\n\t" \
  "s_waitcnt lgkmcnt(8)\n\t" \
  "v_mfma_f32_16x16x32_bf16 v[120:123], v[220:223], v[24:27], v[120:123]\n\t" \
  "ds_read_b64_tr_b16 v[212:213], v239 offset:36864\n\t" \
  "v_mfma_f32_16x16x32_bf16 v[120:123], v[192:195], v[32:35], v[120:123]\n\t" \
  "ds_read_b64_tr_b16 v[214:215], v239 offset:38912\n\t" \
  "v_mfma_f32_16x16x32_bf16 v[120:123], v[196:199], v[40:43], v[120:123]\n\t" \
  "ds_read_b64_tr_b16 v[216:217], v239 offset:40960\n\t" \
  "s_barrier\n\t" \
  "v_mfma_f32_16x16x32_bf16 v[124:127], v[220:223], v[28:31], v[124:127]\n\t" \
  "ds_read_b64_tr_b16 v[218:219], v239 offset:43008\n\t" \
  "v_mfma_f32_16x16x32_bf16 v[124:127], v[192:195], v[36:39], v[124:127]\n\t" \
  "ds_read_b64_tr_b16 v[220:221], v239 offset:45056\n\t" \
  "ds_read_b128 v[20:23], v229 offset:51200\n\t" \
  "v_mfma_f32_16x16x32_bf16 v[124:127], v[196:199], v[44:47], v[124:127]\n\t" \
  "ds_read_b64_tr_b16 v[222:223], v239 offset:47104\n\t" \
  "s_waitcnt lgkmcnt(8)\n\t" \
  "v_mfma_f32_16x16x32_bf16 v[128:131], v[200:203], v[24:27], v[128:131]\n\t" \
  "ds_read_b64_tr_b16 v[192:193], v232 offset:0\n\t" \
  "v_mfma_f32_16x16x32_bf16 v[128:131], v[204:207], v[32:35], v[128:131]\n\t" \
  "ds_read_b64_tr_b16 v[194:195], v232 offset:2048\n\t" \
  "v_mfma_f32_16x16x32_bf16 v[128:131], v[208:211], v[40:43], v[128:131]\n\t" \
  "ds_read_b64_tr_b16 v[196:197], v232 offset:4096\n\t" \
  "v_mfma_f32_16x16x32_bf16 v[132:135], v[200:203], v[28:31], v[132:135]\n\t" \
  "ds_read_b64_tr_b16 v[198:199], v232 offset:6144\n\t" \
  "v_mfma_f32_16x16x32_bf16 v[132:135], v[204:207], v[36:39], v[132:135]\n\t" \
  "ds_read_b64_tr_b16 v[200:201], v232 offset:8192\n\t" \
  "v_mfma_f32_16x16x32_bf16 v[132:135], v[208:211], v[44:47], v[132:135]\n\t" \
  "ds_read_b64_tr_b16 v[202:203], v232 offset:10240\n\t" \
  "s_waitcnt lgkmcnt(6)\n\t" \
  "v_mfma_f32_16x16x32_bf16 v[136:139], v[212:215], v[24:27], v[136:139]\n\t" \
  "ds_read_b64_tr_b16 v[204:205], v233 offset:0\n\t" \
  "v_mfma_f32_16x16x32_bf16 v[136:139], v[216:219], v[32:35], v[136:139]\n\t" \
  "ds_read_b64_tr_b16 v[206:207], v233 offset:2048\n\t" \
  "v_mfma_f32_16x16x32_bf16 v[136:139], v[220:223], v[40:43], v[136:139]\n\t" \
  "ds_read_b64_tr_b16 v[208:209], v233 offset:4096\n\t" \
  "v_mfma_f32_16x16x32_bf16 v[140:143], v[212:215], v[28:31], v[140:143]\n\t" \
  "ds_read_b64_tr_b16 v[210:211], v233 offset:6144\n\t" \
  "v_mfma_f32_16x16x32_bf16 v[140:143], v[216:219], v[36:39], v[140:143]\n\t" \
  "ds_read_b64_tr_b16 v[212:213], v233 offset:8192\n\t" \
  "v_mfma_f32_16x16x32_bf16 v[140:143], v[220:223], v[44:47], v[140:143]\n\t" \
  "ds_read_b64_tr_b16 v[214:215], v233 offset:10240\n\t" \
  "s_waitcnt lgkmcnt(6)\n\t" \
  "v_mfma_f32_16x16x32_bf16 v[80:83], v[192:195], v[0:3], v[80:83]\n\t" \
  "ds_read_b64_tr_b16 v[216:217], v234 offset:0\n\t" \
  "v_mfma_f32_16x16x32_bf16 v[80:83], v[196:199], v[8:11], v[80:83]\n\t" \
  "ds_read_b64_tr_b16 v[218:219], v234 offset:2048\n\t" \
  "v_mfma_f32_16x16x32_bf16 v[80:83], v[200:203], v[16:19], v[80:83]\n\t" \
  "ds_read_b64_tr_b16 v[220:221], v234 offset:4096\n\t" \
  "v_mfma_f32_16x16x32_bf16 v[84:87], v[192:195], v[4:7], v[84:87]\n\t" \
  "ds_read_b64_tr_b16 v[222:223], v234 offset:6144\n\t" \
  "v_mfma_f32_16x16x32_bf16 v[84:87], v[196:199], v[12:15], v[84:87]\n\t" \
  "ds_read_b64_tr_b16 v[192:193], v234 offset:8192\n\t" \
  "v_mfma_f32_16x16x32_bf16 v[84:87], v[200:203], v[20:23], v[84:87]\n\t" \
  "ds_read_b64_tr_b16 v[194:195], v234 offset:10240\n\t" \
  "s_waitcnt lgkmcnt(6)\n\t" \
  "v_mfma_f32_16x16x32_bf16 v[88:91], v[204:207], v[0:3], v[88:91]\n\t" \
  "ds_read_b64_tr_b16 v[196:197], v235 offset:0\n\t" \
  "v_mfma_f32_16x16x32_bf16 v[88:91], v[208:211], v[8:11], v[88:91]\n\t" \
  "ds_read_b64_tr_b16 v[198:199], v235 offset:2048\n\t" \
  "v_mfma_f32_16x16x32_bf16 v[88:91], v[212:215], v[16:19], v[88:91]\n\t" \
  "ds_read_b64_tr_b16 v[200:201], v235 offset:4096\n\t" \
  "v_mfma_f32_16x16x32_bf16 v[92:95], v[204:207], v[4:7], v[92:95]\n\t" \
  "ds_read_b64_tr_b16 v[202:203], v235 offset:6144\n\t" \
  "ds_read_b128 v[24:27], v230 offset:12288\n\t" \
  "v_mfma_f32_16x16x32_bf16 v[92:95], v[208:211], v[12:15], v[92:95]\n\t" \
  "ds_read_b64_tr_b16 v[204:205], v235 offset:8192\n\t" \
  "v_mfma_f32_16x16x32_bf16 v[92:95], v[212:215], v[20:23], v[92:95]\n\t" \
  "ds_read_b64_tr_b16 v[206:207], v235 offset:10240\n\t" \
  "s_waitcnt lgkmcnt(7)\n\t" \
  "v_mfma_f32_16x16x32_bf16 v[96:99], v[216:219], v[0:3], v[96:99]\n\t" \
  "ds_read_b64_tr_b16 v[208:209], v232 offset:12288\n\t" \
  "v_mfma_f32_16x16x32_bf16 v[96:99], v[220:223], v[8:11], v[96:99]\n\t" \
  "ds_read_b64_tr_b16 v[210:211], v232 offset:14336\n\t" \
  "v_mfma_f32_16x16x32_bf16 v[96:99], v[192:195], v[16:19], v[96:99]\n\t" \
  "ds_read_b64_tr_b16 v[212:213], v232 offset:16384\n\t" \
  "ds_read_b128 v[28:31], v230 offset:13312\n\t" \
  "v_mfma_f32_16x16x32_bf16 v[100:103], v[216:219], v[4:7], v[100:103]\n\t" \
  "ds_read_b64_tr_b16 v[214:215], v232 offset:18432\n\t" \
  "v_mfma_f32_16x16x32_bf16 v[100:103], v[220:223], v[12:15], v[100:103]\n\t" \
  "ds_read_b64_tr_b16 v[216:217], v232 offset:20480\n\t" \
  "v_mfma_f32_16x16x32_bf16 v[100:103], v[192:195], v[20:23], v[100:103]\n\t" \
  "ds_read_b64_tr_b16 v[218:219], v232 offset:22528\n\t" \
  "s_waitcnt lgkmcnt(7)\n\t" \
  "v_mfma_f32_16x16x32_bf16 v[104:107], v[196:199], v[0:3], v[104:107]\n\t" \
  "ds_read_b64_tr_b16 v[220:221], v233 offset:12288\n\t" \
  "v_mfma_f32_16x16x32_bf16 v[104:107], v[200:203], v[8:11], v[104:107]\n\t" \
  "ds_read_b64_tr_b16 v[222:223], v233 offset:14336\n\t" \
  "ds_read_b128 v[32:35], v230 offset:14336\n\t" \
  "v_mfma_f32_16x16x32_bf16 v[104:107], v[204:207], v[16:19], v[104:107]\n\t" \
  "ds_read_b64_tr_b16 v[192:193], v233 offset:16384\n\t" \
  "s_waitcnt vmcnt(0)\n\t" \
  "s_barrier\n\t" \
  "v_mfma_f32_16x16x32_bf16 v[108:111], v[196:199], v[4:7], v[108:111]\n\t" \
  "ds_read_b64_tr_b16 v[194:195], v233 offset:18432\n\t" \
  "v_mfma_f32_16x16x32_bf16 v[108:111], v[200:203], v[12:15], v[108:111]\n\t" \
  "ds_read_b64_tr_b16 v[196:197], v233 offset:20480\n\t" \
  "v_mfma_f32_16x16x32_bf16 v[108:111], v[204:207], v[20:23], v[108:111]\n\t" \
  "ds_read_b64_tr_b16 v[198:199], v233 offset:22528\n\t" \
  "s_waitcnt lgkmcnt(7)\n\t" \
  "v_mfma_f32_16x16x32_bf16 v[112:115], v[208:211], v[0:3], v[112:115]\n\t" \
  "ds_read_b64_tr_b16 v[200:201], v234 offset:12288\n\t" \
  "ds_read_b128 v[36:39], v228 offset:36864\n\t" \
  "v_mfma_f32_16x16x32_bf16 v[112:115], v[212:215], v[8:11], v[112:115]\n\t" \
  "ds_read_b64_tr_b16 v[202:203], v234 offset:14336\n\t" \
  "v_mfma_f32_16x16x32_bf16 v[112:115], v[216:219], v[16:19], v[112:115]\n\t" \
  "ds_read_b64_tr_b16 v[204:205], v234 offset:16384\n\t" \
  "v_mfma_f32_16x16x32_bf16 v[116:119], v[208:211], v[4:7], v[116:119]\n\t" \
  "ds_read_b64_tr_b16 v[206:207], v234 offset:18432\n\t" \
  "v_mfma_f32_16x16x32_bf16 v[116:119], v[212:215], v[12:15], v[116:119]\n\t" \
  "ds_read_b64_tr_b16 v[208:209], v234 offset:20480\n\t" \
  "v_mfma_f32_16x16x32_bf16 v[116:119], v[216:219], v[20:23], v[116:119]\n\t" \
  "ds_read_b64_tr_b16 v[210:211], v234 offset:22528\n\t" \
  "ds_read_b128 v[40:43], v228 offset:37888\n\t" \
  "s_waitcnt lgkmcnt(8)\n\t" \
  "v_mfma_f32_16x16x32_bf16 v[120:123], v[220:223], v[0:3], v[120:123]\n\t" \
  "ds_read_b64_tr_b16 v[212:213], v235 offset:12288\n\t" \
  "v_mfma_f32_16x16x32_bf16 v[120:123], v[192:195], v[8:11], v[120:123]\n\t" \
  "ds_read_b64_tr_b16 v[214:215], v235 offset:14336\n\t" \
  "v_mfma_f32_16x16x32_bf16 v[120:123], v[196:199], v[16:19], v[120:123]\n\t" \
  "ds_read_b64_tr_b16 v[216:217], v235 offset:16384\n\t" \
  "v_mfma_f32_16x16x32_bf16 v[124:127], v[220:223], v[4:7], v[124:127]\n\t" \
  "ds_read_b64_tr_b16 v[218:219], v235 offset:18432\n\t" \
  "v_mfma_f32_16x16x32_bf16 v[124:127], v[192:195], v[12:15], v[124:127]\n\t" \
  "ds_read_b64_tr_b16 v[220:221], v235 offset:20480\n\t" \
  "ds_read_b128 v[44:47], v228 offset:38912\n\t" \
  "v_mfma_f32_16x16x32_bf16 v[124:127], v[196:199], v[20:23], v[124:127]\n\t" \
  "ds_read_b64_tr_b16 v[222:223], v235 offset:22528\n\t" \
  "s_waitcnt lgkmcnt(8)\n\t" \
  "v_mfma_f32_16x16x32_bf16 v[128:131], v[200:203], v[0:3], v[128:131]\n\t" \
  "ds_read_b64_tr_b16 v[192:193], v240 offset:0\n\t" \
  "v_add_u32 v231, 36864, v244\n\t" \
  "s_add_u32 s78, %[sp], 4096\n\t" \
  "s_lshr_b32 s75, %[sl], 2\n\t" \
  "s_cmp_eq_u32 s75, %[wi]\n\t" \
  "s_cselect_b64 vcc, -1, 0\n\t" \
  "v_mfma_f32_16x16x32_bf16 v[128:131], v[204:207], v[8:11], v[128:131]\n\t" \
  "ds_read_b64_tr_b16 v[194:195], v240 offset:2048\n\t" \
  "v_mfma_f32_16x16x32_bf16 v[128:131], v[208:211], v[16:19], v[128:131]\n\t" \
  "ds_read_b64_tr_b16 v[196:197], v240 offset:4096\n\t" \
  "v_mfma_f32_16x16x32_bf16 v[132:135], v[200:203], v[4:7], v[132:135]\n\t" \
  "ds_read_b64_tr_b16 v[198:199], v240 offset:6144\n\t" \
  "v_mfma_f32_16x16x32_bf16 v[132:135], v[204:207], v[12:15], v[132:135]\n\t" \
  "ds_read_b64_tr_b16 v[200:201], v240 offset:8192\n\t" \
  "v_mfma_f32_16x16x32_bf16 v[132:135], v[208:211], v[20:23], v[132:135]\n\t" \
  "ds_read_b64_tr_b16 v[202:203], v240 offset:10240\n\t" \
  "s_waitcnt lgkmcnt(6)\n\t" \
  "v_mfma_f32_16x16x32_bf16 v[136:139], v[212:215], v[0:3], v[136:139]\n\t" \
  "ds_read_b64_tr_b16 v[204:205], v241 offset:0\n\t" \
  "v_mfma_f32_16x16x32_bf16 v[136:139], v[216:219], v[8:11], v[136:139]\n\t" \
  "ds_read_b64_tr_b16 v[206:207], v241 offset:2048\n\t" \
  "v_mfma_f32_16x16x32_bf16 v[136:139], v[220:223], v[16:19], v[136:139]\n\t" \
  "ds_read_b64_tr_b16 v[208:209], v241 offset:4096\n\t" \
  "v_mfma_f32_16x16x32_bf16 v[140:143], v[212:215], v[4:7], v[140:143]\n\t" \
  "ds_read_b64_tr_b16 v[210:211], v241 offset:6144\n\t" \
  "v_mfma_f32_16x16x32_bf16 v[140:143], v[216:219], v[12:15], v[140:143]\n\t" \
  "ds_read_b64_tr_b16 v[212:213], v241 offset:8192\n\t" \
  "v_mfma_f32_16x16x32_bf16 v[140:143], v[220:223], v[20:23], v[140:143]\n\t" \
  "ds_read_b64_tr_b16 v[214:215], v241 offset:10240\n\t" \
  "s_waitcnt lgkmcnt(6)\n\t" \
  "v_mfma_f32_16x16x32_bf16 v[80:83], v[192:195], v[24:27], v[80:83]\n\t" \
  "ds_read_b64_tr_b16 v[216:217], v242 offset:0\n\t" \
  "v_mfma_f32_16x16x32_bf16 v[80:83], v[196:199], v[32:35], v[80:83]\n\t" \
  "ds_read_b64_tr_b16 v[218:219], v242 offset:2048\n\t" \
  "v_mfma_f32_16x16x32_bf16 v[80:83], v[200:203], v[40:43], v[80:83]\n\t" \
  "ds_read_b64_tr_b16 v[220:221], v242 offset:4096\n\t" \
  "v_mfma_f32_16x16x32_bf16 v[84:87], v[192:195], v[28:31], v[84:87]\n\t" \
  "ds_read_b64_tr_b16 v[222:223], v242 offset:6144\n\t" \
  "v_mfma_f32_16x16x32_bf16 v[84:87], v[196:199], v[36:39], v[84:87]\n\t" \
  "ds_read_b64_tr_b16 v[192:193], v242 offset:8192\n\t" \
  "v_mfma_f32_16x16x32_bf16 v[84:87], v[200:203], v[44:47], v[84:87]\n\t" \
  "ds_read_b64_tr_b16 v[194:195], v242 offset:10240\n\t" \
  "s_waitcnt lgkmcnt(6)\n\t" \
  "v_mfma_f32_16x16x32_bf16 v[88:91], v[204:207], v[24:27], v[88:91]\n\t" \
  "ds_read_b64_tr_b16 v[196:197], v243 offset:0\n\t" \
  "v_mfma_f32_16x16x32_bf16 v[88:91], v[208:211], v[32:35], v[88:91]\n\t" \
  "ds_read_b64_tr_b16 v[198:199], v243 offset:2048\n\t" \
  "v_mfma_f32_16x16x32_bf16 v[88:91], v[212:215], v[40:43], v[88:91]\n\t" \
  "ds_read_b64_tr_b16 v[200:201], v243 offset:4096\n\t" \
  "s_barrier\n\t" \
  "v_mfma_f32_16x16x32_bf16 v[92:95], v[204:207], v[28:31], v[92:95]\n\t" \
  "ds_read_b64_tr_b16 v[202:203], v243 offset:6144\n\t" \
  "ds_read_b128 v[0:3], v228 offset:24576\n\t" \
  "v_mfma_f32_16x16x32_bf16 v[92:95], v[208:211], v[36:39], v[92:95]\n\t" \
  "ds_read_b64_tr_b16 v[204:205], v243 offset:8192\n\t" \
  "v_mfma_f32_16x16x32_bf16 v[92:95], v[212:215], v[44:47], v[92:95]\n\t" \
  "ds_read_b64_tr_b16 v[206:207], v243 offset:10240\n\t" \
  "s_waitcnt lgkmcnt(7)\n\t" \
  "v_mfma_f32_16x16x32_bf16 v[96:99], v[216:219], v[24:27], v[96:99]\n\t" \
  "ds_read_b64_tr_b16 v[208:209], v232 offset:49152\n\t" \
  "v_mfma_f32_16x16x32_bf16 v[96:99], v[220:223], v[32:35], v[96:99]\n\t" \
  "ds_read_b64_tr_b16 v[210:211], v232 offset:51200\n\t" \
  "v_mfma_f32_16x16x32_bf16 v[96:99], v[192:195], v[40:43], v[96:99]\n\t" \
  "ds_read_b64_tr_b16 v[212:213], v232 offset:53248\n\t" \
  "ds_read_b128 v[4:7], v228 offset:25600\n\t" \
  "v_mfma_f32_16x16x32_bf16 v[100:103], v[216:219], v[28:31], v[100:103]\n\t" \
  "ds_read_b64_tr_b16 v[214:215], v232 offset:55296\n\t" \
  "v_mfma_f32_16x16x32_bf16 v[100:103], v[220:223], v[36:39], v[100:103]\n\t" \
  "ds_read_b64_tr_b16 v[216:217], v232 offset:57344\n\t" \
  "v_mfma_f32_16x16x32_bf16 v[100:103], v[192:195], v[44:47], v[100:103]\n\t" \
  "ds_read_b64_tr_b16 v[218:219], v232 offset:59392\n\t" \
  "s_waitcnt lgkmcnt(7)\n\t" \
  "v_mfma_f32_16x16x32_bf16 v[104:107], v[196:199], v[24:27], v[104:107]\n\t" \
  "ds_read_b64_tr_b16 v[220:221], v233 offset:49152\n\t" \
  "v_mfma_f32_16x16x32_bf16 v[104:107], v[200:203], v[32:35], v[104:107]\n\t" \
  "ds_read_b64_tr_b16 v[222:223], v233 offset:51200\n\t" \
  "ds_read_b128 v[8:11], v228 offset:26624\n\t" \
  "v_mfma_f32_16x16x32_bf16 v[104:107], v[204:207], v[40:43], v[104:107]\n\t" \
  "ds_read_b64_tr_b16 v[192:193], v233 offset:53248\n\t" \
  "v_mfma_f32_16x16x32_bf16 v[108:111], v[196:199], v[28:31], v[108:111]\n\t" \
  "ds_read_b64_tr_b16 v[194:195], v233 offset:55296\n\t" \
  "v_mfma_f32_16x16x32_bf16 v[108:111], v[200:203], v[36:39], v[108:111]\n\t" \
  "ds_read_b64_tr_b16 v[196:197], v233 offset:57344\n\t" \
  "v_mfma_f32_16x16x32_bf16 v[108:111], v[204:207], v[44:47], v[108:111]\n\t" \
  "ds_read_b64_tr_b16 v[198:199], v233 offset:59392\n\t" \
  "s_waitcnt lgkmcnt(7)\n\t" \
  "v_mfma_f32_16x16x32_bf16 v[112:115], v[208:211], v[24:27], v[112:115]\n\t" \
  "ds_read_b64_tr_b16 v[200:201], v234 offset:49152\n\t" \
  "ds_read_b128 v[12:15], v229 offset:0\n\t" \
  "v_mfma_f32_16x16x32_bf16 v[112:115], v[212:215], v[32:35], v[112:115]\n\t" \
  "ds_read_b64_tr_b16 v[202:203], v234 offset:51200\n\t" \
  "v_mfma_f32_16x16x32_bf16 v[112:115], v[216:219], v[40:43], v[112:115]\n\t" \
  "ds_read_b64_tr_b16 v[204:205], v234 offset:53248\n\t" \
  "v_mfma_f32_16x16x32_bf16 v[116:119], v[208:211], v[28:31], v[116:119]\n\t" \
  "ds_read_b64_tr_b16 v[206:207], v234 offset:55296\n\t" \
  "v_mfma_f32_16x16x32_bf16 v[116:119], v[212:215], v[36:39], v[116:119]\n\t" \
  "ds_read_b64_tr_b16 v[208:209], v234 offset:57344\n\t" \
  "v_mfma_f32_16x16x32_bf16 v[116:119], v[216:219], v[44:47], v[116:119]\n\t" \
  "ds_read_b64_tr_b16 v[210:211], v234 offset:59392\n\t" \
  "ds_read_b128 v[16:19], v229 offset:1024\n\t" \
  "s_waitcnt lgkmcnt(8)\n\t" \
  "v_mfma_f32_16x16x32_bf16 v[120:123], v[220:223], v[24:27], v[120:123]\n\t" \
  "ds_read_b64_tr_b16 v[212:213], v235 offset:49152\n\t" \
  "v_mfma_f32_16x16x32_bf16 v[120:123], v[192:195], v[32:35], v[120:123]\n\t" \
  "ds_read_b64_tr_b16 v[214:215], v235 offset:51200\n\t" \
  "v_mfma_f32_16x16x32_bf16 v[120:123], v[196:199], v[40:43], v[120:123]\n\t" \
  "ds_read_b64_tr_b16 v[216:217], v235 offset:53248\n\t" \
  "v_mfma_f32_16x16x32_bf16 v[124:127], v[220:223], v[28:31], v[124:127]\n\t" \
  "ds_read_b64_tr_b16 v[218:219], v235 offset:55296\n\t" \
  "v_mfma_f32_16x16x32_bf16 v[124:127], v[192:195], v[36:39], v[124:127]\n\t" \
  "ds_read_b64_tr_b16 v[220:221], v235 offset:57344\n\t" \
  "ds_read_b128 v[20:23], v229 offset:2048\n\t" \
  "v_mfma_f32_16x16x32_bf16 v[124:127], v[196:199], v[44:47], v[124:127]\n\t" \
  "ds_read_b64_tr_b16 v[222:223], v235 offset:59392\n\t" \
  "s_waitcnt lgkmcnt(8)\n\t" \
  "v_mfma_f32_16x16x32_bf16 v[128:131], v[200:203], v[24:27], v[128:131]\n\t" \
  "ds_read_b64_tr_b16 v[192:193], v240 offset:24576\n\t" \
  "v_mfma_f32_16x16x32_bf16 v[128:131], v[204:207], v[32:35], v[128:131]\n\t" \
  "ds_read_b64_tr_b16 v[194:195], v240 offset:26624\n\t" \
  "v_mfma_f32_16x16x32_bf16 v[128:131], v[208:211], v[40:43], v[128:131]\n\t" \
  "ds_read_b64_tr_b16 v[196:197], v240 offset:28672\n\t" \
  "v_mfma_f32_16x16x32_bf16 v[132:135], v[200:203], v[28:31], v[132:135]\n\t" \
  "ds_read_b64_tr_b16 v[198:199], v240 offset:30720\n\t" \
  "v_mfma_f32_16x16x32_bf16 v[132:135], v[204:207], v[36:39], v[132:135]\n\t" \
  "ds_read_b64_tr_b16 v[200:201], v240 offset:32768\n\t" \
  "v_mfma_f32_16x16x32_bf16 v[132:135], v[208:211], v[44:47], v[132:135]\n\t" \
  "ds_read_b64_tr_b16 v[202:203], v240 offset:34816\n\t" \
  "s_waitcnt lgkmcnt(6)\n\t" \
  "v_mfma_f32_16x16x32_bf16 v[136:139], v[212:215], v[24:27], v[136:139]\n\t" \
  "ds_read_b64_tr_b16 v[204:205], v241 offset:24576\n\t" \
  "v_mfma_f32_16x16x32_bf16 v[136:139], v[216:219], v[32:35], v[136:139]\n\t" \
  "ds_read_b64_tr_b16 v[206:207], v241 offset:26624\n\t" \
  "v_mfma_f32_16x16x32_bf16 v[136:139], v[220:223], v[40:43], v[136:139]\n\t" \
  "ds_read_b64_tr_b16 v[208:209], v241 offset:28672\n\t" \
  "s_barrier\n\t" \
  "v_mfma_f32_16x16x32_bf16 v[140:143], v[212:215], v[28:31], v[140:143]\n\t" \
  "ds_read_b64_tr_b16 v[210:211], v241 offset:30720\n\t" \
  "v_mfma_f32_16x16x32_bf16 v[140:143], v[216:219], v[36:39], v[140:143]\n\t" \
  "ds_read_b64_tr_b16 v[212:213], v241 offset:32768\n\t" \
  "v_mfma_f32_16x16x32_bf16 v[140:143], v[220:223], v[44:47], v[140:143]\n\t" \
  "ds_read_b64_tr_b16 v[214:215], v241 offset:34816\n\t" \
  "s_waitcnt lgkmcnt(6)\n\t" \
  "v_mfma_f32_16x16x32_bf16 v[80:83], v[192:195], v[0:3], v[80:83]\n\t" \
  "ds_read_b64_tr_b16 v[216:217], v242 offset:24576\n\t" \
  "v_mfma_f32_16x16x32_bf16 v[80:83], v[196:199], v[8:11], v[80:83]\n\t" \
  "ds_read_b64_tr_b16 v[218:219], v242 offset:26624\n\t" \
  "v_mfma_f32_16x16x32_bf16 v[80:83], v[200:203], v[16:19], v[80:83]\n\t" \
  "ds_read_b64_tr_b16 v[220:221], v242 offset:28672\n\t" \
  "v_mfma_f32_16x16x32_bf16 v[84:87], v[192:195], v[4:7], v[84:87]\n\t" \
  "ds_read_b64_tr_b16 v[222:223], v242 offset:30720\n\t" \
  "v_mfma_f32_16x16x32_bf16 v[84:87], v[196:199], v[12:15], v[84:87]\n\t" \
  "ds_read_b64_tr_b16 v[192:193], v242 offset:32768\n\t" \
  "v_mfma_f32_16x16x32_bf16 v[84:87], v[200:203], v[20:23], v[84:87]\n\t" \
  "ds_read_b64_tr_b16 v[194:195], v242 offset:34816\n\t" \
  "s_waitcnt lgkmcnt(6)\n\t" \
  "v_mfma_f32_16x16x32_bf16 v[88:91], v[204:207], v[0:3], v[88:91]\n\t" \
  "ds_read_b64_tr_b16 v[196:197], v243 offset:24576\n\t" \
  "v_mfma_f32_16x16x32_bf16 v[88:91], v[208:211], v[8:11], v[88:91]\n\t" \
  "ds_read_b64_tr_b16 v[198:199], v243 offset:26624\n\t" \
  "v_mfma_f32_16x16x32_bf16 v[88:91], v[212:215], v[16:19], v[88:91]\n\t" \
  "ds_read_b64_tr_b16 v[200:201], v243 offset:28672\n\t" \
  "v_mfma_f32_16x16x32_bf16 v[92:95], v[204:207], v[4:7], v[92:95]\n\t" \
  "ds_read_b64_tr_b16 v[202:203], v243 offset:30720\n\t" \
  "ds_read_b128 v[24:27], v229 offset:12288\n\t" \
  "v_mfma_f32_16x16x32_bf16 v[92:95], v[208:211], v[12:15], v[92:95]\n\t" \
  "ds_read_b64_tr_b16 v[204:205], v243 offset:32768\n\t" \
  "v_mfma_f32_16x16x32_bf16 v[92:95], v[212:215], v[20:23], v[92:95]\n\t" \
  "ds_read_b64_tr_b16 v[206:207], v243 offset:34816\n\t" \
  "s_waitcnt lgkmcnt(7)\n\t" \
  "v_mfma_f32_16x16x32_bf16 v[96:99], v[216:219], v[0:3], v[96:99]\n\t" \
  "ds_read_b64_tr_b16 v[208:209], v232 offset:0\n\t" \
  "v_mfma_f32_16x16x32_bf16 v[96:99], v[220:223], v[8:11], v[96:99]\n\t" \
  "ds_read_b64_tr_b16 v[210:211], v232 offset:2048\n\t" \
  "v_mfma_f32_16x16x32_bf16 v[96:99], v[192:195], v[16:19], v[96:99]\n\t" \
  "ds_read_b64_tr_b16 v[212:213], v232 offset:4096\n\t" \
  "ds_read_b128 v[28:31], v229 offset:13312\n\t" \
  "v_mfma_f32_16x16x32_bf16 v[100:103], v[216:219], v[4:7], v[100:103]\n\t" \
  "ds_read_b64_tr_b16 v[214:215], v232 offset:6144\n\t" \
  "v_mfma_f32_16x16x32_bf16 v[100:103], v[220:223], v[12:15], v[100:103]\n\t" \
  "ds_read_b64_tr_b16 v[216:217], v232 offset:8192\n\t" \
  "v_mfma_f32_16x16x32_bf16 v[100:103], v[192:195], v[20:23], v[100:103]\n\t" \
  "ds_read_b64_tr_b16 v[218:219], v232 offset:10240\n\t" \
  "s_waitcnt lgkmcnt(7)\n\t" \
  "v_mfma_f32_16x16x32_bf16 v[104:107], v[196:199], v[0:3], v[104:107]\n\t" \
  "ds_read_b64_tr_b16 v[220:221], v233 offset:0\n\t" \
  "v_mfma_f32_16x16x32_bf16 v[104:107], v[200:203], v[8:11], v[104:107]\n\t" \
  "ds_read_b64_tr_b16 v[222:223], v233 offset:2048\n\t" \
  "ds_read_b128 v[32:35], v229 offset:14336\n\t" \
  "v_mfma_f32_16x16x32_bf16 v[104:107], v[204:207], v[16:19], v[104:107]\n\t" \
  "ds_read_b64_tr_b16 v[192:193], v233 offset:4096\n\t" \
  "v_mfma_f32_16x16x32_bf16 v[108:111], v[196:199], v[4:7], v[108:111]\n\t" \
  "ds_read_b64_tr_b16 v[194:195], v233 offset:6144\n\t" \
  "v_mfma_f32_16x16x32_bf16 v[108:111], v[200:203], v[12:15], v[108:111]\n\t" \
  "ds_read_b64_tr_b16 v[196:197], v233 offset:8192\n\t" \
  "v_mfma_f32_16x16x32_bf16 v[108:111], v[204:207], v[20:23], v[108:111]\n\t" \
  "ds_read_b64_tr_b16 v[198:199], v233 offset:10240\n\t" \
  "s_waitcnt lgkmcnt(7)\n\t" \
  "v_mfma_f32_16x16x32_bf16 v[112:115], v[208:211], v[0:3], v[112:115]\n\t" \
  "ds_read_b64_tr_b16 v[200:201], v234 offset:0\n\t" \
  "ds_read_b128 v[36:39], v229 offset:24576\n\t" \
  "v_mfma_f32_16x16x32_bf16 v[112:115], v[212:215], v[8:11], v[112:115]\n\t" \
  "ds_read_b64_tr_b16 v[202:203], v234 offset:2048\n\t" \
  "v_mfma_f32_16x16x32_bf16 v[112:115], v[216:219], v[16:19], v[112:115]\n\t" \
  "ds_read_b64_tr_b16 v[204:205], v234 offset:4096\n\t" \
  "v_mfma_f32_16x16x32_bf16 v[116:119], v[208:211], v[4:7], v[116:119]\n\t" \
  "ds_read_b64_tr_b16 v[206:207], v234 offset:6144\n\t" \
  "v_mfma_f32_16x16x32_bf16 v[116:119], v[212:215], v[12:15], v[116:119]\n\t" \
  "ds_read_b64_tr_b16 v[208:209], v234 offset:8192\n\t" \
  "v_mfma_f32_16x16x32_bf16 v[116:119], v[216:219], v[20:23], v[116:119]\n\t" \
  "ds_read_b64_tr_b16 v[210:211], v234 offset:10240\n\t" \
  "ds_read_b128 v[40:43], v229 offset:25600\n\t" \
  "s_waitcnt lgkmcnt(8)\n\t" \
  "v_mfma_f32_16x16x32_bf16 v[120:123], v[220:223], v[0:3], v[120:123]\n\t" \
  "ds_read_b64_tr_b16 v[212:213], v235 offset:0\n\t" \
  "v_mfma_f32_16x16x32_bf16 v[120:123], v[192:195], v[8:11], v[120:123]\n\t" \
  "ds_read_b64_tr_b16 v[214:215], v235 offset:2048\n\t" \
  "v_mfma_f32_16x16x32_bf16 v[120:123], v[196:199], v[16:19], v[120:123]\n\t" \
  "ds_read_b64_tr_b16 v[216:217], v235 offset:4096\n\t" \
  "s_barrier\n\t" \
  "v_mfma_f32_16x16x32_bf16 v[124:127], v[220:223], v[4:7], v[124:127]\n\t" \
  "ds_read_b64_tr_b16 v[218:219], v235 offset:6144\n\t" \
  "v_mfma_f32_16x16x32_bf16 v[124:127], v[192:195], v[12:15], v[124:127]\n\t" \
  "ds_read_b64_tr_b16 v[220:221], v235 offset:8192\n\t" \
  "ds_read_b128 v[44:47], v229 offset:26624\n\t" \
  "v_mfma_f32_16x16x32_bf16 v[124:127], v[196:199], v[20:23], v[124:127]\n\t" \
  "ds_read_b64_tr_b16 v[222:223], v235 offset:10240\n\t" \
  "s_waitcnt lgkmcnt(8)\n\t" \
  "v_mfma_f32_16x16x32_bf16 v[128:131], v[200:203], v[0:3], v[128:131]\n\t" \
  "ds_read_b64_tr_b16 v[192:193], v236 offset:36864\n\t" \
  "v_mfma_f32_16x16x32_bf16 v[128:131], v[204:207], v[8:11], v[128:131]\n\t" \
  "ds_read_b64_tr_b16 v[194:195], v236 offset:38912\n\t" \
  "v_mfma_f32_16x16x32_bf16 v[128:131], v[208:211], v[16:19], v[128:131]\n\t" \
  "ds_read_b64_tr_b16 v[196:197], v236 offset:40960\n\t" \
  "v_mfma_f32_16x16x32_bf16 v[132:135], v[200:203], v[4:7], v[132:135]\n\t" \
  "ds_read_b64_tr_b16 v[198:199], v236 offset:43008\n\t" \
  "v_mfma_f32_16x16x32_bf16 v[132:135], v[204:207], v[12:15], v[132:135]\n\t" \
  "ds_read_b64_tr_b16 v[200:201], v236 offset:45056\n\t" \
  "v_mfma_f32_16x16x32_bf16 v[132:135], v[208:211], v[20:23], v[132:135]\n\t" \
  "ds_read_b64_tr_b16 v[202:203], v236 offset:47104\n\t" \
  "s_waitcnt lgkmcnt(6)\n\t" \
  "v_mfma_f32_16x16x32_bf16 v[136:139], v[212:215], v[0:3], v[136:139]\n\t" \
  "ds_read_b64_tr_b16 v[204:205], v237 offset:36864\n\t" \
  "v_mfma_f32_16x16x32_bf16 v[136:139], v[216:219], v[8:11], v[136:139]\n\t" \
  "ds_read_b64_tr_b16 v[206:207], v237 offset:38912\n\t" \
  "v_mfma_f32_16x16x32_bf16 v[136:139], v[220:223], v[16:19], v[136:139]\n\t" \
  "ds_read_b64_tr_b16 v[208:209], v237 offset:40960\n\t" \
  "v_mfma_f32_16x16x32_bf16 v[140:143], v[212:215], v[4:7], v[140:143]\n\t" \
  "ds_read_b64_tr_b16 v[210:211], v237 offset:43008\n\t" \
  "v_mfma_f32_16x16x32_bf16 v[140:143], v[216:219], v[12:15], v[140:143]\n\t" \
  "ds_read_b64_tr_b16 v[212:213], v237 offset:45056\n\t" \
  "v_mfma_f32_16x16x32_bf16 v[140:143], v[220:223], v[20:23], v[140:143]\n\t" \
  "ds_read_b64_tr_b16 v[214:215], v237 offset:47104\n\t" \
  "s_waitcnt lgkmcnt(6)\n\t" \
  "v_mfma_f32_16x16x32_bf16 v[48:51], v[192:195], v[24:27], v[48:51]\n\t" \
  "ds_read_b64_tr_b16 v[216:217], v238 offset:36864\n\t" \
  "v_mfma_f32_16x16x32_bf16 v[48:51], v[196:199], v[32:35], v[48:51]\n\t" \
  "ds_read_b64_tr_b16 v[218:219], v238 offset:38912\n\t" \
  "v_mfma_f32_16x16x32_bf16 v[48:51], v[200:203], v[40:43], v[48:51]\n\t" \
  "ds_read_b64_tr_b16 v[220:221], v238 offset:40960\n\t" \
  "v_mfma_f32_16x16x32_bf16 v[52:55], v[192:195], v[28:31], v[52:55]\n\t" \
  "ds_read_b64_tr_b16 v[222:223], v238 offset:43008\n\t" \
  "v_mfma_f32_16x16x32_bf16 v[52:55], v[196:199], v[36:39], v[52:55]\n\t" \
  "ds_read_b64_tr_b16 v[192:193], v238 offset:45056\n\t" \
  "v_mfma_f32_16x16x32_bf16 v[52:55], v[200:203], v[44:47], v[52:55]\n\t" \
  "ds_read_b64_tr_b16 v[194:195], v238 offset:47104\n\t" \
  "s_waitcnt lgkmcnt(6)\n\t" \
  "v_mfma_f32_16x16x32_bf16 v[56:59], v[204:207], v[24:27], v[56:59]\n\t" \
  "ds_read_b64_tr_b16 v[196:197], v239 offset:36864\n\t" \
  "v_mfma_f32_16x16x32_bf16 v[56:59], v[208:211], v[32:35], v[56:59]\n\t" \
  "ds_read_b64_tr_b16 v[198:199], v239 offset:38912\n\t" \
  "v_mfma_f32_16x16x32_bf16 v[56:59], v[212:215], v[40:43], v[56:59]\n\t" \
  "ds_read_b64_tr_b16 v[200:201], v239 offset:40960\n\t" \
  "v_mfma_f32_16x16x32_bf16 v[60:63], v[204:207], v[28:31], v[60:63]\n\t" \
  "ds_read_b64_tr_b16 v[202:203], v239 offset:43008\n\t" \
  "v_mfma_f32_16x16x32_bf16 v[60:63], v[208:211], v[36:39], v[60:63]\n\t" \
  "ds_read_b64_tr_b16 v[204:205], v239 offset:45056\n\t" \
  "v_mfma_f32_16x16x32_bf16 v[60:63], v[212:215], v[44:47], v[60:63]\n\t" \
  "ds_read_b64_tr_b16 v[206:207], v239 offset:47104\n\t" \
  "s_waitcnt lgkmcnt(6)\n\t" \
  "v_mfma_f32_16x16x32_bf16 v[64:67], v[216:219], v[24:27], v[64:67]\n\t" \
  "ds_read_b64_tr_b16 v[208:209], v236 offset:49152\n\t" \
  "v_mfma_f32_16x16x32_bf16 v[64:67], v[220:223], v[32:35], v[64:67]\n\t" \
  "ds_read_b64_tr_b16 v[210:211], v236 offset:51200\n\t" \
  "v_mfma_f32_16x16x32_bf16 v[64:67], v[192:195], v[40:43], v[64:67]\n\t" \
  "ds_read_b64_tr_b16 v[212:213], v236 offset:53248\n\t" \
  "v_mfma_f32_16x16x32_bf16 v[68:71], v[216:219], v[28:31], v[68:71]\n\t" \
  "ds_read_b64_tr_b16 v[214:215], v236 offset:55296\n\t" \
  "v_mfma_f32_16x16x32_bf16 v[68:71], v[220:223], v[36:39], v[68:71]\n\t" \
  "ds_read_b64_tr_b16 v[216:217], v236 offset:57344\n\t" \
  "v_mfma_f32_16x16x32_bf16 v[68:71], v[192:195], v[44:47], v[68:71]\n\t" \
  "ds_read_b64_tr_b16 v[218:219], v236 offset:59392\n\t" \
  "s_waitcnt lgkmcnt(6)\n\t" \
  "v_mfma_f32_16x16x32_bf16 v[72:75], v[196:199], v[24:27], v[72:75]\n\t" \
  "ds_read_b64_tr_b16 v[220:221], v237 offset:49152\n\t" \
  "v_mfma_f32_16x16x32_bf16 v[72:75], v[200:203], v[32:35], v[72:75]\n\t" \
  "ds_read_b64_tr_b16 v[222:223], v237 offset:51200\n\t" \
  "v_mfma_f32_16x16x32_bf16 v[72:75], v[204:207], v[40:43], v[72:75]\n\t" \
  "ds_read_b64_tr_b16 v[192:193], v237 offset:53248\n\t" \
  "s_barrier\n\t" \
  "v_mfma_f32_16x16x32_bf16 v[76:79], v[196:199], v[28:31], v[76:79]\n\t" \
  "ds_read_b64_tr_b16 v[194:195], v237 offset:55296\n\t" \
  "v_mfma_f32_16x16x32_bf16 v[76:79], v[200:203], v[36:39], v[76:79]\n\t" \
  "ds_read_b64_tr_b16 v[196:197], v237 offset:57344\n\t" \
  "v_mfma_f32_16x16x32_bf16 v[76:79], v[204:207], v[44:47], v[76:79]\n\t" \
  "ds_read_b64_tr_b16 v[198:199], v237 offset:59392\n\t" \
  "s_waitcnt lgkmcnt(6)\n\t" \
  "v_mfma_f32_16x16x32_bf16 v[80:83], v[208:211], v[24:27], v[80:83]\n\t" \
  "ds_read_b64_tr_b16 v[200:201], v238 offset:49152\n\t" \
  "v_mfma_f32_16x16x32_bf16 v[80:83], v[212:215], v[32:35], v[80:83]\n\t" \
  "ds_read_b64_tr_b16 v[202:203], v238 offset:51200\n\t" \
  "v_mfma_f32_16x16x32_bf16 v[80:83], v[216:219], v[40:43], v[80:83]\n\t" \
  "ds_read_b64_tr_b16 v[204:205], v238 offset:53248\n\t" \
  "v_mfma_f32_16x16x32_bf16 v[84:87], v[208:211], v[28:31], v[84:87]\n\t" \
  "ds_read_b64_tr_b16 v[206:207], v238 offset:55296\n\t" \
  "v_mfma_f32_16x16x32_bf16 v[84:87], v[212:215], v[36:39], v[84:87]\n\t" \
  "ds_read_b64_tr_b16 v[208:209], v238 offset:57344\n\t" \
  "v_mfma_f32_16x16x32_bf16 v[84:87], v[216:219], v[44:47], v[84:87]\n\t" \
  "ds_read_b64_tr_b16 v[210:211], v238 offset:59392\n\t" \
  "s_waitcnt lgkmcnt(6)\n\t" \
  "v_mfma_f32_16x16x32_bf16 v[88:91], v[220:223], v[24:27], v[88:91]\n\t" \
  "ds_read_b64_tr_b16 v[212:213], v239 offset:49152\n\t" \
  "v_mfma_f32_16x16x32_bf16 v[88:91], v[192:195], v[32:35], v[88:91]\n\t" \
  "ds_read_b64_tr_b16 v[214:215], v239 offset:51200\n\t" \
  "v_mfma_f32_16x16x32_bf16 v[88:91], v[196:199], v[40:43], v[88:91]\n\t" \
  "ds_read_b64_tr_b16 v[216:217], v239 offset:53248\n\t" \
  "v_mfma_f32_16x16x32_bf16 v[92:95], v[220:223], v[28:31], v[92:95]\n\t" \
  "ds_read_b64_tr_b16 v[218:219], v239 offset:55296\n\t" \
  "ds_read_b128 v[0:3], v228 offset:12288\n\t" \
  "v_mfma_f32_16x16x32_bf16 v[92:95], v[192:195], v[36:39], v[92:95]\n\t" \
  "ds_read_b64_tr_b16 v[220:221], v239 offset:57344\n\t" \
  "v_mfma_f32_16x16x32_bf16 v[92:95], v[196:199], v[44:47], v[92:95]\n\t" \
  "ds_read_b64_tr_b16 v[222:223], v239 offset:59392\n\t" \
  "s_waitcnt lgkmcnt(7)\n\t" \
  "v_mfma_f32_16x16x32_bf16 v[96:99], v[200:203], v[24:27], v[96:99]\n\t" \
  "ds_read_b64_tr_b16 v[192:193], v232 offset:36864\n\t" \
  "v_mfma_f32_16x16x32_bf16 v[96:99], v[204:207], v[32:35], v[96:99]\n\t" \
  "ds_read_b64_tr_b16 v[194:195], v232 offset:38912\n\t" \
  "v_mfma_f32_16x16x32_bf16 v[96:99], v[208:211], v[40:43], v[96:99]\n\t" \
  "ds_read_b64_tr_b16 v[196:197], v232 offset:40960\n\t" \
  "ds_read_b128 v[4:7], v228 offset:13312\n\t" \
  "v_mfma_f32_16x16x32_bf16 v[100:103], v[200:203], v[28:31], v[100:103]\n\t" \
  "ds_read_b64_tr_b16 v[198:199], v232 offset:43008\n\t" \
  "v_mfma_f32_16x16x32_bf16 v[100:103], v[204:207], v[36:39], v[100:103]\n\t" \
  "ds_read_b64_tr_b16 v[200:201], v232 offset:45056\n\t" \
  "v_mfma_f32_16x16x32_bf16 v[100:103], v[208:211], v[44:47], v[100:103]\n\t" \
  "ds_read_b64_tr_b16 v[202:203], v232 offset:47104\n\t" \
  "s_waitcnt lgkmcnt(7)\n\t" \
  "v_mfma_f32_16x16x32_bf16 v[104:107], v[212:215], v[24:27], v[104:107]\n\t" \
  "ds_read_b64_tr_b16 v[204:205], v233 offset:36864\n\t" \
  "v_mfma_f32_16x16x32_bf16 v[104:107], v[216:219], v[32:35], v[104:107]\n\t" \
  "ds_read_b64_tr_b16 v[206:207], v233 offset:38912\n\t" \
  "ds_read_b128 v[8:11], v228 offset:14336\n\t" \
  "v_mfma_f32_16x16x32_bf16 v[104:107], v[220:223], v[40:43], v[104:107]\n\t" \
  "ds_read_b64_tr_b16 v[208:209], v233 offset:40960\n\t" \
  "v_mfma_f32_16x16x32_bf16 v[108:111], v[212:215], v[28:31], v[108:111]\n\t" \
  "ds_read_b64_tr_b16 v[210:211], v233 offset:43008\n\t" \
  "v_mfma_f32_16x16x32_bf16 v[108:111], v[216:219], v[36:39], v[108:111]\n\t" \
  "ds_read_b64_tr_b16 v[212:213], v233 offset:45056\n\t" \
  "v_mfma_f32_16x16x32_bf16 v[108:111], v[220:223], v[44:47], v[108:111]\n\t" \
  "ds_read_b64_tr_b16 v[214:215], v233 offset:47104\n\t" \
  "s_waitcnt lgkmcnt(7)\n\t" \
  "v_mfma_f32_16x16x32_bf16 v[112:115], v[192:195], v[24:27], v[112:115]\n\t" \
  "ds_read_b64_tr_b16 v[216:217], v234 offset:36864\n\t" \
  "ds_read_b128 v[12:15], v230 offset:12288\n\t" \
  "v_mfma_f32_16x16x32_bf16 v[112:115], v[196:199], v[32:35], v[112:115]\n\t" \
  "ds_read_b64_tr_b16 v[218:219], v234 offset:38912\n\t" \
  "v_mfma_f32_16x16x32_bf16 v[112:115], v[200:203], v[40:43], v[112:115]\n\t" \
  "ds_read_b64_tr_b16 v[220:221], v234 offset:40960\n\t" \
  "v_mfma_f32_16x16x32_bf16 v[116:119], v[192:195], v[28:31], v[116:119]\n\t" \
  "ds_read_b64_tr_b16 v[222:223], v234 offset:43008\n\t" \
  "v_mfma_f32_16x16x32_bf16 v[116:119], v[196:199], v[36:39], v[116:119]\n\t" \
  "ds_read_b64_tr_b16 v[192:193], v234 offset:45056\n\t" \
  "v_mfma_f32_16x16x32_bf16 v[116:119], v[200:203], v[44:47], v[116:119]\n\t" \
  "ds_read_b64_tr_b16 v[194:195], v234 offset:47104\n\t" \
  "ds_read_b128 v[16:19], v230 offset:13312\n\t" \
  "s_waitcnt lgkmcnt(8)\n\t" \
  "v_mfma_f32_16x16x32_bf16 v[120:123], v[204:207], v[24:27], v[120:123]\n\t" \
  "ds_read_b64_tr_b16 v[196:197], v235 offset:36864\n\t" \
  "v_mfma_f32_16x16x32_bf16 v[120:123], v[208:211], v[32:35], v[120:123]\n\t" \
  "ds_read_b64_tr_b16 v[198:199], v235 offset:38912\n\t" \
  "v_mfma_f32_16x16x32_bf16 v[120:123], v[212:215], v[40:43], v[120:123]\n\t" \
  "ds_read_b64_tr_b16 v[200:201], v235 offset:40960\n\t" \
  "s_barrier\n\t" \
  "v_mfma_f32_16x16x32_bf16 v[124:127], v[204:207], v[28:31], v[124:127]\n\t" \
  "ds_read_b64_tr_b16 v[202:203], v235 offset:43008\n\t" \
  "v_mfma_f32_16x16x32_bf16 v[124:127], v[208:211], v[36:39], v[124:127]\n\t" \
  "ds_read_b64_tr_b16 v[204:205], v235 offset:45056\n\t" \
  "ds_read_b128 v[20:23], v230 offset:14336\n\t" \
  "v_mfma_f32_16x16x32_bf16 v[124:127], v[212:215], v[44:47], v[124:127]\n\t" \
  "ds_read_b64_tr_b16 v[206:207], v235 offset:47104\n\t" \
  "s_waitcnt lgkmcnt(8)\n\t" \
  "v_mfma_f32_16x16x32_bf16 v[128:131], v[216:219], v[24:27], v[128:131]\n\t" \
  "ds_read_b64_tr_b16 v[208:209], v232 offset:49152\n\t" \
  "v_mfma_f32_16x16x32_bf16 v[128:131], v[220:223], v[32:35], v[128:131]\n\t" \
  "ds_read_b64_tr_b16 v[210:211], v232 offset:51200\n\t" \
  "v_mfma_f32_16x16x32_bf16 v[128:131], v[192:195], v[40:43], v[128:131]\n\t" \
  "ds_read_b64_tr_b16 v[212:213], v232 offset:53248\n\t" \
  "v_mfma_f32_16x16x32_bf16 v[132:135], v[216:219], v[28:31], v[132:135]\n\t" \
  "ds_read_b64_tr_b16 v[214:215], v232 offset:55296\n\t" \
  "v_mfma_f32_16x16x32_bf16 v[132:135], v[220:223], v[36:39], v[132:135]\n\t" \
  "ds_read_b64_tr_b16 v[216:217], v232 offset:57344\n\t" \
  "v_mfma_f32_16x16x32_bf16 v[132:135], v[192:195], v[44:47], v[132:135]\n\t" \
  "ds_read_b64_tr_b16 v[218:219], v232 offset:59392\n\t" \
  "s_waitcnt lgkmcnt(6)\n\t" \
  "v_mfma_f32_16x16x32_bf16 v[136:139], v[196:199], v[24:27], v[136:139]\n\t" \
  "ds_read_b64_tr_b16 v[220:221], v233 offset:49152\n\t" \
  "v_mfma_f32_16x16x32_bf16 v[136:139], v[200:203], v[32:35], v[136:139]\n\t" \
  "ds_read_b64_tr_b16 v[222:223], v233 offset:51200\n\t" \
  "v_mfma_f32_16x16x32_bf16 v[136:139], v[204:207], v[40:43], v[136:139]\n\t" \
  "ds_read_b64_tr_b16 v[192:193], v233 offset:53248\n\t" \
  "v_mfma_f32_16x16x32_bf16 v[140:143], v[196:199], v[28:31], v[140:143]\n\t" \
  "ds_read_b64_tr_b16 v[194:195], v233 offset:55296\n\t" \
  "v_mfma_f32_16x16x32_bf16 v[140:143], v[200:203], v[36:39], v[140:143]\n\t" \
  "ds_read_b64_tr_b16 v[196:197], v233 offset:57344\n\t" \
  "v_mfma_f32_16x16x32_bf16 v[140:143], v[204:207], v[44:47], v[140:143]\n\t" \
  "ds_read_b64_tr_b16 v[198:199], v233 offset:59392\n\t" \
  "s_waitcnt lgkmcnt(6)\n\t" \
  "v_mfma_f32_16x16x32_bf16 v[80:83], v[208:211], v[0:3], v[80:83]\n\t" \
  "ds_read_b64_tr_b16 v[200:201], v234 offset:49152\n\t" \
  "v_mfma_f32_16x16x32_bf16 v[80:83], v[212:215], v[8:11], v[80:83]\n\t" \
  "ds_read_b64_tr_b16 v[202:203], v234 offset:51200\n\t" \
  "v_mfma_f32_16x16x32_bf16 v[80:83], v[216:219], v[16:19], v[80:83]\n\t" \
  "ds_read_b64_tr_b16 v[204:205], v234 offset:53248\n\t" \
  "v_mfma_f32_16x16x32_bf16 v[84:87], v[208:211], v[4:7], v[84:87]\n\t" \
  "ds_read_b64_tr_b16 v[206:207], v234 offset:55296\n\t" \
  "v_mfma_f32_16x16x32_bf16 v[84:87], v[212:215], v[12:15], v[84:87]\n\t" \
  "ds_read_b64_tr_b16 v[208:209], v234 offset:57344\n\t" \
  "v_mfma_f32_16x16x32_bf16 v[84:87], v[216:219], v[20:23], v[84:87]\n\t" \
  "ds_read_b64_tr_b16 v[210:211], v234 offset:59392\n\t" \
  "s_waitcnt lgkmcnt(6)\n\t" \
  "v_mfma_f32_16x16x32_bf16 v[88:91], v[220:223], v[0:3], v[88:91]\n\t" \
  "ds_read_b64_tr_b16 v[212:213], v235 offset:49152\n\t" \
  "v_mfma_f32_16x16x32_bf16 v[88:91], v[192:195], v[8:11], v[88:91]\n\t" \
  "ds_read_b64_tr_b16 v[214:215], v235 offset:51200\n\t" \
  "v_mfma_f32_16x16x32_bf16 v[88:91], v[196:199], v[16:19], v[88:91]\n\t" \
  "ds_read_b64_tr_b16 v[216:217], v235 offset:53248\n\t" \
  "v_mfma_f32_16x16x32_bf16 v[92:95], v[220:223], v[4:7], v[92:95]\n\t" \
  "ds_read_b64_tr_b16 v[218:219], v235 offset:55296\n\t" \
  "v_mfma_f32_16x16x32_bf16 v[92:95], v[192:195], v[12:15], v[92:95]\n\t" \
  "ds_read_b64_tr_b16 v[220:221], v235 offset:57344\n\t" \
  "v_mfma_f32_16x16x32_bf16 v[92:95], v[196:199], v[20:23], v[92:95]\n\t" \
  "ds_read_b64_tr_b16 v[222:223], v235 offset:59392\n\t" \
  "s_waitcnt lgkmcnt(6)\n\t" \
  "v_mfma_f32_16x16x32_bf16 v[96:99], v[200:203], v[0:3], v[96:99]\n\t" \
  "ds_read_b64_tr_b16 v[192:193], v240 offset:0\n\t" \
  "v_mfma_f32_16x16x32_bf16 v[96:99], v[204:207], v[8:11], v[96:99]\n\t" \
  "ds_read_b64_tr_b16 v[194:195], v240 offset:2048\n\t" \
  "v_mfma_f32_16x16x32_bf16 v[96:99], v[208:211], v[16:19], v[96:99]\n\t" \
  "ds_read_b64_tr_b16 v[196:197], v240 offset:4096\n\t" \
  "v_mfma_f32_16x16x32_bf16 v[100:103], v[200:203], v[4:7], v[100:103]\n\t" \
  "ds_read_b64_tr_b16 v[198:199], v240 offset:6144\n\t" \
  "v_mfma_f32_16x16x32_bf16 v[100:103], v[204:207], v[12:15], v[100:103]\n\t" \
  "ds_read_b64_tr_b16 v[200:201], v240 offset:8192\n\t" \
  "v_mfma_f32_16x16x32_bf16 v[100:103], v[208:211], v[20:23], v[100:103]\n\t" \
  "ds_read_b64_tr_b16 v[202:203], v240 offset:10240\n\t" \
  "s_waitcnt lgkmcnt(6)\n\t" \
  "v_mfma_f32_16x16x32_bf16 v[104:107], v[212:215], v[0:3], v[104:107]\n\t" \
  "ds_read_b64_tr_b16 v[204:205], v241 offset:0\n\t" \
  "v_mfma_f32_16x16x32_bf16 v[104:107], v[216:219], v[8:11], v[104:107]\n\t" \
  "ds_read_b64_tr_b16 v[206:207], v241 offset:2048\n\t" \
  "v_mfma_f32_16x16x32_bf16 v[104:107], v[220:223], v[16:19], v[104:107]\n\t" \
  "ds_read_b64_tr_b16 v[208:209], v241 offset:4096\n\t" \
  "s_barrier\n\t" \
  "v_mfma_f32_16x16x32_bf16 v[108:111], v[212:215], v[4:7], v[108:111]\n\t" \
  "ds_read_b64_tr_b16 v[210:211], v241 offset:6144\n\t" \
  "v_mfma_f32_16x16x32_bf16 v[108:111], v[216:219], v[12:15], v[108:111]\n\t" \
  "ds_read_b64_tr_b16 v[212:213], v241 offset:8192\n\t" \
  "v_mfma_f32_16x16x32_bf16 v[108:111], v[220:223], v[20:23], v[108:111]\n\t" \
  "ds_read_b64_tr_b16 v[214:215], v241 offset:10240\n\t" \
  "s_waitcnt lgkmcnt(6)\n\t" \
  "v_mfma_f32_16x16x32_bf16 v[112:115], v[192:195], v[0:3], v[112:115]\n\t" \
  "ds_read_b64_tr_b16 v[216:217], v242 offset:0\n\t" \
  "v_mfma_f32_16x16x32_bf16 v[112:115], v[196:199], v[8:11], v[112:115]\n\t" \
  "ds_read_b64_tr_b16 v[218:219], v242 offset:2048\n\t" \
  "v_mfma_f32_16x16x32_bf16 v[112:115], v[200:203], v[16:19], v[112:115]\n\t" \
  "ds_read_b64_tr_b16 v[220:221], v242 offset:4096\n\t" \
  "v_mfma_f32_16x16x32_bf16 v[116:119], v[192:195], v[4:7], v[116:119]\n\t" \
  "ds_read_b64_tr_b16 v[222:223], v242 offset:6144\n\t" \
  "v_mfma_f32_16x16x32_bf16 v[116:119], v[196:199], v[12:15], v[116:119]\n\t" \
  "ds_read_b64_tr_b16 v[192:193], v242 offset:8192\n\t" \
  "v_mfma_f32_16x16x32_bf16 v[116:119], v[200:203], v[20:23], v[116:119]\n\t" \
  "ds_read_b64_tr_b16 v[194:195], v242 offset:10240\n\t" \
  "s_waitcnt lgkmcnt(6)\n\t" \
  "v_mfma_f32_16x16x32_bf16 v[120:123], v[204:207], v[0:3], v[120:123]\n\t" \
  "ds_read_b64_tr_b16 v[196:197], v243 offset:0\n\t" \
  "v_mfma_f32_16x16x32_bf16 v[120:123], v[208:211], v[8:11], v[120:123]\n\t" \
  "ds_read_b64_tr_b16 v[198:199], v243 offset:2048\n\t" \
  "v_mfma_f32_16x16x32_bf16 v[120:123], v[212:215], v[16:19], v[120:123]\n\t" \
  "ds_read_b64_tr_b16 v[200:201], v243 offset:4096\n\t" \
  "v_mfma_f32_16x16x32_bf16 v[124:127], v[204:207], v[4:7], v[124:127]\n\t" \
  "ds_read_b64_tr_b16 v[202:203], v243 offset:6144\n\t" \
  "v_mfma_f32_16x16x32_bf16 v[124:127], v[208:211], v[12:15], v[124:127]\n\t" \
  "ds_read_b64_tr_b16 v[204:205], v243 offset:8192\n\t" \
  "v_mfma_f32_16x16x32_bf16 v[124:127], v[212:215], v[20:23], v[124:127]\n\t" \
  "ds_read_b64_tr_b16 v[206:207], v243 offset:10240\n\t" \
  "s_waitcnt lgkmcnt(6)\n\t" \
  "v_mfma_f32_16x16x32_bf16 v[128:131], v[216:219], v[0:3], v[128:131]\n\t" \
  "ds_read_b64_tr_b16 v[208:209], v232 offset:24576\n\t" \
  "v_mfma_f32_16x16x32_bf16 v[128:131], v[220:223], v[8:11], v[128:131]\n\t" \
  "ds_read_b64_tr_b16 v[210:211], v232 offset:26624\n\t" \
  "v_mfma_f32_16x16x32_bf16 v[128:131], v[192:195], v[16:19], v[128:131]\n\t" \
  "ds_read_b64_tr_b16 v[212:213], v232 offset:28672\n\t" \
  "v_mfma_f32_16x16x32_bf16 v[132:135], v[216:219], v[4:7], v[132:135]\n\t" \
  "ds_read_b64_tr_b16 v[214:215], v232 offset:30720\n\t" \
  "v_mfma_f32_16x16x32_bf16 v[132:135], v[220:223], v[12:15], v[132:135]\n\t" \
  "ds_read_b64_tr_b16 v[216:217], v232 offset:32768\n\t" \
  "v_mfma_f32_16x16x32_bf16 v[132:135], v[192:195], v[20:23], v[132:135]\n\t" \
  "ds_read_b64_tr_b16 v[218:219], v232 offset:34816\n\t" \
  "s_waitcnt lgkmcnt(6)\n\t" \
  "v_mfma_f32_16x16x32_bf16 v[136:139], v[196:199], v[0:3], v[136:139]\n\t" \
  "ds_read_b64_tr_b16 v[220:221], v233 offset:24576\n\t" \
  "v_mfma_f32_16x16x32_bf16 v[136:139], v[200:203], v[8:11], v[136:139]\n\t" \
  "ds_read_b64_tr_b16 v[222:223], v233 offset:26624\n\t" \
  "v_mfma_f32_16x16x32_bf16 v[136:139], v[204:207], v[16:19], v[136:139]\n\t" \
  "ds_read_b64_tr_b16 v[192:193], v233 offset:28672\n\t" \
  "v_mfma_f32_16x16x32_bf16 v[140:143], v[196:199], v[4:7], v[140:143]\n\t" \
  "ds_read_b64_tr_b16 v[194:195], v233 offset:30720\n\t" \
  "v_mfma_f32_16x16x32_bf16 v[140:143], v[200:203], v[12:15], v[140:143]\n\t" \
  "ds_read_b64_tr_b16 v[196:197], v233 offset:32768\n\t" \
  "v_mfma_f32_16x16x32_bf16 v[140:143], v[204:207], v[20:23], v[140:143]\n\t" \
  "ds_read_b64_tr_b16 v[198:199], v233 offset:34816\n\t" \
  "s_waitcnt lgkmcnt(6)\n\t" \
  "v_mfma_f32_16x16x32_bf16 v[48:51], v[208:211], v[144:147], v[48:51]\n\t" \
  "ds_read_b64_tr_b16 v[200:201], v234 offset:24576\n\t" \
  "v_mfma_f32_16x16x32_bf16 v[48:51], v[212:215], v[160:163], v[48:51]\n\t" \
  "ds_read_b64_tr_b16 v[202:203], v234 offset:26624\n\t" \
  "v_mfma_f32_16x16x32_bf16 v[48:51], v[216:219], v[176:179], v[48:51]\n\t" \
  "ds_read_b64_tr_b16 v[204:205], v234 offset:28672\n\t" \
  "v_mfma_f32_16x16x32_bf16 v[52:55], v[208:211], v[148:151], v[52:55]\n\t" \
  "ds_read_b64_tr_b16 v[206:207], v234 offset:30720\n\t" \
  "v_mfma_f32_16x16x32_bf16 v[52:55], v[212:215], v[164:167], v[52:55]\n\t" \
  "ds_read_b64_tr_b16 v[208:209], v234 offset:32768\n\t" \
  "v_mfma_f32_16x16x32_bf16 v[52:55], v[216:219], v[180:183], v[52:55]\n\t" \
  "ds_read_b64_tr_b16 v[210:211], v234 offset:34816\n\t" \
  "s_waitcnt lgkmcnt(6)\n\t" \
  "v_mfma_f32_16x16x32_bf16 v[56:59], v[220:223], v[144:147], v[56:59]\n\t" \
  "ds_read_b64_tr_b16 v[212:213], v235 offset:24576\n\t" \
  "v_mfma_f32_16x16x32_bf16 v[56:59], v[192:195], v[160:163], v[56:59]\n\t" \
  "ds_read_b64_tr_b16 v[214:215], v235 offset:26624\n\t" \
  "v_mfma_f32_16x16x32_bf16 v[56:59], v[196:199], v[176:179], v[56:59]\n\t" \
  "ds_read_b64_tr_b16 v[216:217], v235 offset:28672\n\t" \
  "s_barrier\n\t" \
  "v_mfma_f32_16x16x32_bf16 v[60:63], v[220:223], v[148:151], v[60:63]\n\t" \
  "ds_read_b64_tr_b16 v[218:219], v235 offset:30720\n\t" \
  "ds_read_b128 v[24:27], v229 offset:0\n\t" \
  "v_cvt_pk_bf16_f32 v48, v48, v49\n\t" \
  "v_mfma_f32_16x16x32_bf16 v[60:63], v[192:195], v[164:167], v[60:63]\n\t" \
  "ds_read_b64_tr_b16 v[220:221], v235 offset:32768\n\t" \
  "v_cvt_pk_bf16_f32 v49, v50, v51\n\t" \
  "v_mfma_f32_16x16x32_bf16 v[60:63], v[196:199], v[180:183], v[60:63]\n\t" \
  "ds_read_b64_tr_b16 v[222:223], v235 offset:34816\n\t" \
  "v_cvt_pk_bf16_f32 v52, v52, v53\n\t" \
  "s_waitcnt lgkmcnt(7)\n\t" \
  "v_mfma_f32_16x16x32_bf16 v[64:67], v[200:203], v[144:147], v[64:67]\n\t" \
  "ds_read_b64_tr_b16 v[192:193], v240 offset:24576\n\t" \
  "v_cvt_pk_bf16_f32 v53, v54, v55\n\t" \
  "v_mfma_f32_16x16x32_bf16 v[64:67], v[204:207], v[160:163], v[64:67]\n\t" \
  "ds_read_b64_tr_b16 v[194:195], v240 offset:26624\n\t" \
  "v_mfma_f32_16x16x32_bf16 v[64:67], v[208:211], v[176:179], v[64:67]\n\t" \
  "ds_read_b64_tr_b16 v[196:197], v240 offset:28672\n\t" \
  "ds_read_b128 v[28:31], v229 offset:1024\n\t" \
  "v_mfma_f32_16x16x32_bf16 v[68:71], v[200:203], v[148:151], v[68:71]\n\t" \
  "ds_read_b64_tr_b16 v[198:199], v240 offset:30720\n\t" \
  "v_mfma_f32_16x16x32_bf16 v[68:71], v[204:207], v[164:167], v[68:71]\n\t" \
  "ds_read_b64_tr_b16 v[200:201], v240 offset:32768\n\t" \
  "v_mfma_f32_16x16x32_bf16 v[68:71], v[208:211], v[180:183], v[68:71]\n\t" \
  "ds_read_b64_tr_b16 v[202:203], v240 offset:34816\n\t" \
  "v_cvt_pk_bf16_f32 v50, v56, v57\n\t" \
  "s_waitcnt lgkmcnt(7)\n\t" \
  "v_mfma_f32_16x16x32_bf16 v[72:75], v[212:215], v[144:147], v[72:75]\n\t" \
  "ds_read_b64_tr_b16 v[204:205], v241 offset:24576\n\t" \
  "v_cvt_pk_bf16_f32 v51, v58, v59\n\t" \
  "v_mfma_f32_16x16x32_bf16 v[72:75], v[216:219], v[160:163], v[72:75]\n\t" \
  "ds_read_b64_tr_b16 v[206:207], v241 offset:26624\n\t" \
  "ds_read_b128 v[32:35], v229 offset:2048\n\t" \
  "v_cvt_pk_bf16_f32 v54, v60, v61\n\t" \
  "v_mfma_f32_16x16x32_bf16 v[72:75], v[220:223], v[176:179], v[72:75]\n\t" \
  "ds_read_b64_tr_b16 v[208:209], v241 offset:28672\n\t" \
  "v_cvt_pk_bf16_f32 v55, v62, v63\n\t" \
  "v_mfma_f32_16x16x32_bf16 v[76:79], v[212:215], v[148:151], v[76:79]\n\t" \
  "ds_read_b64_tr_b16 v[210:211], v241 offset:30720\n\t" \
  "s_add_u32 s75, %[sp4], 0\n\t" \
  "buffer_store_dwordx4 v[48:51], v244, s[48:51], s75 offen sc1\n\t" \
  "v_mfma_f32_16x16x32_bf16 v[76:79], v[216:219], v[164:167], v[76:79]\n\t" \
  "ds_read_b64_tr_b16 v[212:213], v241 offset:32768\n\t" \
  "v_mfma_f32_16x16x32_bf16 v[76:79], v[220:223], v[180:183], v[76:79]\n\t" \
  "ds_read_b64_tr_b16 v[214:215], v241 offset:34816\n\t" \
  "s_add_u32 s75, %[sp4], 16384\n\t" \
  "buffer_store_dwordx4 v[52:55], v244, s[48:51], s75 offen sc1\n\t" \
  "s_waitcnt lgkmcnt(7)\n\t" \
  "v_mfma_f32_16x16x32_bf16 v[80:83], v[192:195], v[144:147], v[80:83]\n\t" \
  "ds_read_b64_tr_b16 v[216:217], v242 offset:24576\n\t" \
  "ds_read_b128 v[36:39], v228 offset:0\n\t" \
  "v_add3_u32 v56, v48, v49, %[ep]\n\t" \
  "v_mfma_f32_16x16x32_bf16 v[80:83], v[196:199], v[160:163], v[80:83]\n\t" \
  "ds_read_b64_tr_b16 v[218:219], v242 offset:26624\n\t" \
  "v_add3_u32 v56, v56, v50, v51\n\t" \
  "v_cvt_pk_bf16_f32 v64, v64, v65\n\t" \
  "v_mfma_f32_16x16x32_bf16 v[80:83], v[200:203], v[176:179], v[80:83]\n\t" \
  "ds_read_b64_tr_b16 v[220:221], v242 offset:28672\n\t" \
  "v_add3_u32 v57, v52, v53, %[ep]\n\t" \
  "v_cvt_pk_bf16_f32 v65, v66, v67\n\t" \
  "v_mfma_f32_16x16x32_bf16 v[84:87], v[192:195], v[148:151], v[84:87]\n\t" \
  "ds_read_b64_tr_b16 v[222:223], v242 offset:30720\n\t" \
  "v_add3_u32 v57, v57, v54, v55\n\t" \
  "v_cvt_pk_bf16_f32 v68, v68, v69\n\t" \
  "v_mfma_f32_16x16x32_bf16 v[84:87], v[196:199], v[164:167], v[84:87]\n\t" \
  "ds_read_b64_tr_b16 v[192:193], v242 offset:32768\n\t" \
  "v_cvt_pk_bf16_f32 v69, v70, v71\n\t" \
  "v_mfma_f32_16x16x32_bf16 v[84:87], v[200:203], v[180:183], v[84:87]\n\t" \
  "ds_read_b64_tr_b16 v[194:195], v242 offset:34816\n\t" \
  "ds_read_b128 v[40:43], v228 offset:1024\n\t" \
  "s_waitcnt lgkmcnt(8)\n\t" \
  "v_mfma_f32_16x16x32_bf16 v[88:91], v[204:207], v[144:147], v[88:91]\n\t" \
  "ds_read_b64_tr_b16 v[196:197], v243 offset:24576\n\t" \
  "v_mfma_f32_16x16x32_bf16 v[88:91], v[208:211], v[160:163], v[88:91]\n\t" \
  "ds_read_b64_tr_b16 v[198:199], v243 offset:26624\n\t" \
  "v_mfma_f32_16x16x32_bf16 v[88:91], v[212:215], v[176:179], v[88:91]\n\t" \
  "ds_read_b64_tr_b16 v[200:201], v243 offset:28672\n\t" \
  "v_mfma_f32_16x16x32_bf16 v[92:95], v[204:207], v[148:151], v[92:95]\n\t" \
  "ds_read_b64_tr_b16 v[202:203], v243 offset:30720\n\t" \
  "ds_read_b128 v[0:3], v229 offset:12288\n\t" \
  "v_cvt_pk_bf16_f32 v66, v72, v73\n\t" \
  "v_mfma_f32_16x16x32_bf16 v[92:95], v[208:211], v[164:167], v[92:95]\n\t" \
  "ds_read_b64_tr_b16 v[204:205], v243 offset:32768\n\t" \
  "ds_read_b128 v[44:47], v228 offset:2048\n\t" \
  "v_cvt_pk_bf16_f32 v67, v74, v75\n\t" \
  "v_mfma_f32_16x16x32_bf16 v[92:95], v[212:215], v[180:183], v[92:95]\n\t" \
  "s_waitcnt lgkmcnt(14)\n\t" \
  "ds_read_b64_tr_b16 v[206:207], v243 offset:34816\n\t" \
  "v_cvt_pk_bf16_f32 v70, v76, v77\n\t" \
  "s_waitcnt lgkmcnt(9)\n\t" \
  "v_mfma_f32_16x16x32_bf16 v[96:99], v[216:219], v[144:147], v[96:99]\n\t" \
  "ds_read_b64_tr_b16 v[208:209], v236 offset:24576\n\t" \
  "v_cvt_pk_bf16_f32 v71, v78, v79\n\t" \
  "v_mfma_f32_16x16x32_bf16 v[96:99], v[220:223], v[160:163], v[96:99]\n\t" \
  "ds_read_b64_tr_b16 v[210:211], v236 offset:26624\n\t" \
  "s_add_u32 s75, %[sp4], 32768\n\t" \
  "buffer_store_dwordx4 v[64:67], v244, s[48:51], s75 offen sc1\n\t" \
  "v_mfma_f32_16x16x32_bf16 v[96:99], v[192:195], v[176:179], v[96:99]\n\t" \
  "ds_read_b64_tr_b16 v[212:213], v236 offset:28672\n\t" \
  "ds_read_b128 v[4:7], v229 offset:13312\n\t" \
  "v_mfma_f32_16x16x32_bf16 v[100:103], v[216:219], v[148:151], v[100:103]\n\t" \
  "ds_read_b64_tr_b16 v[214:215], v236 offset:30720\n\t" \
  "s_add_u32 s75, %[sp4], 49152\n\t" \
  "buffer_store_dwordx4 v[68:71], v244, s[48:51], s75 offen sc1\n\t" \
  "v_mfma_f32_16x16x32_bf16 v[100:103], v[220:223], v[164:167], v[100:103]\n\t" \
  "ds_read_b64_tr_b16 v[216:217], v236 offset:32768\n\t" \
  "v_add3_u32 v58, v64, v65, %[ep]\n\t" \
  "v_mfma_f32_16x16x32_bf16 v[100:103], v[192:195], v[180:183], v[100:103]\n\t" \
  "s_waitcnt lgkmcnt(14)\n\t" \
  "ds_read_b64_tr_b16 v[218:219], v236 offset:34816\n\t" \
  "v_add3_u32 v58, v58, v66, v67\n\t" \
  "s_waitcnt lgkmcnt(7)\n\t" \
  "v_mfma_f32_16x16x32_bf16 v[104:107], v[196:199], v[144:147], v[104:107]\n\t" \
  "ds_read_b64_tr_b16 v[220:221], v237 offset:24576\n\t" \
  "v_add3_u32 v59, v68, v69, %[ep]\n\t" \
  "v_mfma_f32_16x16x32_bf16 v[104:107], v[200:203], v[160:163], v[104:107]\n\t" \
  "ds_read_b64_tr_b16 v[222:223], v237 offset:26624\n\t" \
  "ds_read_b128 v[8:11], v229 offset:14336\n\t" \
  "v_add3_u32 v59, v59, v70, v71\n\t" \
  "v_mfma_f32_16x16x32_bf16 v[104:107], v[204:207], v[176:179], v[104:107]\n\t" \
  "ds_read_b64_tr_b16 v[192:193], v237 offset:28672\n\t" \
  "buffer_store_dwordx4 v[56:59], v244, s[48:51], %[sc4] offen sc1\n\t" \
  "s_barrier\n\t" \
  "v_mfma_f32_16x16x32_bf16 v[108:111], v[196:199], v[148:151], v[108:111]\n\t" \
  "ds_read_b64_tr_b16 v[194:195], v237 offset:30720\n\t" \
  "v_mfma_f32_16x16x32_bf16 v[108:111], v[200:203], v[164:167], v[108:111]\n\t" \
  "ds_read_b64_tr_b16 v[196:197], v237 offset:32768\n\t" \
  "v_mfma_f32_16x16x32_bf16 v[108:111], v[204:207], v[180:183], v[108:111]\n\t" \
  "ds_read_b64_tr_b16 v[198:199], v237 offset:34816\n\t" \
  "s_waitcnt lgkmcnt(7)\n\t" \
  "v_mfma_f32_16x16x32_bf16 v[80:83], v[208:211], v[24:27], v[80:83]\n\t" \
  "ds_read_b64_tr_b16 v[200:201], v238 offset:24576\n\t" \
  "ds_read_b128 v[12:15], v229 offset:36864\n\t" \
  "v_mfma_f32_16x16x32_bf16 v[80:83], v[212:215], v[32:35], v[80:83]\n\t" \
  "ds_read_b64_tr_b16 v[202:203], v238 offset:26624\n\t" \
  "v_mfma_f32_16x16x32_bf16 v[80:83], v[216:219], v[40:43], v[80:83]\n\t" \
  "ds_read_b64_tr_b16 v[204:205], v238 offset:28672\n\t" \
  "v_mfma_f32_16x16x32_bf16 v[84:87], v[208:211], v[28:31], v[84:87]\n\t" \
  "ds_read_b64_tr_b16 v[206:207], v238 offset:30720\n\t" \
  "v_mfma_f32_16x16x32_bf16 v[84:87], v[212:215], v[36:39], v[84:87]\n\t" \
  "ds_read_b64_tr_b16 v[208:209], v238 offset:32768\n\t" \
  "v_mfma_f32_16x16x32_bf16 v[84:87], v[216:219], v[44:47], v[84:87]\n\t" \
  "ds_read_b64_tr_b16 v[210:211], v238 offset:34816\n\t" \
  "ds_read_b128 v[16:19], v229 offset:37888\n\t" \
  "s_waitcnt lgkmcnt(8)\n\t" \
  "v_mfma_f32_16x16x32_bf16 v[88:91], v[220:223], v[24:27], v[88:91]\n\t" \
  "ds_read_b64_tr_b16 v[212:213], v239 offset:24576\n\t" \
  "v_mfma_f32_16x16x32_bf16 v[88:91], v[192:195], v[32:35], v[88:91]\n\t" \
  "ds_read_b64_tr_b16 v[214:215], v239 offset:26624\n\t" \
  "v_mfma_f32_16x16x32_bf16 v[88:91], v[196:199], v[40:43], v[88:91]\n\t" \
  "ds_read_b64_tr_b16 v[216:217], v239 offset:28672\n\t" \
  "v_mfma_f32_16x16x32_bf16 v[92:95], v[220:223], v[28:31], v[92:95]\n\t" \
  "ds_read_b64_tr_b16 v[218:219], v239 offset:30720\n\t" \
  "v_mfma_f32_16x16x32_bf16 v[92:95], v[192:195], v[36:39], v[92:95]\n\t" \
  "ds_read_b64_tr_b16 v[220:221], v239 offset:32768\n\t" \
  "ds_read_b128 v[20:23], v229 offset:38912\n\t" \
  "v_mfma_f32_16x16x32_bf16 v[92:95], v[196:199], v[44:47], v[92:95]\n\t" \
  "ds_read_b64_tr_b16 v[222:223], v239 offset:34816\n\t" \
  "s_waitcnt lgkmcnt(8)\n\t" \
  "v_mfma_f32_16x16x32_bf16 v[96:99], v[200:203], v[24:27], v[96:99]\n\t" \
  "ds_read_b64_tr_b16 v[192:193], v236 offset:49152\n\t" \
  "v_mfma_f32_16x16x32_bf16 v[96:99], v[204:207], v[32:35], v[96:99]\n\t" \
  "ds_read_b64_tr_b16 v[194:195], v236 offset:51200\n\t" \
  "v_mfma_f32_16x16x32_bf16 v[96:99], v[208:211], v[40:43], v[96:99]\n\t" \
  "ds_read_b64_tr_b16 v[196:197], v236 offset:53248\n\t" \
  "v_mfma_f32_16x16x32_bf16 v[100:103], v[200:203], v[28:31], v[100:103]\n\t" \
  "ds_read_b64_tr_b16 v[198:199], v236 offset:55296\n\t" \
  "v_mfma_f32_16x16x32_bf16 v[100:103], v[204:207], v[36:39], v[100:103]\n\t" \
  "ds_read_b64_tr_b16 v[200:201], v236 offset:57344\n\t" \
  "v_mfma_f32_16x16x32_bf16 v[100:103], v[208:211], v[44:47], v[100:103]\n\t" \
  "ds_read_b64_tr_b16 v[202:203], v236 offset:59392\n\t" \
  "s_waitcnt lgkmcnt(6)\n\t" \
  "v_mfma_f32_16x16x32_bf16 v[104:107], v[212:215], v[24:27], v[104:107]\n\t" \
  "ds_read_b64_tr_b16 v[204:205], v237 offset:49152\n\t" \
  "v_mfma_f32_16x16x32_bf16 v[104:107], v[216:219], v[32:35], v[104:107]\n\t" \
  "ds_read_b64_tr_b16 v[206:207], v237 offset:51200\n\t" \
  "v_mfma_f32_16x16x32_bf16 v[104:107], v[220:223], v[40:43], v[104:107]\n\t" \
  "ds_read_b64_tr_b16 v[208:209], v237 offset:53248\n\t" \
  "v_mfma_f32_16x16x32_bf16 v[108:111], v[212:215], v[28:31], v[108:111]\n\t" \
  "ds_read_b64_tr_b16 v[210:211], v237 offset:55296\n\t" \
  "v_mfma_f32_16x16x32_bf16 v[108:111], v[216:219], v[36:39], v[108:111]\n\t" \
  "ds_read_b64_tr_b16 v[212:213], v237 offset:57344\n\t" \
  "v_mfma_f32_16x16x32_bf16 v[108:111], v[220:223], v[44:47], v[108:111]\n\t" \
  "ds_read_b64_tr_b16 v[214:215], v237 offset:59392\n\t" \
  "s_waitcnt lgkmcnt(6)\n\t" \
  "v_mfma_f32_16x16x32_bf16 v[80:83], v[192:195], v[0:3], v[80:83]\n\t" \
  "ds_read_b64_tr_b16 v[216:217], v238 offset:49152\n\t" \
  "v_mfma_f32_16x16x32_bf16 v[80:83], v[196:199], v[8:11], v[80:83]\n\t" \
  "ds_read_b64_tr_b16 v[218:219], v238 offset:51200\n\t" \
  "v_mfma_f32_16x16x32_bf16 v[80:83], v[200:203], v[16:19], v[80:83]\n\t" \
  "ds_read_b64_tr_b16 v[220:221], v238 offset:53248\n\t" \
  "v_mfma_f32_16x16x32_bf16 v[84:87], v[192:195], v[4:7], v[84:87]\n\t" \
  "ds_read_b64_tr_b16 v[222:223], v238 offset:55296\n\t" \
  "v_mfma_f32_16x16x32_bf16 v[84:87], v[196:199], v[12:15], v[84:87]\n\t" \
  "ds_read_b64_tr_b16 v[192:193], v238 offset:57344\n\t" \
  "v_mfma_f32_16x16x32_bf16 v[84:87], v[200:203], v[20:23], v[84:87]\n\t" \
  "ds_read_b64_tr_b16 v[194:195], v238 offset:59392\n\t" \
  "s_waitcnt lgkmcnt(6)\n\t" \
  "v_mfma_f32_16x16x32_bf16 v[88:91], v[204:207], v[0:3], v[88:91]\n\t" \
  "ds_read_b64_tr_b16 v[196:197], v239 offset:49152\n\t" \
  "v_mfma_f32_16x16x32_bf16 v[88:91], v[208:211], v[8:11], v[88:91]\n\t" \
  "ds_read_b64_tr_b16 v[198:199], v239 offset:51200\n\t" \
  "v_mfma_f32_16x16x32_bf16 v[88:91], v[212:215], v[16:19], v[88:91]\n\t" \
  "ds_read_b64_tr_b16 v[200:201], v239 offset:53248\n\t" \
  "s_barrier\n\t" \
  "v_mfma_f32_16x16x32_bf16 v[92:95], v[204:207], v[4:7], v[92:95]\n\t" \
  "ds_read_b64_tr_b16 v[202:203], v239 offset:55296\n\t" \
  "v_cvt_pk_bf16_f32 v80, v80, v81\n\t" \
  "v_mfma_f32_16x16x32_bf16 v[92:95], v[208:211], v[12:15], v[92:95]\n\t" \
  "ds_read_b64_tr_b16 v[204:205], v239 offset:57344\n\t" \
  "v_cvt_pk_bf16_f32 v81, v82, v83\n\t" \
  "v_mfma_f32_16x16x32_bf16 v[92:95], v[212:215], v[20:23], v[92:95]\n\t" \
  "ds_read_b64_tr_b16 v[206:207], v239 offset:59392\n\t" \
  "v_cvt_pk_bf16_f32 v84, v84, v85\n\t" \
  "s_waitcnt lgkmcnt(6)\n\t" \
  "v_mfma_f32_16x16x32_bf16 v[96:99], v[216:219], v[0:3], v[96:99]\n\t" \
  "ds_read_b64_tr_b16 v[208:209], v232 offset:12288\n\t" \
  "v_cvt_pk_bf16_f32 v85, v86, v87\n\t" \
  "v_mfma_f32_16x16x32_bf16 v[96:99], v[220:223], v[8:11], v[96:99]\n\t" \
  "ds_read_b64_tr_b16 v[210:211], v232 offset:14336\n\t" \
  "v_mfma_f32_16x16x32_bf16 v[96:99], v[192:195], v[16:19], v[96:99]\n\t" \
  "ds_read_b64_tr_b16 v[212:213], v232 offset:16384\n\t" \
  "v_mfma_f32_16x16x32_bf16 v[100:103], v[216:219], v[4:7], v[100:103]\n\t" \
  "ds_read_b64_tr_b16 v[214:215], v232 offset:18432\n\t" \
  "v_mfma_f32_16x16x32_bf16 v[100:103], v[220:223], v[12:15], v[100:103]\n\t" \
  "ds_read_b64_tr_b16 v[216:217], v232 offset:20480\n\t" \
  "v_mfma_f32_16x16x32_bf16 v[100:103], v[192:195], v[20:23], v[100:103]\n\t" \
  "ds_read_b64_tr_b16 v[218:219], v232 offset:22528\n\t" \
  "v_cvt_pk_bf16_f32 v82, v88, v89\n\t" \
  "s_waitcnt lgkmcnt(6)\n\t" \
  "v_mfma_f32_16x16x32_bf16 v[104:107], v[196:199], v[0:3], v[104:107]\n\t" \
  "ds_read_b64_tr_b16 v[220:221], v233 offset:12288\n\t" \
  "v_cvt_pk_bf16_f32 v83, v90, v91\n\t" \
  "v_mfma_f32_16x16x32_bf16 v[104:107], v[200:203], v[8:11], v[104:107]\n\t" \
  "ds_read_b64_tr_b16 v[222:223], v233 offset:14336\n\t" \
  "v_cvt_pk_bf16_f32 v86, v92, v93\n\t" \
  "v_mfma_f32_16x16x32_bf16 v[104:107], v[204:207], v[16:19], v[104:107]\n\t" \
  "ds_read_b64_tr_b16 v[192:193], v233 offset:16384\n\t" \
  "v_cvt_pk_bf16_f32 v87, v94, v95\n\t" \
  "v_mfma_f32_16x16x32_bf16 v[108:111], v[196:199], v[4:7], v[108:111]\n\t" \
  "ds_read_b64_tr_b16 v[194:195], v233 offset:18432\n\t" \
  "s_cbranch_vccnz L_O1_%=\n\t" \
  "buffer_store_dwordx4 v[80:83], v244, s[48:51], %[sp] offen offset:0 sc1\n\t" \
  "L_U1_%=:\n\t" \
  "v_mfma_f32_16x16x32_bf16 v[108:111], v[200:203], v[12:15], v[108:111]\n\t" \
  "ds_read_b64_tr_b16 v[196:197], v233 offset:20480\n\t" \
  "v_mfma_f32_16x16x32_bf16 v[108:111], v[204:207], v[20:23], v[108:111]\n\t" \
  "ds_read_b64_tr_b16 v[198:199], v233 offset:22528\n\t" \
  "s_cbranch_vccnz L_O2_%=\n\t" \
  "buffer_store_dwordx4 v[84:87], v244, s[48:51], %[sp] offen offset:1024 sc1\n\t" \
  "L_U2_%=:\n\t" \
  "s_waitcnt lgkmcnt(6)\n\t" \
  "v_mfma_f32_16x16x32_bf16 v[112:115], v[208:211], v[144:147], v[112:115]\n\t" \
  "ds_read_b64_tr_b16 v[200:201], v234 offset:12288\n\t" \
  "v_add3_u32 v88, v80, v81, %[ep]\n\t" \
  "v_mfma_f32_16x16x32_bf16 v[112:115], v[212:215], v[160:163], v[112:115]\n\t" \
  "ds_read_b64_tr_b16 v[202:203], v234 offset:14336\n\t" \
  "v_add3_u32 v88, v88, v82, v83\n\t" \
  "v_cvt_pk_bf16_f32 v96, v96, v97\n\t" \
  "v_mfma_f32_16x16x32_bf16 v[112:115], v[216:219], v[176:179], v[112:115]\n\t" \
  "ds_read_b64_tr_b16 v[204:205], v234 offset:16384\n\t" \
  "v_add3_u32 v89, v84, v85, %[ep]\n\t" \
  "v_cvt_pk_bf16_f32 v97, v98, v99\n\t" \
  "v_mfma_f32_16x16x32_bf16 v[116:119], v[208:211], v[148:151], v[116:119]\n\t" \
  "ds_read_b64_tr_b16 v[206:207], v234 offset:18432\n\t" \
  "v_add3_u32 v89, v89, v86, v87\n\t" \
  "v_cvt_pk_bf16_f32 v100, v100, v101\n\t" \
  "v_mfma_f32_16x16x32_bf16 v[116:119], v[212:215], v[164:167], v[116:119]\n\t" \
  "ds_read_b64_tr_b16 v[208:209], v234 offset:20480\n\t" \
  "v_cvt_pk_bf16_f32 v101, v102, v103\n\t" \
  "v_mfma_f32_16x16x32_bf16 v[116:119], v[216:219], v[180:183], v[116:119]\n\t" \
  "ds_read_b64_tr_b16 v[210:211], v234 offset:22528\n\t" \
  "s_waitcnt lgkmcnt(6)\n\t" \
  "v_mfma_f32_16x16x32_bf16 v[120:123], v[220:223], v[144:147], v[120:123]\n\t" \
  "ds_read_b64_tr_b16 v[212:213], v235 offset:12288\n\t" \
  "v_mfma_f32_16x16x32_bf16 v[120:123], v[192:195], v[160:163], v[120:123]\n\t" \
  "ds_read_b64_tr_b16 v[214:215], v235 offset:14336\n\t" \
  "v_mfma_f32_16x16x32_bf16 v[120:123], v[196:199], v[176:179], v[120:123]\n\t" \
  "ds_read_b64_tr_b16 v[216:217], v235 offset:16384\n\t" \
  "v_mfma_f32_16x16x32_bf16 v[124:127], v[220:223], v[148:151], v[124:127]\n\t" \
  "ds_read_b64_tr_b16 v[218:219], v235 offset:18432\n\t" \
  "v_cvt_pk_bf16_f32 v98, v104, v105\n\t" \
  "v_mfma_f32_16x16x32_bf16 v[124:127], v[192:195], v[164:167], v[124:127]\n\t" \
  "ds_read_b64_tr_b16 v[220:221], v235 offset:20480\n\t" \
  "v_cvt_pk_bf16_f32 v99, v106, v107\n\t" \
  "v_mfma_f32_16x16x32_bf16 v[124:127], v[196:199], v[180:183], v[124:127]\n\t" \
  "ds_read_b64_tr_b16 v[222:223], v235 offset:22528\n\t" \
  "v_cvt_pk_bf16_f32 v102, v108, v109\n\t" \
  "s_waitcnt lgkmcnt(6)\n\t" \
  "v_mfma_f32_16x16x32_bf16 v[128:131], v[200:203], v[144:147], v[128:131]\n\t" \
  "ds_read_b64_tr_b16 v[192:193], v232 offset:49152\n\t" \
  "v_cvt_pk_bf16_f32 v103, v110, v111\n\t" \
  "v_mfma_f32_16x16x32_bf16 v[128:131], v[204:207], v[160:163], v[128:131]\n\t" \
  "ds_read_b64_tr_b16 v[194:195], v232 offset:51200\n\t" \
  "s_cbranch_vccnz L_O3_%=\n\t" \
  "buffer_store_dwordx4 v[96:99], v244, s[48:51], %[sp] offen offset:2048 sc1\n\t" \
  "L_U3_%=:\n\t" \
  "v_mfma_f32_16x16x32_bf16 v[128:131], v[208:211], v[176:179], v[128:131]\n\t" \
  "ds_read_b64_tr_b16 v[196:197], v232 offset:53248\n\t" \
  "v_mfma_f32_16x16x32_bf16 v[132:135], v[200:203], v[148:151], v[132:135]\n\t" \
  "ds_read_b64_tr_b16 v[198:199], v232 offset:55296\n\t" \
  "s_cbranch_vccnz L_O4_%=\n\t" \
  "buffer_store_dwordx4 v[100:103], v244, s[48:51], %[sp] offen offset:3072 sc1\n\t" \
  "L_U4_%=:\n\t" \
  "v_mfma_f32_16x16x32_bf16 v[132:135], v[204:207], v[164:167], v[132:135]\n\t" \
  "ds_read_b64_tr_b16 v[200:201], v232 offset:57344\n\t" \
  "v_add3_u32 v90, v96, v97, %[ep]\n\t" \
  "v_mfma_f32_16x16x32_bf16 v[132:135], v[208:211], v[180:183], v[132:135]\n\t" \
  "ds_read_b64_tr_b16 v[202:203], v232 offset:59392\n\t" \
  "v_add3_u32 v90, v90, v98, v99\n\t" \
  "s_waitcnt lgkmcnt(6)\n\t" \
  "v_mfma_f32_16x16x32_bf16 v[136:139], v[212:215], v[144:147], v[136:139]\n\t" \
  "ds_read_b64_tr_b16 v[204:205], v233 offset:49152\n\t" \
  "v_add3_u32 v91, v100, v101, %[ep]\n\t" \
  "v_mfma_f32_16x16x32_bf16 v[136:139], v[216:219], v[160:163], v[136:139]\n\t" \
  "ds_read_b64_tr_b16 v[206:207], v233 offset:51200\n\t" \
  "v_add3_u32 v91, v91, v102, v103\n\t" \
  "v_mfma_f32_16x16x32_bf16 v[136:139], v[220:223], v[176:179], v[136:139]\n\t" \
  "ds_read_b64_tr_b16 v[208:209], v233 offset:53248\n\t" \
  "s_cbranch_vccnz L_U5_%=\n\t" \
  "buffer_store_dwordx4 v[88:91], v244, s[48:51], %[sc] offen offset:0 sc1\n\t" \
  "L_U5_%=:\n\t" \
  "s_barrier\n\t" \
  "v_mfma_f32_16x16x32_bf16 v[140:143], v[212:215], v[148:151], v[140:143]\n\t" \
  "ds_read_b64_tr_b16 v[210:211], v233 offset:55296\n\t" \
  "v_mfma_f32_16x16x32_bf16 v[140:143], v[216:219], v[164:167], v[140:143]\n\t" \
  "ds_read_b64_tr_b16 v[212:213], v233 offset:57344\n\t" \
  "v_mfma_f32_16x16x32_bf16 v[140:143], v[220:223], v[180:183], v[140:143]\n\t" \
  "ds_read_b64_tr_b16 v[214:215], v233 offset:59392\n\t" \
  "s_waitcnt lgkmcnt(6)\n\t" \
  "v_mfma_f32_16x16x32_bf16 v[112:115], v[192:195], v[24:27], v[112:115]\n\t" \
  "ds_read_b64_tr_b16 v[216:217], v234 offset:49152\n\t" \
  "v_mfma_f32_16x16x32_bf16 v[112:115], v[196:199], v[32:35], v[112:115]\n\t" \
  "ds_read_b64_tr_b16 v[218:219], v234 offset:51200\n\t" \
  "v_mfma_f32_16x16x32_bf16 v[112:115], v[200:203], v[40:43], v[112:115]\n\t" \
  "ds_read_b64_tr_b16 v[220:221], v234 offset:53248\n\t" \
  "v_mfma_f32_16x16x32_bf16 v[116:119], v[192:195], v[28:31], v[116:119]\n\t" \
  "ds_read_b64_tr_b16 v[222:223], v234 offset:55296\n\t" \
  "v_mfma_f32_16x16x32_bf16 v[116:119], v[196:199], v[36:39], v[116:119]\n\t" \
  "ds_read_b64_tr_b16 v[192:193], v234 offset:57344\n\t" \
  "v_mfma_f32_16x16x32_bf16 v[116:119], v[200:203], v[44:47], v[116:119]\n\t" \
  "ds_read_b64_tr_b16 v[194:195], v234 offset:59392\n\t" \
  "s_waitcnt lgkmcnt(6)\n\t" \
  "v_mfma_f32_16x16x32_bf16 v[120:123], v[204:207], v[24:27], v[120:123]\n\t" \
  "ds_read_b64_tr_b16 v[196:197], v235 offset:49152\n\t" \
  "v_mfma_f32_16x16x32_bf16 v[120:123], v[208:211], v[32:35], v[120:123]\n\t" \
  "ds_read_b64_tr_b16 v[198:199], v235 offset:51200\n\t" \
  "v_mfma_f32_16x16x32_bf16 v[120:123], v[212:215], v[40:43], v[120:123]\n\t" \
  "ds_read_b64_tr_b16 v[200:201], v235 offset:53248\n\t" \
  "v_mfma_f32_16x16x32_bf16 v[124:127], v[204:207], v[28:31], v[124:127]\n\t" \
  "ds_read_b64_tr_b16 v[202:203], v235 offset:55296\n\t" \
  "v_mfma_f32_16x16x32_bf16 v[124:127], v[208:211], v[36:39], v[124:127]\n\t" \
  "ds_read_b64_tr_b16 v[204:205], v235 offset:57344\n\t" \
  "v_mfma_f32_16x16x32_bf16 v[124:127], v[212:215], v[44:47], v[124:127]\n\t" \
  "ds_read_b64_tr_b16 v[206:207], v235 offset:59392\n\t" \
  "s_waitcnt lgkmcnt(6)\n\t" \
  "v_mfma_f32_16x16x32_bf16 v[128:131], v[216:219], v[24:27], v[128:131]\n\t" \
  "ds_read_b64_tr_b16 v[208:209], v240 offset:12288\n\t" \
  "v_mfma_f32_16x16x32_bf16 v[128:131], v[220:223], v[32:35], v[128:131]\n\t" \
  "ds_read_b64_tr_b16 v[210:211], v240 offset:14336\n\t" \
  "v_mfma_f32_16x16x32_bf16 v[128:131], v[192:195], v[40:43], v[128:131]\n\t" \
  "ds_read_b64_tr_b16 v[212:213], v240 offset:16384\n\t" \
  "v_mfma_f32_16x16x32_bf16 v[132:135], v[216:219], v[28:31], v[132:135]\n\t" \
  "ds_read_b64_tr_b16 v[214:215], v240 offset:18432\n\t" \
  "v_mfma_f32_16x16x32_bf16 v[132:135], v[220:223], v[36:39], v[132:135]\n\t" \
  "ds_read_b64_tr_b16 v[216:217], v240 offset:20480\n\t" \
  "v_mfma_f32_16x16x32_bf16 v[132:135], v[192:195], v[44:47], v[132:135]\n\t" \
  "ds_read_b64_tr_b16 v[218:219], v240 offset:22528\n\t" \
  "s_waitcnt lgkmcnt(6)\n\t" \
  "v_mfma_f32_16x16x32_bf16 v[136:139], v[196:199], v[24:27], v[136:139]\n\t" \
  "ds_read_b64_tr_b16 v[220:221], v241 offset:12288\n\t" \
  "v_mfma_f32_16x16x32_bf16 v[136:139], v[200:203], v[32:35], v[136:139]\n\t" \
  "ds_read_b64_tr_b16 v[222:223], v241 offset:14336\n\t" \
  "v_mfma_f32_16x16x32_bf16 v[136:139], v[204:207], v[40:43], v[136:139]\n\t" \
  "ds_read_b64_tr_b16 v[192:193], v241 offset:16384\n\t" \
  "v_mfma_f32_16x16x32_bf16 v[140:143], v[196:199], v[28:31], v[140:143]\n\t" \
  "ds_read_b64_tr_b16 v[194:195], v241 offset:18432\n\t" \
  "v_mfma_f32_16x16x32_bf16 v[140:143], v[200:203], v[36:39], v[140:143]\n\t" \
  "ds_read_b64_tr_b16 v[196:197], v241 offset:20480\n\t" \
  "v_mfma_f32_16x16x32_bf16 v[140:143], v[204:207], v[44:47], v[140:143]\n\t" \
  "ds_read_b64_tr_b16 v[198:199], v241 offset:22528\n\t" \
  "s_waitcnt lgkmcnt(6)\n\t" \
  "v_mfma_f32_16x16x32_bf16 v[112:115], v[208:211], v[0:3], v[112:115]\n\t" \
  "ds_read_b64_tr_b16 v[200:201], v242 offset:12288\n\t" \
  "v_mfma_f32_16x16x32_bf16 v[112:115], v[212:215], v[8:11], v[112:115]\n\t" \
  "ds_read_b64_tr_b16 v[202:203], v242 offset:14336\n\t" \
  "v_mfma_f32_16x16x32_bf16 v[112:115], v[216:219], v[16:19], v[112:115]\n\t" \
  "ds_read_b64_tr_b16 v[204:205], v242 offset:16384\n\t" \
  "v_mfma_f32_16x16x32_bf16 v[116:119], v[208:211], v[4:7], v[116:119]\n\t" \
  "ds_read_b64_tr_b16 v[206:207], v242 offset:18432\n\t" \
  "v_mfma_f32_16x16x32_bf16 v[116:119], v[212:215], v[12:15], v[116:119]\n\t" \
  "ds_read_b64_tr_b16 v[208:209], v242 offset:20480\n\t" \
  "v_mfma_f32_16x16x32_bf16 v[116:119], v[216:219], v[20:23], v[116:119]\n\t" \
  "ds_read_b64_tr_b16 v[210:211], v242 offset:22528\n\t" \
  "s_waitcnt lgkmcnt(6)\n\t" \
  "v_mfma_f32_16x16x32_bf16 v[120:123], v[220:223], v[0:3], v[120:123]\n\t" \
  "ds_read_b64_tr_b16 v[212:213], v243 offset:12288\n\t" \
  "v_mfma_f32_16x16x32_bf16 v[120:123], v[192:195], v[8:11], v[120:123]\n\t" \
  "ds_read_b64_tr_b16 v[214:215], v243 offset:14336\n\t" \
  "v_mfma_f32_16x16x32_bf16 v[120:123], v[196:199], v[16:19], v[120:123]\n\t" \
  "ds_read_b64_tr_b16 v[216:217], v243 offset:16384\n\t" \
  "v_mfma_f32_16x16x32_bf16 v[124:127], v[220:223], v[4:7], v[124:127]\n\t" \
  "ds_read_b64_tr_b16 v[218:219], v243 offset:18432\n\t" \
  "v_cvt_pk_bf16_f32 v112, v112, v113\n\t" \
  "v_mfma_f32_16x16x32_bf16 v[124:127], v[192:195], v[12:15], v[124:127]\n\t" \
  "ds_read_b64_tr_b16 v[220:221], v243 offset:20480\n\t" \
  "v_cvt_pk_bf16_f32 v113, v114, v115\n\t" \
  "v_mfma_f32_16x16x32_bf16 v[124:127], v[196:199], v[20:23], v[124:127]\n\t" \
  "ds_read_b64_tr_b16 v[222:223], v243 offset:22528\n\t" \
  "v_cvt_pk_bf16_f32 v116, v116, v117\n\t" \
  "s_waitcnt lgkmcnt(6)\n\t" \
  "v_mfma_f32_16x16x32_bf16 v[128:131], v[200:203], v[0:3], v[128:131]\n\t" \
  "v_cvt_pk_bf16_f32 v117, v118, v119\n\t" \
  "v_mfma_f32_16x16x32_bf16 v[128:131], v[204:207], v[8:11], v[128:131]\n\t" \
  "v_mfma_f32_16x16x32_bf16 v[128:131], v[208:211], v[16:19], v[128:131]\n\t" \
  "v_mfma_f32_16x16x32_bf16 v[132:135], v[200:203], v[4:7], v[132:135]\n\t" \
  "v_cvt_pk_bf16_f32 v114, v120, v121\n\t" \
  "v_mfma_f32_16x16x32_bf16 v[132:135], v[204:207], v[12:15], v[132:135]\n\t" \
  "v_cvt_pk_bf16_f32 v115, v122, v123\n\t" \
  "v_mfma_f32_16x16x32_bf16 v[132:135], v[208:211], v[20:23], v[132:135]\n\t" \
  "v_cvt_pk_bf16_f32 v118, v124, v125\n\t" \
  "s_waitcnt lgkmcnt(0)\n\t" \
  "v_mfma_f32_16x16x32_bf16 v[136:139], v[212:215], v[0:3], v[136:139]\n\t" \
  "v_cvt_pk_bf16_f32 v119, v126, v127\n\t" \
  "v_mfma_f32_16x16x32_bf16 v[136:139], v[216:219], v[8:11], v[136:139]\n\t" \
  "s_cbranch_vccnz L_O6_%=\n\t" \
  "buffer_store_dwordx4 v[112:115], v244, s[48:51], s78 offen offset:0 sc1\n\t" \
  "L_U6_%=:\n\t" \
  "v_mfma_f32_16x16x32_bf16 v[136:139], v[220:223], v[16:19], v[136:139]\n\t" \
  "s_cbranch_vccnz L_O7_%=\n\t" \
  "buffer_store_dwordx4 v[116:119], v244, s[48:51], s78 offen offset:1024 sc1\n\t" \
  "L_U7_%=:\n\t" \
  "v_mfma_f32_16x16x32_bf16 v[140:143], v[212:215], v[4:7], v[140:143]\n\t" \
  "v_add3_u32 v120, v112, v113, %[ep]\n\t" \
  "v_cvt_pk_bf16_f32 v128, v128, v129\n\t" \
  "v_mfma_f32_16x16x32_bf16 v[140:143], v[216:219], v[12:15], v[140:143]\n\t" \
  "v_add3_u32 v120, v120, v114, v115\n\t" \
  "v_cvt_pk_bf16_f32 v129, v130, v131\n\t" \
  "v_mfma_f32_16x16x32_bf16 v[140:143], v[220:223], v[20:23], v[140:143]\n\t" \
  "v_add3_u32 v121, v116, v117, %[ep]\n\t" \
  "v_cvt_pk_bf16_f32 v132, v132, v133\n\t" \
  "s_nop 7\n\t" \
  "v_add3_u32 v121, v121, v118, v119\n\t" \
  "v_cvt_pk_bf16_f32 v133, v134, v135\n\t" \
  "v_cvt_pk_bf16_f32 v130, v136, v137\n\t" \
  "v_cvt_pk_bf16_f32 v131, v138, v139\n\t" \
  "v_cvt_pk_bf16_f32 v134, v140, v141\n\t" \
  "v_cvt_pk_bf16_f32 v135, v142, v143\n\t" \
  "s_cbranch_vccnz L_O8_%=\n\t" \
  "buffer_store_dwordx4 v[128:131], v244, s[48:51], s78 offen offset:2048 sc1\n\t" \
  "L_U8_%=:\n\t" \
  "s_cbranch_vccnz L_O9_%=\n\t" \
  "buffer_store_dwordx4 v[132:135], v244, s[48:51], s78 offen offset:3072 sc1\n\t" \
  "L_U9_%=:\n\t" \
  "v_add3_u32 v122, v128, v129, %[ep]\n\t" \
  "v_add3_u32 v122, v122, v130, v131\n\t" \
  "v_add3_u32 v123, v132, v133, %[ep]\n\t" \
  "v_add3_u32 v123, v123, v134, v135\n\t" \
  "s_cbranch_vccnz L_U10_%=\n\t" \
  "buffer_store_dwordx4 v[120:123], v244, s[48:51], %[sc] offen offset:1024 sc1\n\t" \
  "L_U10_%=:\n\t" \
  "s_setprio 0\n\t" \
  "s_branch L_OE_%=\n\t" \
  "L_O1_%=:\n\t" \
  "ds_write_b128 v231, v[80:83] offset:0\n\t" \
  "s_branch L_U1_%=\n\t" \
  "L_O2_%=:\n\t" \
  "ds_write_b128 v231, v[84:87] offset:1024\n\t" \
  "s_branch L_U2_%=\n\t" \
  "L_O3_%=:\n\t" \
  "ds_write_b128 v231, v[96:99] offset:2048\n\t" \
  "s_branch L_U3_%=\n\t" \
  "L_O4_%=:\n\t" \
  "ds_write_b128 v231, v[100:103] offset:3072\n\t" \
  "s_branch L_U4_%=\n\t" \
  "L_O6_%=:\n\t" \
  "ds_write_b128 v231, v[112:115] offset:4096\n\t" \
  "s_branch L_U6_%=\n\t" \
  "L_O7_%=:\n\t" \
  "ds_write_b128 v231, v[116:119] offset:5120\n\t" \
  "s_branch L_U7_%=\n\t" \
  "L_O8_%=:\n\t" \
  "ds_write_b128 v231, v[128:131] offset:6144\n\t" \
  "s_branch L_U8_%=\n\t" \
  "L_O9_%=:\n\t" \
  "ds_write_b128 v231, v[132:135] offset:7168\n\t" \
  "s_branch L_U9_%=\n\t" \
  "L_OE_%=:\n\t" \
  ""
#define CORE_L0 \
  "s_mov_b64 s[40:41], %[w3]\n\t" \
  "s_mov_b32 s42, 5308416\n\t" \
  "s_mov_b32 s43, 0x20000\n\t" \
  "s_mov_b32 s76, %[h0]\n\t" \
  "L_PF_%=:\n\t" \
  "s_cmp_ge_u32 s76, 6\n\t" \
  "s_cselect_b32 s75, %[b1], %[b0]\n\t" \
  "s_mul_i32 s82, s76, 884736\n\t" \
  "s_add_u32 s75, s75, s82\n\t" \
  "s_mul_i32 s82, s76, 12288\n\t" \
  "s_lshl_b32 s79, %[wi], 10\n\t" \
  "s_add_u32 m0, s79, s82\n\t" \
  "s_add_u32 s76, s76, 1\n\t" \
  "buffer_load_dwordx4 %[g3], s[40:43], s75 offen lds\n\t" \
  "s_add_u32 m0, m0, 4096\n\t" \
  "s_add_u32 s75, s75, 294912\n\t" \
  "buffer_load_dwordx4 %[g3], s[40:43], s75 offen lds\n\t" \
  "s_add_u32 m0, m0, 4096\n\t" \
  "s_add_u32 s75, s75, 294912\n\t" \
  "buffer_load_dwordx4 %[g3], s[40:43], s75 offen lds\n\t" \
  "s_cmp_lt_u32 s76, %[h1]\n\t" \
  "s_cbranch_scc1 L_PF_%=\n\t" \
  ""
#define CORE_L1 \
  "s_mov_b64 s[40:41], %[w3]\n\t" \
  "s_mov_b32 s42, 5308416\n\t" \
  "s_mov_b32 s43, 0x20000\n\t" \
  "s_mov_b64 s[44:45], %[w4]\n\t" \
  "s_mov_b32 s46, 5308416\n\t" \
  "s_mov_b32 s47, 0x20000\n\t" \
  "s_mov_b64 s[48:49], %[ws]\n\t" \
  "s_mov_b32 s50, 134217728\n\t" \
  "s_mov_b32 s51, 0x20000\n\t" \
  "s_mul_i32 s71, %[sl], 73728\n\t" \
  "s_add_u32 s71, s71, %[shx]\n\t" \
  "v_mbcnt_lo_u32_b32 v238, -1, 0\n\t" \
  "v_mbcnt_hi_u32_b32 v238, -1, v238\n\t" \
  "v_lshlrev_b32 v232, 4, v238\n\t" \
  "s_lshl_b32 s79, %[wi], 10\n\t" \
  "s_mul_i32 s80, %[wi], 3072\n\t" \
  "s_lshr_b32 s75, %[sl], 1\n\t" \
  "s_mul_i32 s75, s75, 1152\n\t" \
  "s_mul_i32 s76, %[wi], 73728\n\t" \
  "s_add_u32 s75, s75, s76\n\t" \
  "s_add_u32 s58, s75, 512\n\t" \
  "s_and_b32 s76, %[sl], 1\n\t" \
  "s_mul_i32 s76, s76, 640\n\t" \
  "s_add_u32 s75, s75, s76\n\t" \
  "s_add_u32 s76, %[rot], 0\n\t" \
  "s_and_b32 s76, s76, 3\n\t" \
  "s_mul_i32 s59, s76, 73728\n\t" \
  "s_lshl_b32 s76, s76, 7\n\t" \
  "s_add_u32 s54, s75, s76\n\t" \
  "s_add_u32 s76, %[rot], 1\n\t" \
  "s_and_b32 s76, s76, 3\n\t" \
  "s_mul_i32 s60, s76, 73728\n\t" \
  "s_lshl_b32 s76, s76, 7\n\t" \
  "s_add_u32 s55, s75, s76\n\t" \
  "s_add_u32 s76, %[rot], 2\n\t" \
  "s_and_b32 s76, s76, 3\n\t" \
  "s_mul_i32 s61, s76, 73728\n\t" \
  "s_lshl_b32 s76, s76, 7\n\t" \
  "s_add_u32 s56, s75, s76\n\t" \
  "s_add_u32 s76, %[rot], 3\n\t" \
  "s_and_b32 s76, s76, 3\n\t" \
  "s_mul_i32 s62, s76, 73728\n\t" \
  "s_lshl_b32 s76, s76, 7\n\t" \
  "s_add_u32 s57, s75, s76\n\t" \
  "s_and_b32 s82, %[sl], 3\n\t" \
  "s_lshl_b32 s82, s82, 8\n\t" \
  "s_and_b32 s75, %[sl], 2\n\t" \
  "s_lshl_b32 s75, s75, 6\n\t" \
  "s_add_u32 s82, s82, s75\n\t" \
  "s_sub_u32 s81, 512, s82\n\t" \
  "s_mul_i32 s75, %[wi], 9216\n\t" \
  "s_add_u32 s82, s82, s75\n\t" \
  "s_xor_b32 s75, %[sl], 0\n\t" \
  "s_mul_i32 s63, s75, 331776\n\t" \
  "s_add_u32 s63, s63, s82\n\t" \
  "s_bitcmp1_b32 s75, 0\n\t" \
  "s_cselect_b32 s75, 36864, 0\n\t" \
  "s_cselect_b32 s76, 0, 294912\n\t" \
  "s_add_u32 s67, s63, s76\n\t" \
  "s_add_u32 s63, s63, s75\n\t" \
  "s_xor_b32 s75, %[sl], 1\n\t" \
  "s_mul_i32 s72, s75, 73728\n\t" \
  "s_add_u32 s72, s72, %[shx]\n\t" \
  "s_mul_i32 s64, s75, 331776\n\t" \
  "s_add_u32 s64, s64, s82\n\t" \
  "s_bitcmp1_b32 s75, 0\n\t" \
  "s_cselect_b32 s75, 36864, 0\n\t" \
  "s_cselect_b32 s76, 0, 294912\n\t" \
  "s_add_u32 s68, s64, s76\n\t" \
  "s_add_u32 s64, s64, s75\n\t" \
  "s_xor_b32 s75, %[sl], 2\n\t" \
  "s_mul_i32 s73, s75, 73728\n\t" \
  "s_add_u32 s73, s73, %[shx]\n\t" \
  "s_mul_i32 s65, s75, 331776\n\t" \
  "s_add_u32 s65, s65, s82\n\t" \
  "s_bitcmp1_b32 s75, 0\n\t" \
  "s_cselect_b32 s75, 36864, 0\n\t" \
  "s_cselect_b32 s76, 0, 294912\n\t" \
  "s_add_u32 s69, s65, s76\n\t" \
  "s_add_u32 s65, s65, s75\n\t" \
  "s_xor_b32 s75, %[sl], 3\n\t" \
  "s_mul_i32 s74, s75, 73728\n\t" \
  "s_add_u32 s74, s74, %[shx]\n\t" \
  "s_mul_i32 s66, s75, 331776\n\t" \
  "s_add_u32 s66, s66, s82\n\t" \
  "s_bitcmp1_b32 s75, 0\n\t" \
  "s_cselect_b32 s75, 36864, 0\n\t" \
  "s_cselect_b32 s76, 0, 294912\n\t" \
  "s_add_u32 s70, s66, s76\n\t" \
  "s_add_u32 s66, s66, s75\n\t" \
  "v_and_b32 v239, 0x70, v232\n\t" \
  "v_lshlrev_b32 v235, 1, v238\n\t" \
  "v_and_b32 v235, 0x60, v235\n\t" \
  "v_xor_b32 v239, v239, v235\n\t" \
  "v_lshrrev_b32 v235, 3, v238\n\t" \
  "v_mul_u32_u24 v233, 9216, v235\n\t" \
  "v_add_u32 v233, v233, v239\n\t" \
  "v_mul_u32_u24 v234, 1152, v235\n\t" \
  "v_add_u32 v234, v234, v239\n\t" \
  "v_and_b32 v239, 3, v238\n\t" \
  "v_max_u32 v239, 1, v239\n\t" \
  "v_xor_b32 v239, %[sl], v239\n\t" \
  "v_lshlrev_b32 v235, 10, v239\n\t" \
  "s_lshl_b32 s75, %[sl], 10\n\t" \
  "v_lshl_add_u32 v236, v238, 2, s75\n\t" \
  "v_mov_b32 v237, %[ep]\n\t" \
  "s_waitcnt vmcnt(0) lgkmcnt(0)\n\t" \
  "s_barrier\n\t" \
  "s_sleep 16\n\t" \
  "s_add_u32 m0, s79, 122880\n\t" \
  "s_add_u32 s75, s55, 3538944\n\t" \
  "buffer_load_dwordx4 v233, s[40:43], s75 offen lds\n\t" \
  "s_add_u32 m0, s79, 126976\n\t" \
  "s_add_u32 s75, s55, 3833856\n\t" \
  "buffer_load_dwordx4 v233, s[40:43], s75 offen lds\n\t" \
  "s_add_u32 m0, s79, 131072\n\t" \
  "s_add_u32 s75, s55, 4128768\n\t" \
  "buffer_load_dwordx4 v233, s[40:43], s75 offen lds\n\t" \
  "s_add_u32 m0, s79, 135168\n\t" \
  "s_add_u32 s75, s55, 4423680\n\t" \
  "buffer_load_dwordx4 v233, s[40:43], s75 offen lds\n\t" \
  "s_add_u32 m0, s79, 139264\n\t" \
  "s_add_u32 s75, s55, 4718592\n\t" \
  "buffer_load_dwordx4 v233, s[40:43], s75 offen lds\n\t" \
  "s_add_u32 m0, s79, 143360\n\t" \
  "s_add_u32 s75, s55, 5013504\n\t" \
  "buffer_load_dwordx4 v233, s[40:43], s75 offen lds\n\t" \
  "s_add_u32 m0, s79, 147456\n\t" \
  "s_add_u32 s75, s56, 0\n\t" \
  "buffer_load_dwordx4 v233, s[40:43], s75 offen lds\n\t" \
  "s_add_u32 m0, s79, 151552\n\t" \
  "s_add_u32 s75, s56, 294912\n\t" \
  "buffer_load_dwordx4 v233, s[40:43], s75 offen lds\n\t" \
  "s_add_u32 m0, s79, 155648\n\t" \
  "s_add_u32 s75, s56, 589824\n\t" \
  "buffer_load_dwordx4 v233, s[40:43], s75 offen lds\n\t" \
  "s_mov_b32 exec_hi, 0x1fff\n\t" \
  "buffer_load_dword v227, %[pf3], s[40:43], 0 offen\n\t" \
  "s_mov_b32 exec_hi, -1\n\t" \
  "s_mov_b32 s75, 0\n\t" \
  "s_mov_b64 exec, 0x7ffffff\n\t" \
  "buffer_load_dword v227, %[pf4], s[44:47], s75 offen\n\t" \
  "s_mov_b64 exec, -1\n\t" \
  "s_mov_b32 s75, 442368\n\t" \
  "s_mov_b64 exec, 0x7ffffff\n\t" \
  "buffer_load_dword v227, %[pf4], s[44:47], s75 offen\n\t" \
  "s_mov_b64 exec, -1\n\t" \
  "s_waitcnt vmcnt(6)\n\t" \
  "s_barrier\n\t" \
  "s_cmp_eq_u32 %[wi], 0\n\t" \
  "s_cbranch_scc0 L_Y_%=\n\t" \
  "v_mov_b32 v239, %[fyo]\n\t" \
  "s_mov_b64 exec, 1\n\t" \
  "buffer_store_dword v237, v239, s[48:51], 0 offen sc1\n\t" \
  "s_mov_b64 exec, -1\n\t" \
  "L_Y_%=:\n\t" \
  "s_add_u32 m0, s79, 0\n\t" \
  "s_add_u32 s75, s56, 884736\n\t" \
  "buffer_load_dwordx4 v233, s[40:43], s75 offen lds\n\t" \
  "s_add_u32 m0, s79, 4096\n\t" \
  "s_add_u32 s75, s56, 1179648\n\t" \
  "buffer_load_dwordx4 v233, s[40:43], s75 offen lds\n\t" \
  "s_add_u32 m0, s79, 8192\n\t" \
  "s_add_u32 s75, s56, 1474560\n\t" \
  "buffer_load_dwordx4 v233, s[40:43], s75 offen lds\n\t" \
  "s_add_u32 m0, s79, 12288\n\t" \
  "s_add_u32 s75, s56, 1769472\n\t" \
  "buffer_load_dwordx4 v233, s[40:43], s75 offen lds\n\t" \
  "s_add_u32 m0, s79, 16384\n\t" \
  "s_add_u32 s75, s56, 2064384\n\t" \
  "buffer_load_dwordx4 v233, s[40:43], s75 offen lds\n\t" \
  "s_add_u32 m0, s79, 20480\n\t" \
  "s_add_u32 s75, s56, 2359296\n\t" \
  "buffer_load_dwordx4 v233, s[40:43], s75 offen lds\n\t" \
  "s_add_u32 m0, s79, 24576\n\t" \
  "s_add_u32 s75, s56, 2654208\n\t" \
  "buffer_load_dwordx4 v233, s[40:43], s75 offen lds\n\t" \
  "s_add_u32 m0, s79, 28672\n\t" \
  "s_add_u32 s75, s56, 2949120\n\t" \
  "buffer_load_dwordx4 v233, s[40:43], s75 offen lds\n\t" \
  "s_add_u32 m0, s79, 32768\n\t" \
  "s_add_u32 s75, s56, 3244032\n\t" \
  "buffer_load_dwordx4 v233, s[40:43], s75 offen lds\n\t" \
  "s_add_u32 m0, s79, 36864\n\t" \
  "s_add_u32 s75, s56, 3538944\n\t" \
  "buffer_load_dwordx4 v233, s[40:43], s75 offen lds\n\t" \
  "s_add_u32 m0, s79, 40960\n\t" \
  "s_add_u32 s75, s56, 3833856\n\t" \
  "buffer_load_dwordx4 v233, s[40:43], s75 offen lds\n\t" \
  "s_add_u32 m0, s79, 45056\n\t" \
  "s_add_u32 s75, s56, 4128768\n\t" \
  "buffer_load_dwordx4 v233, s[40:43], s75 offen lds\n\t" \
  "s_add_u32 m0, s79, 49152\n\t" \
  "s_add_u32 s75, s56, 4423680\n\t" \
  "buffer_load_dwordx4 v233, s[40:43], s75 offen lds\n\t" \
  "s_add_u32 m0, s79, 53248\n\t" \
  "s_add_u32 s75, s56, 4718592\n\t" \
  "buffer_load_dwordx4 v233, s[40:43], s75 offen lds\n\t" \
  "s_add_u32 m0, s79, 57344\n\t" \
  "s_add_u32 s75, s56, 5013504\n\t" \
  "buffer_load_dwordx4 v233, s[40:43], s75 offen lds\n\t" \
  "s_mov_b32 s75, 884736\n\t" \
  "s_mov_b64 exec, 0x7ffffff\n\t" \
  "buffer_load_dword v227, %[pf4], s[44:47], s75 offen\n\t" \
  "s_mov_b64 exec, -1\n\t" \
  "buffer_load_dwordx4 v[100:103], v232, s[48:51], %[sxo] offen sc1\n\t" \
  "s_add_u32 s75, %[sxo], 4096\n\t" \
  "buffer_load_dwordx4 v[104:107], v232, s[48:51], s75 offen sc1\n\t" \
  "buffer_load_dwordx4 v[108:111], v232, s[48:51], %[sxo2] offen sc1\n\t" \
  "global_load_dwordx4 v[112:115], %[aq], %[q]\n\t" \
  "global_load_dwordx4 v[116:119], %[aq], %[q] offset:128\n\t" \
  "global_load_dwordx4 v[120:123], %[aq2], %[q]\n\t" \
  "s_waitcnt vmcnt(7)\n\t" \
  "s_barrier\n\t" \
  "s_add_u32 m0, s79, 61440\n\t" \
  "s_add_u32 s75, s57, 0\n\t" \
  "buffer_load_dwordx4 v233, s[40:43], s75 offen lds\n\t" \
  "s_add_u32 m0, s79, 65536\n\t" \
  "s_add_u32 s75, s57, 294912\n\t" \
  "buffer_load_dwordx4 v233, s[40:43], s75 offen lds\n\t" \
  "s_add_u32 m0, s79, 69632\n\t" \
  "s_add_u32 s75, s57, 589824\n\t" \
  "buffer_load_dwordx4 v233, s[40:43], s75 offen lds\n\t" \
  "s_add_u32 m0, s79, 73728\n\t" \
  "s_add_u32 s75, s57, 884736\n\t" \
  "buffer_load_dwordx4 v233, s[40:43], s75 offen lds\n\t" \
  "s_add_u32 m0, s79, 77824\n\t" \
  "s_add_u32 s75, s57, 1179648\n\t" \
  "buffer_load_dwordx4 v233, s[40:43], s75 offen lds\n\t" \
  "s_add_u32 m0, s79, 81920\n\t" \
  "s_add_u32 s75, s57, 1474560\n\t" \
  "buffer_load_dwordx4 v233, s[40:43], s75 offen lds\n\t" \
  "s_add_u32 m0, s79, 86016\n\t" \
  "s_add_u32 s75, s57, 1769472\n\t" \
  "buffer_load_dwordx4 v233, s[40:43], s75 offen lds\n\t" \
  "s_add_u32 m0, s79, 90112\n\t" \
  "s_add_u32 s75, s57, 2064384\n\t" \
  "buffer_load_dwordx4 v233, s[40:43], s75 offen lds\n\t" \
  "s_add_u32 m0, s79, 94208\n\t" \
  "s_add_u32 s75, s57, 2359296\n\t" \
  "buffer_load_dwordx4 v233, s[40:43], s75 offen lds\n\t" \
  "s_add_u32 m0, s79, 98304\n\t" \
  "s_add_u32 s75, s57, 2654208\n\t" \
  "buffer_load_dwordx4 v233, s[40:43], s75 offen lds\n\t" \
  "s_add_u32 m0, s79, 102400\n\t" \
  "s_add_u32 s75, s57, 2949120\n\t" \
  "buffer_load_dwordx4 v233, s[40:43], s75 offen lds\n\t" \
  "s_add_u32 m0, s79, 106496\n\t" \
  "s_add_u32 s75, s57, 3244032\n\t" \
  "buffer_load_dwordx4 v233, s[40:43], s75 offen lds\n\t" \
  "s_add_u32 m0, s79, 110592\n\t" \
  "s_add_u32 s75, s57, 3538944\n\t" \
  "buffer_load_dwordx4 v233, s[40:43], s75 offen lds\n\t" \
  "s_add_u32 m0, s79, 114688\n\t" \
  "s_add_u32 s75, s57, 3833856\n\t" \
  "buffer_load_dwordx4 v233, s[40:43], s75 offen lds\n\t" \
  "s_add_u32 m0, s79, 118784\n\t" \
  "s_add_u32 s75, s57, 4128768\n\t" \
  "buffer_load_dwordx4 v233, s[40:43], s75 offen lds\n\t" \
  "s_add_u32 m0, s79, 122880\n\t" \
  "s_add_u32 s75, s57, 4423680\n\t" \
  "buffer_load_dwordx4 v233, s[40:43], s75 offen lds\n\t" \
  "s_add_u32 m0, s79, 126976\n\t" \
  "s_add_u32 s75, s57, 4718592\n\t" \
  "buffer_load_dwordx4 v233, s[40:43], s75 offen lds\n\t" \
  "s_add_u32 m0, s79, 131072\n\t" \
  "s_add_u32 s75, s57, 5013504\n\t" \
  "buffer_load_dwordx4 v233, s[40:43], s75 offen lds\n\t" \
  "s_mul_i32 s75, %[wi], 3072\n\t" \
  "s_add_u32 s76, s75, 122880\n\t" \
  "s_add_u32 s75, s75, 24576\n\t" \
  "v_add_u32 v220, s76, v232\n\t" \
  "v_add_u32 v221, s75, v232\n\t" \
  "v_lshrrev_b32 v222, 1, v232\n\t" \
  "v_add_u32 v222, s76, v222\n\t" \
  "v_lshlrev_b32 v191, 2, v232\n\t" \
  "v_and_b32 v191, 0x3c0, v191\n\t" \
  "v_lshrrev_b32 v192, 5, v232\n\t" \
  "v_and_b32 v192, 24, v192\n\t" \
  "v_add3_u32 v223, v191, v192, s75\n\t" \
  "v_and_b32 v193, 0xf0, v232\n\t" \
  "v_add3_u32 v224, v191, v192, v193\n\t" \
  "v_add_u32 v224, s76, v224\n\t" \
  "v_lshrrev_b32 v193, 2, v232\n\t" \
  "v_and_b32 v193, 0xf0, v193\n\t" \
  "v_add3_u32 v225, v193, v232, s76\n\t" \
  "s_lshl_b32 s75, %[wi], 10\n\t" \
  "s_add_u32 s75, s75, 36864\n\t" \
  "v_add_u32 v137, s75, v232\n\t" \
  "s_barrier\n\t" \
  "s_cmp_eq_u32 %[wi], 0\n\t" \
  "s_cbranch_scc0 L_S11_%=\n\t" \
  "s_mov_b64 exec, 0xffff\n\t" \
  "buffer_store_dword v237, v236, s[48:51], %[sfl] offen offset:0 sc1\n\t" \
  "s_mov_b64 exec, -1\n\t" \
  "L_S11_%=:\n\t" \
  "s_add_u32 m0, s79, 0\n\t" \
  "s_add_u32 s75, s58, 0\n\t" \
  "buffer_load_dwordx4 v233, s[40:43], s75 offen lds\n\t" \
  "s_add_u32 m0, s79, 4096\n\t" \
  "s_add_u32 s75, s58, 294912\n\t" \
  "buffer_load_dwordx4 v233, s[40:43], s75 offen lds\n\t" \
  "s_add_u32 m0, s79, 8192\n\t" \
  "s_add_u32 s75, s58, 589824\n\t" \
  "buffer_load_dwordx4 v233, s[40:43], s75 offen lds\n\t" \
  "s_add_u32 m0, s79, 135168\n\t" \
  "s_add_u32 s75, s58, 884736\n\t" \
  "buffer_load_dwordx4 v233, s[40:43], s75 offen lds\n\t" \
  "s_add_u32 m0, s79, 139264\n\t" \
  "s_add_u32 s75, s58, 1179648\n\t" \
  "buffer_load_dwordx4 v233, s[40:43], s75 offen lds\n\t" \
  "s_add_u32 m0, s79, 143360\n\t" \
  "s_add_u32 s75, s58, 1474560\n\t" \
  "buffer_load_dwordx4 v233, s[40:43], s75 offen lds\n\t" \
  "s_add_u32 m0, s79, 147456\n\t" \
  "s_add_u32 s75, s58, 1769472\n\t" \
  "buffer_load_dwordx4 v233, s[40:43], s75 offen lds\n\t" \
  "s_add_u32 m0, s79, 151552\n\t" \
  "s_add_u32 s75, s58, 2064384\n\t" \
  "buffer_load_dwordx4 v233, s[40:43], s75 offen lds\n\t" \
  "s_add_u32 m0, s79, 155648\n\t" \
  "s_add_u32 s75, s58, 2359296\n\t" \
  "buffer_load_dwordx4 v233, s[40:43], s75 offen lds\n\t" \
  "s_waitcnt vmcnt(18)\n\t" \
  "s_barrier\n\t" \
  "s_add_u32 m0, s79, 12288\n\t" \
  "s_add_u32 s75, s58, 2654208\n\t" \
  "buffer_load_dwordx4 v233, s[40:43], s75 offen lds\n\t" \
  "s_add_u32 m0, s79, 16384\n\t" \
  "s_add_u32 s75, s58, 2949120\n\t" \
  "buffer_load_dwordx4 v233, s[40:43], s75 offen lds\n\t" \
  "s_add_u32 m0, s79, 20480\n\t" \
  "s_add_u32 s75, s58, 3244032\n\t" \
  "buffer_load_dwordx4 v233, s[40:43], s75 offen lds\n\t" \
  "s_add_u32 m0, s79, 36864\n\t" \
  "s_add_u32 s75, s58, 3538944\n\t" \
  "buffer_load_dwordx4 v233, s[40:43], s75 offen lds\n\t" \
  "s_add_u32 m0, s79, 40960\n\t" \
  "s_add_u32 s75, s58, 3833856\n\t" \
  "buffer_load_dwordx4 v233, s[40:43], s75 offen lds\n\t" \
  "s_add_u32 m0, s79, 45056\n\t" \
  "s_add_u32 s75, s58, 4128768\n\t" \
  "buffer_load_dwordx4 v233, s[40:43], s75 offen lds\n\t" \
  "s_waitcnt vmcnt(15)\n\t" \
  "s_barrier\n\t" \
  "s_cmp_eq_u32 %[wi], 0\n\t" \
  "s_cbranch_scc0 L_S12_%=\n\t" \
  "s_mov_b64 exec, 0xffff\n\t" \
  "buffer_store_dword v237, v236, s[48:51], %[sfl] offen offset:128 sc1\n\t" \
  "s_mov_b64 exec, -1\n\t" \
  "L_S12_%=:\n\t" \
  "buffer_load_dword v240, v235, s[48:51], %[sfl] offen offset:0 sc1\n\t" \
  "s_add_u32 m0, s79, 49152\n\t" \
  "s_add_u32 s75, s58, 4423680\n\t" \
  "buffer_load_dwordx4 v233, s[40:43], s75 offen lds\n\t" \
  "s_add_u32 m0, s79, 53248\n\t" \
  "s_add_u32 s75, s58, 4718592\n\t" \
  "buffer_load_dwordx4 v233, s[40:43], s75 offen lds\n\t" \
  "s_add_u32 m0, s79, 57344\n\t" \
  "s_add_u32 s75, s58, 5013504\n\t" \
  "buffer_load_dwordx4 v233, s[40:43], s75 offen lds\n\t" \
  "s_barrier\n\t" \
  "s_cmp_eq_u32 %[wi], 0\n\t" \
  "s_cbranch_scc0 L_S13_%=\n\t" \
  "s_mov_b64 exec, 0xffff\n\t" \
  "buffer_store_dword v237, v236, s[48:51], %[sfl] offen offset:256 sc1\n\t" \
  "s_mov_b64 exec, -1\n\t" \
  "L_S13_%=:\n\t" \
  "s_mov_b32 s82, 0\n\t" \
  "s_waitcnt vmcnt(3)\n\t" \
  "L_P14_%=:\n\t" \
  "v_cmp_eq_u32 vcc, v237, v240\n\t" \
  "s_cmp_eq_u32 vcc_lo, -1\n\t" \
  "s_cbranch_scc1 L_Q14_%=\n\t" \
  "s_add_u32 s82, s82, 1\n\t" \
  "s_cmp_gt_u32 s82, 400000\n\t" \
  "s_cbranch_scc1 L_Q14_%=\n\t" \
  "buffer_load_dword v240, v235, s[48:51], %[sfl] offen offset:0 sc1\n\t" \
  "s_waitcnt vmcnt(0)\n\t" \
  "s_branch L_P14_%=\n\t" \
  "L_Q14_%=:\n\t" \
  "buffer_load_dword v241, v235, s[48:51], %[sfl] offen offset:128 sc1\n\t" \
  "s_add_u32 m0, s80, 24576\n\t" \
  "s_add_u32 s75, s72, 0\n\t" \
  "buffer_load_dwordx4 v232, s[48:51], s75 offen sc1 lds\n\t" \
  "s_add_u32 m0, s80, 25600\n\t" \
  "s_add_u32 s75, s72, 1024\n\t" \
  "buffer_load_dwordx4 v232, s[48:51], s75 offen sc1 lds\n\t" \
  "s_add_u32 m0, s80, 26624\n\t" \
  "s_add_u32 s75, s73, 0\n\t" \
  "buffer_load_dwordx4 v232, s[48:51], s75 offen sc1 lds\n\t" \
  "s_add_u32 m0, s80, 61440\n\t" \
  "s_add_u32 s75, s73, 1024\n\t" \
  "buffer_load_dwordx4 v232, s[48:51], s75 offen sc1 lds\n\t" \
  "s_add_u32 m0, s80, 62464\n\t" \
  "s_add_u32 s75, s74, 0\n\t" \
  "buffer_load_dwordx4 v232, s[48:51], s75 offen sc1 lds\n\t" \
  "s_add_u32 m0, s80, 63488\n\t" \
  "s_add_u32 s75, s74, 1024\n\t" \
  "buffer_load_dwordx4 v232, s[48:51], s75 offen sc1 lds\n\t" \
  "s_add_u32 m0, s79, 73728\n\t" \
  "s_add_u32 s75, s64, s59\n\t" \
  "buffer_load_dwordx4 v234, s[44:47], s75 offen lds\n\t" \
  "s_add_u32 m0, s79, 77824\n\t" \
  "s_add_u32 s75, s65, s59\n\t" \
  "buffer_load_dwordx4 v234, s[44:47], s75 offen lds\n\t" \
  "s_add_u32 m0, s79, 81920\n\t" \
  "s_add_u32 s75, s66, s59\n\t" \
  "buffer_load_dwordx4 v234, s[44:47], s75 offen lds\n\t" \
  "s_barrier\n\t" \
  "s_add_u32 m0, s80, 86016\n\t" \
  "s_add_u32 s75, s72, 2048\n\t" \
  "buffer_load_dwordx4 v232, s[48:51], s75 offen sc1 lds\n\t" \
  "s_add_u32 m0, s80, 87040\n\t" \
  "s_add_u32 s75, s72, 3072\n\t" \
  "buffer_load_dwordx4 v232, s[48:51], s75 offen sc1 lds\n\t" \
  "s_add_u32 m0, s80, 88064\n\t" \
  "s_add_u32 s75, s73, 2048\n\t" \
  "buffer_load_dwordx4 v232, s[48:51], s75 offen sc1 lds\n\t" \
  "s_add_u32 m0, s79, 98304\n\t" \
  "s_add_u32 s75, s64, s59\n\t" \
  "s_add_u32 s75, s75, 128\n\t" \
  "buffer_load_dwordx4 v234, s[44:47], s75 offen lds\n\t" \
  "s_add_u32 m0, s79, 102400\n\t" \
  "s_add_u32 s75, s65, s59\n\t" \
  "s_add_u32 s75, s75, 128\n\t" \
  "buffer_load_dwordx4 v234, s[44:47], s75 offen lds\n\t" \
  "s_add_u32 m0, s79, 106496\n\t" \
  "s_add_u32 s75, s66, s59\n\t" \
  "s_add_u32 s75, s75, 128\n\t" \
  "buffer_load_dwordx4 v234, s[44:47], s75 offen lds\n\t" \
  "s_add_u32 m0, s80, 110592\n\t" \
  "s_add_u32 s75, s73, 3072\n\t" \
  "buffer_load_dwordx4 v232, s[48:51], s75 offen sc1 lds\n\t" \
  "s_add_u32 m0, s80, 111616\n\t" \
  "s_add_u32 s75, s74, 2048\n\t" \
  "buffer_load_dwordx4 v232, s[48:51], s75 offen sc1 lds\n\t" \
  "s_add_u32 m0, s80, 112640\n\t" \
  "s_add_u32 s75, s74, 3072\n\t" \
  "buffer_load_dwordx4 v232, s[48:51], s75 offen sc1 lds\n\t" \
  "s_waitcnt vmcnt(12)\n\t" \
  "s_barrier\n\t" \
  "s_cmp_eq_u32 %[wi], 0\n\t" \
  "s_cbranch_scc0 L_S15_%=\n\t" \
  "s_mov_b64 exec, 0xffff\n\t" \
  "buffer_store_dword v237, v236, s[48:51], %[sfl] offen offset:384 sc1\n\t" \
  "s_mov_b64 exec, -1\n\t" \
  "L_S15_%=:\n\t" \
  "s_mov_b32 s82, 0\n\t" \
  "L_P16_%=:\n\t" \
  "v_cmp_eq_u32 vcc, v237, v241\n\t" \
  "s_cmp_eq_u32 vcc_lo, -1\n\t" \
  "s_cbranch_scc1 L_Q16_%=\n\t" \
  "s_add_u32 s82, s82, 1\n\t" \
  "s_cmp_gt_u32 s82, 400000\n\t" \
  "s_cbranch_scc1 L_Q16_%=\n\t" \
  "buffer_load_dword v241, v235, s[48:51], %[sfl] offen offset:128 sc1\n\t" \
  "s_waitcnt vmcnt(0)\n\t" \
  "s_branch L_P16_%=\n\t" \
  "L_Q16_%=:\n\t" \
  "buffer_load_dword v242, v235, s[48:51], %[sfl] offen offset:256 sc1\n\t" \
  "s_add_u32 m0, s79, 0\n\t" \
  "s_add_u32 s75, s64, s59\n\t" \
  "s_add_u32 s75, s75, 36864\n\t" \
  "buffer_load_dwordx4 v234, s[44:47], s75 offen lds\n\t" \
  "s_add_u32 m0, s79, 4096\n\t" \
  "s_add_u32 s75, s65, s59\n\t" \
  "s_add_u32 s75, s75, 36864\n\t" \
  "buffer_load_dwordx4 v234, s[44:47], s75 offen lds\n\t" \
  "s_add_u32 m0, s79, 8192\n\t" \
  "s_add_u32 s75, s66, s59\n\t" \
  "s_add_u32 s75, s75, 36864\n\t" \
  "buffer_load_dwordx4 v234, s[44:47], s75 offen lds\n\t" \
  "s_add_u32 m0, s80, 122880\n\t" \
  "s_add_u32 s75, s72, 4096\n\t" \
  "buffer_load_dwordx4 v232, s[48:51], s75 offen sc1 lds\n\t" \
  "s_add_u32 m0, s80, 123904\n\t" \
  "s_add_u32 s75, s72, 5120\n\t" \
  "buffer_load_dwordx4 v232, s[48:51], s75 offen sc1 lds\n\t" \
  "s_add_u32 m0, s80, 124928\n\t" \
  "s_add_u32 s75, s73, 4096\n\t" \
  "buffer_load_dwordx4 v232, s[48:51], s75 offen sc1 lds\n\t" \
  "s_add_u32 m0, s79, 135168\n\t" \
  "s_add_u32 s75, s64, s59\n\t" \
  "s_add_u32 s75, s75, 36992\n\t" \
  "buffer_load_dwordx4 v234, s[44:47], s75 offen lds\n\t" \
  "s_add_u32 m0, s79, 139264\n\t" \
  "s_add_u32 s75, s65, s59\n\t" \
  "s_add_u32 s75, s75, 36992\n\t" \
  "buffer_load_dwordx4 v234, s[44:47], s75 offen lds\n\t" \
  "s_add_u32 m0, s79, 143360\n\t" \
  "s_add_u32 s75, s66, s59\n\t" \
  "s_add_u32 s75, s75, 36992\n\t" \
  "buffer_load_dwordx4 v234, s[44:47], s75 offen lds\n\t" \
  "s_waitcnt vmcnt(13)\n\t" \
  "s_barrier\n\t" \
  "s_add_u32 m0, s80, 12288\n\t" \
  "s_add_u32 s75, s73, 5120\n\t" \
  "buffer_load_dwordx4 v232, s[48:51], s75 offen sc1 lds\n\t" \
  "s_add_u32 m0, s80, 13312\n\t" \
  "s_add_u32 s75, s74, 4096\n\t" \
  "buffer_load_dwordx4 v232, s[48:51], s75 offen sc1 lds\n\t" \
  "s_add_u32 m0, s80, 14336\n\t" \
  "s_add_u32 s75, s74, 5120\n\t" \
  "buffer_load_dwordx4 v232, s[48:51], s75 offen sc1 lds\n\t" \
  "s_add_u32 m0, s79, 36864\n\t" \
  "s_add_u32 s75, s64, s60\n\t" \
  "buffer_load_dwordx4 v234, s[44:47], s75 offen lds\n\t" \
  "s_add_u32 m0, s79, 40960\n\t" \
  "s_add_u32 s75, s65, s60\n\t" \
  "buffer_load_dwordx4 v234, s[44:47], s75 offen lds\n\t" \
  "s_add_u32 m0, s79, 45056\n\t" \
  "s_add_u32 s75, s66, s60\n\t" \
  "buffer_load_dwordx4 v234, s[44:47], s75 offen lds\n\t" \
  "s_add_u32 m0, s80, 147456\n\t" \
  "s_add_u32 s75, s71, 0\n\t" \
  "buffer_load_dwordx4 v232, s[48:51], s75 offen sc1 lds\n\t" \
  "s_add_u32 m0, s80, 148480\n\t" \
  "s_add_u32 s75, s71, 1024\n\t" \
  "buffer_load_dwordx4 v232, s[48:51], s75 offen sc1 lds\n\t" \
  "s_add_u32 m0, s80, 149504\n\t" \
  "s_add_u32 s75, s71, 2048\n\t" \
  "buffer_load_dwordx4 v232, s[48:51], s75 offen sc1 lds\n\t" \
  "s_waitcnt vmcnt(15)\n\t" \
  "s_barrier\n\t" \
  "s_cmp_eq_u32 %[wi], 0\n\t" \
  "s_cbranch_scc0 L_S17_%=\n\t" \
  "s_mov_b64 exec, 0xffff\n\t" \
  "buffer_store_dword v237, v236, s[48:51], %[sfl] offen offset:512 sc1\n\t" \
  "s_mov_b64 exec, -1\n\t" \
  "L_S17_%=:\n\t" \
  "s_mov_b32 s82, 0\n\t" \
  "L_P18_%=:\n\t" \
  "v_cmp_eq_u32 vcc, v237, v242\n\t" \
  "s_cmp_eq_u32 vcc_lo, -1\n\t" \
  "s_cbranch_scc1 L_Q18_%=\n\t" \
  "s_add_u32 s82, s82, 1\n\t" \
  "s_cmp_gt_u32 s82, 400000\n\t" \
  "s_cbranch_scc1 L_Q18_%=\n\t" \
  "buffer_load_dword v242, v235, s[48:51], %[sfl] offen offset:256 sc1\n\t" \
  "s_waitcnt vmcnt(0)\n\t" \
  "s_branch L_P18_%=\n\t" \
  "L_Q18_%=:\n\t" \
  "buffer_load_dword v243, v235, s[48:51], %[sfl] offen offset:384 sc1\n\t" \
  "s_add_u32 m0, s79, 24576\n\t" \
  "s_add_u32 s75, s64, s60\n\t" \
  "s_add_u32 s75, s75, 128\n\t" \
  "buffer_load_dwordx4 v234, s[44:47], s75 offen lds\n\t" \
  "s_add_u32 m0, s79, 28672\n\t" \
  "s_add_u32 s75, s65, s60\n\t" \
  "s_add_u32 s75, s75, 128\n\t" \
  "buffer_load_dwordx4 v234, s[44:47], s75 offen lds\n\t" \
  "s_add_u32 m0, s79, 32768\n\t" \
  "s_add_u32 s75, s66, s60\n\t" \
  "s_add_u32 s75, s75, 128\n\t" \
  "buffer_load_dwordx4 v234, s[44:47], s75 offen lds\n\t" \
  "s_add_u32 m0, s80, 49152\n\t" \
  "s_add_u32 s75, s71, 3072\n\t" \
  "buffer_load_dwordx4 v232, s[48:51], s75 offen sc1 lds\n\t" \
  "s_add_u32 m0, s80, 50176\n\t" \
  "s_add_u32 s75, s71, 4096\n\t" \
  "buffer_load_dwordx4 v232, s[48:51], s75 offen sc1 lds\n\t" \
  "s_add_u32 m0, s80, 51200\n\t" \
  "s_add_u32 s75, s71, 5120\n\t" \
  "buffer_load_dwordx4 v232, s[48:51], s75 offen sc1 lds\n\t" \
  "s_add_u32 m0, s79, 61440\n\t" \
  "s_add_u32 s75, s63, s59\n\t" \
  "s_add_u32 s75, s75, s81\n\t" \
  "buffer_load_dwordx4 v234, s[44:47], s75 offen lds\n\t" \
  "s_add_u32 m0, s79, 65536\n\t" \
  "s_add_u32 s75, s63, s59\n\t" \
  "s_add_u32 s75, s75, s81\n\t" \
  "s_add_u32 s75, s75, 36864\n\t" \
  "buffer_load_dwordx4 v234, s[44:47], s75 offen lds\n\t" \
  "s_add_u32 m0, s79, 69632\n\t" \
  "s_add_u32 s75, s63, s60\n\t" \
  "s_add_u32 s75, s75, s81\n\t" \
  "buffer_load_dwordx4 v234, s[44:47], s75 offen lds\n\t" \
  "s_waitcnt vmcnt(13)\n\t" \
  "s_barrier\n\t" \
  "s_add_u32 m0, s79, 73728\n\t" \
  "s_add_u32 s75, s63, s59\n\t" \
  "buffer_load_dwordx4 v234, s[44:47], s75 offen lds\n\t" \
  "s_add_u32 m0, s79, 77824\n\t" \
  "s_add_u32 s75, s63, s59\n\t" \
  "s_add_u32 s75, s75, 36864\n\t" \
  "buffer_load_dwordx4 v234, s[44:47], s75 offen lds\n\t" \
  "s_add_u32 m0, s79, 81920\n\t" \
  "s_add_u32 s75, s63, s60\n\t" \
  "buffer_load_dwordx4 v234, s[44:47], s75 offen lds\n\t" \
  "s_add_u32 m0, s80, 86016\n\t" \
  "s_add_u32 s75, s72, 6144\n\t" \
  "buffer_load_dwordx4 v232, s[48:51], s75 offen sc1 lds\n\t" \
  "s_add_u32 m0, s80, 87040\n\t" \
  "s_add_u32 s75, s72, 7168\n\t" \
  "buffer_load_dwordx4 v232, s[48:51], s75 offen sc1 lds\n\t" \
  "s_add_u32 m0, s80, 88064\n\t" \
  "s_add_u32 s75, s73, 6144\n\t" \
  "buffer_load_dwordx4 v232, s[48:51], s75 offen sc1 lds\n\t" \
  "s_add_u32 m0, s79, 98304\n\t" \
  "s_add_u32 s75, s63, s59\n\t" \
  "s_add_u32 s75, s75, 128\n\t" \
  "buffer_load_dwordx4 v234, s[44:47], s75 offen lds\n\t" \
  "s_add_u32 m0, s79, 102400\n\t" \
  "s_add_u32 s75, s63, s59\n\t" \
  "s_add_u32 s75, s75, 36992\n\t" \
  "buffer_load_dwordx4 v234, s[44:47], s75 offen lds\n\t" \
  "s_add_u32 m0, s79, 106496\n\t" \
  "s_add_u32 s75, s63, s60\n\t" \
  "s_add_u32 s75, s75, 128\n\t" \
  "buffer_load_dwordx4 v234, s[44:47], s75 offen lds\n\t" \
  "s_waitcnt vmcnt(12)\n\t" \
  "s_barrier\n\t" \
  "s_cmp_eq_u32 %[wi], 0\n\t" \
  "s_cbranch_scc0 L_S19_%=\n\t" \
  "s_mov_b64 exec, 0xffff\n\t" \
  "buffer_store_dword v237, v236, s[48:51], %[sfl] offen offset:640 sc1\n\t" \
  "s_mov_b64 exec, -1\n\t" \
  "L_S19_%=:\n\t" \
  "s_mov_b32 s82, 0\n\t" \
  "L_P20_%=:\n\t" \
  "v_cmp_eq_u32 vcc, v237, v243\n\t" \
  "s_cmp_eq_u32 vcc_lo, -1\n\t" \
  "s_cbranch_scc1 L_Q20_%=\n\t" \
  "s_add_u32 s82, s82, 1\n\t" \
  "s_cmp_gt_u32 s82, 400000\n\t" \
  "s_cbranch_scc1 L_Q20_%=\n\t" \
  "buffer_load_dword v243, v235, s[48:51], %[sfl] offen offset:384 sc1\n\t" \
  "s_waitcnt vmcnt(0)\n\t" \
  "s_branch L_P20_%=\n\t" \
  "L_Q20_%=:\n\t" \
  "buffer_load_dword v244, v235, s[48:51], %[sfl] offen offset:512 sc1\n\t" \
  "s_add_u32 m0, s80, 110592\n\t" \
  "s_add_u32 s75, s73, 7168\n\t" \
  "buffer_load_dwordx4 v232, s[48:51], s75 offen sc1 lds\n\t" \
  "s_add_u32 m0, s80, 111616\n\t" \
  "s_add_u32 s75, s74, 6144\n\t" \
  "buffer_load_dwordx4 v232, s[48:51], s75 offen sc1 lds\n\t" \
  "s_add_u32 m0, s80, 112640\n\t" \
  "s_add_u32 s75, s74, 7168\n\t" \
  "buffer_load_dwordx4 v232, s[48:51], s75 offen sc1 lds\n\t" \
  "s_add_u32 m0, s79, 0\n\t" \
  "s_add_u32 s75, s64, s60\n\t" \
  "s_add_u32 s75, s75, 36864\n\t" \
  "buffer_load_dwordx4 v234, s[44:47], s75 offen lds\n\t" \
  "s_add_u32 m0, s79, 4096\n\t" \
  "s_add_u32 s75, s65, s60\n\t" \
  "s_add_u32 s75, s75, 36864\n\t" \
  "buffer_load_dwordx4 v234, s[44:47], s75 offen lds\n\t" \
  "s_add_u32 m0, s79, 8192\n\t" \
  "s_add_u32 s75, s66, s60\n\t" \
  "s_add_u32 s75, s75, 36864\n\t" \
  "buffer_load_dwordx4 v234, s[44:47], s75 offen lds\n\t" \
  "s_add_u32 m0, s80, 135168\n\t" \
  "s_add_u32 s75, s72, 8192\n\t" \
  "buffer_load_dwordx4 v232, s[48:51], s75 offen sc1 lds\n\t" \
  "s_add_u32 m0, s80, 136192\n\t" \
  "s_add_u32 s75, s72, 9216\n\t" \
  "buffer_load_dwordx4 v232, s[48:51], s75 offen sc1 lds\n\t" \
  "s_add_u32 m0, s80, 137216\n\t" \
  "s_add_u32 s75, s73, 8192\n\t" \
  "buffer_load_dwordx4 v232, s[48:51], s75 offen sc1 lds\n\t" \
  "s_waitcnt vmcnt(16)\n\t" \
  "s_barrier\n\t" \
  "s_add_u32 m0, s79, 12288\n\t" \
  "s_add_u32 s75, s64, s60\n\t" \
  "s_add_u32 s75, s75, 36992\n\t" \
  "buffer_load_dwordx4 v234, s[44:47], s75 offen lds\n\t" \
  "s_add_u32 m0, s79, 16384\n\t" \
  "s_add_u32 s75, s65, s60\n\t" \
  "s_add_u32 s75, s75, 36992\n\t" \
  "buffer_load_dwordx4 v234, s[44:47], s75 offen lds\n\t" \
  "s_add_u32 m0, s79, 20480\n\t" \
  "s_add_u32 s75, s66, s60\n\t" \
  "s_add_u32 s75, s75, 36992\n\t" \
  "buffer_load_dwordx4 v234, s[44:47], s75 offen lds\n\t" \
  "s_add_u32 m0, s80, 36864\n\t" \
  "s_add_u32 s75, s73, 9216\n\t" \
  "buffer_load_dwordx4 v232, s[48:51], s75 offen sc1 lds\n\t" \
  "s_add_u32 m0, s80, 37888\n\t" \
  "s_add_u32 s75, s74, 8192\n\t" \
  "buffer_load_dwordx4 v232, s[48:51], s75 offen sc1 lds\n\t" \
  "s_add_u32 m0, s80, 38912\n\t" \
  "s_add_u32 s75, s74, 9216\n\t" \
  "buffer_load_dwordx4 v232, s[48:51], s75 offen sc1 lds\n\t" \
  "s_add_u32 m0, s79, 122880\n\t" \
  "s_add_u32 s75, s64, s61\n\t" \
  "buffer_load_dwordx4 v234, s[44:47], s75 offen lds\n\t" \
  "s_add_u32 m0, s79, 126976\n\t" \
  "s_add_u32 s75, s65, s61\n\t" \
  "buffer_load_dwordx4 v234, s[44:47], s75 offen lds\n\t" \
  "s_waitcnt vmcnt(14)\n\t" \
  "s_barrier\n\t" \
  "s_cmp_eq_u32 %[wi], 0\n\t" \
  "s_cbranch_scc0 L_S21_%=\n\t" \
  "s_mov_b64 exec, 0xffff\n\t" \
  "buffer_store_dword v237, v236, s[48:51], %[sfl] offen offset:768 sc1\n\t" \
  "s_mov_b64 exec, -1\n\t" \
  "L_S21_%=:\n\t" \
  "s_mov_b32 s82, 0\n\t" \
  "L_P22_%=:\n\t" \
  "v_cmp_eq_u32 vcc, v237, v244\n\t" \
  "s_cmp_eq_u32 vcc_lo, -1\n\t" \
  "s_cbranch_scc1 L_Q22_%=\n\t" \
  "s_add_u32 s82, s82, 1\n\t" \
  "s_cmp_gt_u32 s82, 400000\n\t" \
  "s_cbranch_scc1 L_Q22_%=\n\t" \
  "buffer_load_dword v244, v235, s[48:51], %[sfl] offen offset:512 sc1\n\t" \
  "s_waitcnt vmcnt(0)\n\t" \
  "s_branch L_P22_%=\n\t" \
  "L_Q22_%=:\n\t" \
  "buffer_load_dword v245, v235, s[48:51], %[sfl] offen offset:640 sc1\n\t" \
  "s_add_u32 m0, s79, 131072\n\t" \
  "s_add_u32 s75, s66, s61\n\t" \
  "buffer_load_dwordx4 v234, s[44:47], s75 offen lds\n\t" \
  "s_add_u32 m0, s80, 24576\n\t" \
  "s_add_u32 s75, s72, 10240\n\t" \
  "buffer_load_dwordx4 v232, s[48:51], s75 offen sc1 lds\n\t" \
  "s_add_u32 m0, s80, 25600\n\t" \
  "s_add_u32 s75, s72, 11264\n\t" \
  "buffer_load_dwordx4 v232, s[48:51], s75 offen sc1 lds\n\t" \
  "s_add_u32 m0, s80, 26624\n\t" \
  "s_add_u32 s75, s73, 10240\n\t" \
  "buffer_load_dwordx4 v232, s[48:51], s75 offen sc1 lds\n\t" \
  "s_add_u32 m0, s79, 49152\n\t" \
  "s_add_u32 s75, s64, s61\n\t" \
  "s_add_u32 s75, s75, 128\n\t" \
  "buffer_load_dwordx4 v234, s[44:47], s75 offen lds\n\t" \
  "s_add_u32 m0, s79, 53248\n\t" \
  "s_add_u32 s75, s65, s61\n\t" \
  "s_add_u32 s75, s75, 128\n\t" \
  "buffer_load_dwordx4 v234, s[44:47], s75 offen lds\n\t" \
  "s_add_u32 m0, s79, 57344\n\t" \
  "s_add_u32 s75, s66, s61\n\t" \
  "s_add_u32 s75, s75, 128\n\t" \
  "buffer_load_dwordx4 v234, s[44:47], s75 offen lds\n\t" \
  "s_add_u32 m0, s80, 61440\n\t" \
  "s_add_u32 s75, s73, 11264\n\t" \
  "buffer_load_dwordx4 v232, s[48:51], s75 offen sc1 lds\n\t" \
  "s_waitcnt vmcnt(14)\n\t" \
  "s_barrier\n\t" \
  "s_add_u32 m0, s80, 62464\n\t" \
  "s_add_u32 s75, s74, 10240\n\t" \
  "buffer_load_dwordx4 v232, s[48:51], s75 offen sc1 lds\n\t" \
  "s_add_u32 m0, s80, 63488\n\t" \
  "s_add_u32 s75, s74, 11264\n\t" \
  "buffer_load_dwordx4 v232, s[48:51], s75 offen sc1 lds\n\t" \
  "s_add_u32 m0, s79, 147456\n\t" \
  "s_add_u32 s75, s64, s61\n\t" \
  "s_add_u32 s75, s75, 36864\n\t" \
  "buffer_load_dwordx4 v234, s[44:47], s75 offen lds\n\t" \
  "s_add_u32 m0, s79, 151552\n\t" \
  "s_add_u32 s75, s65, s61\n\t" \
  "s_add_u32 s75, s75, 36864\n\t" \
  "buffer_load_dwordx4 v234, s[44:47], s75 offen lds\n\t" \
  "s_add_u32 m0, s79, 155648\n\t" \
  "s_add_u32 s75, s66, s61\n\t" \
  "s_add_u32 s75, s75, 36864\n\t" \
  "buffer_load_dwordx4 v234, s[44:47], s75 offen lds\n\t" \
  "s_add_u32 m0, s80, 73728\n\t" \
  "s_add_u32 s75, s71, 6144\n\t" \
  "buffer_load_dwordx4 v232, s[48:51], s75 offen sc1 lds\n\t" \
  "s_add_u32 m0, s80, 74752\n\t" \
  "s_add_u32 s75, s71, 7168\n\t" \
  "buffer_load_dwordx4 v232, s[48:51], s75 offen sc1 lds\n\t" \
  "s_add_u32 m0, s80, 75776\n\t" \
  "s_add_u32 s75, s71, 8192\n\t" \
  "buffer_load_dwordx4 v232, s[48:51], s75 offen sc1 lds\n\t" \
  "s_waitcnt vmcnt(15)\n\t" \
  "s_barrier\n\t" \
  "s_cmp_eq_u32 %[wi], 0\n\t" \
  "s_cbranch_scc0 L_S23_%=\n\t" \
  "s_mov_b64 exec, 0xffff\n\t" \
  "buffer_store_dword v237, v236, s[48:51], %[sfl] offen offset:896 sc1\n\t" \
  "s_mov_b64 exec, -1\n\t" \
  "L_S23_%=:\n\t" \
  "s_mov_b32 s82, 0\n\t" \
  "L_P24_%=:\n\t" \
  "v_cmp_eq_u32 vcc, v237, v245\n\t" \
  "s_cmp_eq_u32 vcc_lo, -1\n\t" \
  "s_cbranch_scc1 L_Q24_%=\n\t" \
  "s_add_u32 s82, s82, 1\n\t" \
  "s_cmp_gt_u32 s82, 400000\n\t" \
  "s_cbranch_scc1 L_Q24_%=\n\t" \
  "buffer_load_dword v245, v235, s[48:51], %[sfl] offen offset:640 sc1\n\t" \
  "s_waitcnt vmcnt(0)\n\t" \
  "s_branch L_P24_%=\n\t" \
  "L_Q24_%=:\n\t" \
  "buffer_load_dword v246, v235, s[48:51], %[sfl] offen offset:768 sc1\n\t" \
  "s_add_u32 m0, s79, 0\n\t" \
  "s_add_u32 s75, s64, s61\n\t" \
  "s_add_u32 s75, s75, 36992\n\t" \
  "buffer_load_dwordx4 v234, s[44:47], s75 offen lds\n\t" \
  "s_add_u32 m0, s79, 4096\n\t" \
  "s_add_u32 s75, s65, s61\n\t" \
  "s_add_u32 s75, s75, 36992\n\t" \
  "buffer_load_dwordx4 v234, s[44:47], s75 offen lds\n\t" \
  "s_add_u32 m0, s79, 8192\n\t" \
  "s_add_u32 s75, s66, s61\n\t" \
  "s_add_u32 s75, s75, 36992\n\t" \
  "buffer_load_dwordx4 v234, s[44:47], s75 offen lds\n\t" \
  "s_add_u32 m0, s80, 86016\n\t" \
  "s_add_u32 s75, s71, 9216\n\t" \
  "buffer_load_dwordx4 v232, s[48:51], s75 offen sc1 lds\n\t" \
  "s_add_u32 m0, s80, 87040\n\t" \
  "s_add_u32 s75, s71, 10240\n\t" \
  "buffer_load_dwordx4 v232, s[48:51], s75 offen sc1 lds\n\t" \
  "s_add_u32 m0, s80, 88064\n\t" \
  "s_add_u32 s75, s71, 11264\n\t" \
  "buffer_load_dwordx4 v232, s[48:51], s75 offen sc1 lds\n\t" \
  "s_add_u32 m0, s79, 98304\n\t" \
  "s_add_u32 s75, s63, s60\n\t" \
  "s_add_u32 s75, s75, s81\n\t" \
  "s_add_u32 s75, s75, 36864\n\t" \
  "buffer_load_dwordx4 v234, s[44:47], s75 offen lds\n\t" \
  "s_add_u32 m0, s79, 102400\n\t" \
  "s_add_u32 s75, s63, s61\n\t" \
  "s_add_u32 s75, s75, s81\n\t" \
  "buffer_load_dwordx4 v234, s[44:47], s75 offen lds\n\t" \
  "s_waitcnt vmcnt(12)\n\t" \
  "s_barrier\n\t" \
  "s_add_u32 m0, s79, 106496\n\t" \
  "s_add_u32 s75, s63, s61\n\t" \
  "s_add_u32 s75, s75, s81\n\t" \
  "s_add_u32 s75, s75, 36864\n\t" \
  "buffer_load_dwordx4 v234, s[44:47], s75 offen lds\n\t" \
  "s_add_u32 m0, s79, 110592\n\t" \
  "s_add_u32 s75, s63, s60\n\t" \
  "s_add_u32 s75, s75, 36864\n\t" \
  "buffer_load_dwordx4 v234, s[44:47], s75 offen lds\n\t" \
  "s_add_u32 m0, s79, 114688\n\t" \
  "s_add_u32 s75, s63, s61\n\t" \
  "buffer_load_dwordx4 v234, s[44:47], s75 offen lds\n\t" \
  "s_add_u32 m0, s79, 118784\n\t" \
  "s_add_u32 s75, s63, s61\n\t" \
  "s_add_u32 s75, s75, 36864\n\t" \
  "buffer_load_dwordx4 v234, s[44:47], s75 offen lds\n\t" \
  "s_add_u32 m0, s80, 12288\n\t" \
  "s_add_u32 s75, s72, 12288\n\t" \
  "buffer_load_dwordx4 v232, s[48:51], s75 offen sc1 lds\n\t" \
  "s_add_u32 m0, s80, 13312\n\t" \
  "s_add_u32 s75, s72, 13312\n\t" \
  "buffer_load_dwordx4 v232, s[48:51], s75 offen sc1 lds\n\t" \
  "s_add_u32 m0, s80, 14336\n\t" \
  "s_add_u32 s75, s73, 12288\n\t" \
  "buffer_load_dwordx4 v232, s[48:51], s75 offen sc1 lds\n\t" \
  "s_add_u32 m0, s79, 36864\n\t" \
  "s_add_u32 s75, s63, s60\n\t" \
  "s_add_u32 s75, s75, 36992\n\t" \
  "buffer_load_dwordx4 v234, s[44:47], s75 offen lds\n\t" \
  "s_mov_b32 s75, 196608\n\t" \
  "buffer_load_dwordx4 v[228:231], v232, s[48:51], s75 offen sc1\n\t" \
  "s_waitcnt vmcnt(11)\n\t" \
  "s_barrier\n\t" \
  "s_mov_b32 s82, 0\n\t" \
  "L_P25_%=:\n\t" \
  "v_cmp_eq_u32 vcc, v237, v246\n\t" \
  "s_cmp_eq_u32 vcc_lo, -1\n\t" \
  "s_cbranch_scc1 L_Q25_%=\n\t" \
  "s_add_u32 s82, s82, 1\n\t" \
  "s_cmp_gt_u32 s82, 400000\n\t" \
  "s_cbranch_scc1 L_Q25_%=\n\t" \
  "buffer_load_dword v246, v235, s[48:51], %[sfl] offen offset:768 sc1\n\t" \
  "s_waitcnt vmcnt(0)\n\t" \
  "s_branch L_P25_%=\n\t" \
  "L_Q25_%=:\n\t" \
  "buffer_load_dword v247, v235, s[48:51], %[sfl] offen offset:896 sc1\n\t" \
  "s_add_u32 m0, s79, 40960\n\t" \
  "s_add_u32 s75, s63, s61\n\t" \
  "s_add_u32 s75, s75, 128\n\t" \
  "buffer_load_dwordx4 v234, s[44:47], s75 offen lds\n\t" \
  "s_add_u32 m0, s79, 45056\n\t" \
  "s_add_u32 s75, s63, s61\n\t" \
  "s_add_u32 s75, s75, 36992\n\t" \
  "buffer_load_dwordx4 v234, s[44:47], s75 offen lds\n\t" \
  "s_add_u32 m0, s80, 135168\n\t" \
  "s_add_u32 s75, s73, 13312\n\t" \
  "buffer_load_dwordx4 v232, s[48:51], s75 offen sc1 lds\n\t" \
  "s_add_u32 m0, s80, 136192\n\t" \
  "s_add_u32 s75, s74, 12288\n\t" \
  "buffer_load_dwordx4 v232, s[48:51], s75 offen sc1 lds\n\t" \
  "s_add_u32 m0, s80, 137216\n\t" \
  "s_add_u32 s75, s74, 13312\n\t" \
  "buffer_load_dwordx4 v232, s[48:51], s75 offen sc1 lds\n\t" \
  "s_add_u32 m0, s79, 49152\n\t" \
  "s_add_u32 s75, s64, s62\n\t" \
  "buffer_load_dwordx4 v234, s[44:47], s75 offen lds\n\t" \
  "s_add_u32 m0, s79, 53248\n\t" \
  "s_add_u32 s75, s65, s62\n\t" \
  "buffer_load_dwordx4 v234, s[44:47], s75 offen lds\n\t" \
  "s_add_u32 m0, s79, 57344\n\t" \
  "s_add_u32 s75, s66, s62\n\t" \
  "buffer_load_dwordx4 v234, s[44:47], s75 offen lds\n\t" \
  "s_waitcnt vmcnt(14)\n\t" \
  "s_barrier\n\t" \
  "s_mov_b32 s82, 0\n\t" \
  "s_waitcnt vmcnt(9)\n\t" \
  "L_PY_%=:\n\t" \
  "v_xor_b32 v228, v228, v237\n\t" \
  "v_xor_b32 v229, v229, v237\n\t" \
  "v_xor_b32 v230, v230, v237\n\t" \
  "v_xor_b32 v231, v231, v237\n\t" \
  "v_or3_b32 v228, v228, v229, v230\n\t" \
  "v_or_b32 v228, v228, v231\n\t" \
  "v_cmp_ne_u32 vcc, 0, v228\n\t" \
  "s_cmp_eq_u64 vcc, 0\n\t" \
  "s_cbranch_scc1 L_QY_%=\n\t" \
  "s_add_u32 s82, s82, 1\n\t" \
  "s_cmp_gt_u32 s82, 400000\n\t" \
  "s_cbranch_scc1 L_QY_%=\n\t" \
  "s_mov_b32 s75, 196608\n\t" \
  "buffer_load_dwordx4 v[228:231], v232, s[48:51], s75 offen sc1\n\t" \
  "s_waitcnt vmcnt(0)\n\t" \
  "s_branch L_PY_%=\n\t" \
  "L_QY_%=:\n\t" \
  "buffer_load_dwordx4 v[124:127], %[ay], s[48:51], 0 offen sc1\n\t" \
  "s_movk_i32 s75, 0x1000\n\t" \
  "buffer_load_dwordx4 v[128:131], %[ay], s[48:51], s75 offen sc1\n\t" \
  "buffer_load_dwordx4 v[132:135], %[ay2], s[48:51], 0 offen sc1\n\t" \
  "buffer_load_dword v136, %[ar], s[48:51], 0 offen sc1\n\t" \
  "s_add_u32 m0, s79, 122880\n\t" \
  "s_add_u32 s75, s64, s62\n\t" \
  "s_add_u32 s75, s75, 128\n\t" \
  "buffer_load_dwordx4 v234, s[44:47], s75 offen lds\n\t" \
  "s_add_u32 m0, s79, 126976\n\t" \
  "s_add_u32 s75, s65, s62\n\t" \
  "s_add_u32 s75, s75, 128\n\t" \
  "buffer_load_dwordx4 v234, s[44:47], s75 offen lds\n\t" \
  "s_add_u32 m0, s79, 131072\n\t" \
  "s_add_u32 s75, s66, s62\n\t" \
  "s_add_u32 s75, s75, 128\n\t" \
  "buffer_load_dwordx4 v234, s[44:47], s75 offen lds\n\t" \
  "s_add_u32 m0, s79, 24576\n\t" \
  "s_add_u32 s75, s63, s62\n\t" \
  "s_add_u32 s75, s75, s81\n\t" \
  "buffer_load_dwordx4 v234, s[44:47], s75 offen lds\n\t" \
  "s_add_u32 m0, s79, 28672\n\t" \
  "s_add_u32 s75, s63, s62\n\t" \
  "s_add_u32 s75, s75, s81\n\t" \
  "s_add_u32 s75, s75, 36864\n\t" \
  "buffer_load_dwordx4 v234, s[44:47], s75 offen lds\n\t" \
  "s_add_u32 m0, s79, 32768\n\t" \
  "s_add_u32 s75, s67, s81\n\t" \
  "buffer_load_dwordx4 v234, s[44:47], s75 offen lds\n\t" \
  "s_add_u32 m0, s80, 61440\n\t" \
  "s_add_u32 s75, s72, 14336\n\t" \
  "buffer_load_dwordx4 v232, s[48:51], s75 offen sc1 lds\n\t" \
  "s_add_u32 m0, s80, 62464\n\t" \
  "s_add_u32 s75, s72, 15360\n\t" \
  "buffer_load_dwordx4 v232, s[48:51], s75 offen sc1 lds\n\t" \
  "s_waitcnt vmcnt(15)\n\t" \
  "s_barrier\n\t" \
  "s_mov_b32 s82, 0\n\t" \
  "L_P26_%=:\n\t" \
  "v_cmp_eq_u32 vcc, v237, v247\n\t" \
  "s_cmp_eq_u32 vcc_lo, -1\n\t" \
  "s_cbranch_scc1 L_Q26_%=\n\t" \
  "s_add_u32 s82, s82, 1\n\t" \
  "s_cmp_gt_u32 s82, 400000\n\t" \
  "s_cbranch_scc1 L_Q26_%=\n\t" \
  "buffer_load_dword v247, v235, s[48:51], %[sfl] offen offset:896 sc1\n\t" \
  "s_waitcnt vmcnt(0)\n\t" \
  "s_branch L_P26_%=\n\t" \
  "L_Q26_%=:\n\t" \
  "s_add_u32 m0, s80, 63488\n\t" \
  "s_add_u32 s75, s73, 14336\n\t" \
  "buffer_load_dwordx4 v232, s[48:51], s75 offen sc1 lds\n\t" \
  "s_add_u32 m0, s79, 147456\n\t" \
  "s_add_u32 s75, s63, s62\n\t" \
  "buffer_load_dwordx4 v234, s[44:47], s75 offen lds\n\t" \
  "s_add_u32 m0, s79, 151552\n\t" \
  "s_add_u32 s75, s63, s62\n\t" \
  "s_add_u32 s75, s75, 36864\n\t" \
  "buffer_load_dwordx4 v234, s[44:47], s75 offen lds\n\t" \
  "s_add_u32 m0, s79, 155648\n\t" \
  "s_add_u32 s75, s67, 0\n\t" \
  "buffer_load_dwordx4 v234, s[44:47], s75 offen lds\n\t" \
  "s_add_u32 m0, s80, 0\n\t" \
  "s_add_u32 s75, s73, 15360\n\t" \
  "buffer_load_dwordx4 v232, s[48:51], s75 offen sc1 lds\n\t" \
  "s_add_u32 m0, s80, 1024\n\t" \
  "s_add_u32 s75, s74, 14336\n\t" \
  "buffer_load_dwordx4 v232, s[48:51], s75 offen sc1 lds\n\t" \
  "s_add_u32 m0, s80, 2048\n\t" \
  "s_add_u32 s75, s74, 15360\n\t" \
  "buffer_load_dwordx4 v232, s[48:51], s75 offen sc1 lds\n\t" \
  "s_add_u32 m0, s80, 73728\n\t" \
  "s_add_u32 s75, s72, 16384\n\t" \
  "buffer_load_dwordx4 v232, s[48:51], s75 offen sc1 lds\n\t" \
  "s_waitcnt vmcnt(13)\n\t" \
  "s_barrier\n\t" \
  "s_add_u32 m0, s80, 74752\n\t" \
  "s_add_u32 s75, s72, 17408\n\t" \
  "buffer_load_dwordx4 v232, s[48:51], s75 offen sc1 lds\n\t" \
  "s_add_u32 m0, s80, 75776\n\t" \
  "s_add_u32 s75, s73, 16384\n\t" \
  "buffer_load_dwordx4 v232, s[48:51], s75 offen sc1 lds\n\t" \
  "s_add_u32 m0, s79, 86016\n\t" \
  "s_add_u32 s75, s64, s62\n\t" \
  "s_add_u32 s75, s75, 36864\n\t" \
  "buffer_load_dwordx4 v234, s[44:47], s75 offen lds\n\t" \
  "s_add_u32 m0, s79, 90112\n\t" \
  "s_add_u32 s75, s65, s62\n\t" \
  "s_add_u32 s75, s75, 36864\n\t" \
  "buffer_load_dwordx4 v234, s[44:47], s75 offen lds\n\t" \
  "s_add_u32 m0, s79, 94208\n\t" \
  "s_add_u32 s75, s66, s62\n\t" \
  "s_add_u32 s75, s75, 36864\n\t" \
  "buffer_load_dwordx4 v234, s[44:47], s75 offen lds\n\t" \
  "s_add_u32 m0, s80, 98304\n\t" \
  "s_add_u32 s75, s73, 17408\n\t" \
  "buffer_load_dwordx4 v232, s[48:51], s75 offen sc1 lds\n\t" \
  "s_add_u32 m0, s80, 99328\n\t" \
  "s_add_u32 s75, s74, 16384\n\t" \
  "buffer_load_dwordx4 v232, s[48:51], s75 offen sc1 lds\n\t" \
  "s_add_u32 m0, s80, 100352\n\t" \
  "s_add_u32 s75, s74, 17408\n\t" \
  "buffer_load_dwordx4 v232, s[48:51], s75 offen sc1 lds\n\t" \
  "s_waitcnt vmcnt(18)\n\t" \
  "s_barrier\n\t" \
  "s_add_u32 m0, s79, 110592\n\t" \
  "s_add_u32 s75, s68, 0\n\t" \
  "buffer_load_dwordx4 v234, s[44:47], s75 offen lds\n\t" \
  "s_add_u32 m0, s79, 114688\n\t" \
  "s_add_u32 s75, s69, 0\n\t" \
  "buffer_load_dwordx4 v234, s[44:47], s75 offen lds\n\t" \
  "s_add_u32 m0, s79, 118784\n\t" \
  "s_add_u32 s75, s70, 0\n\t" \
  "buffer_load_dwordx4 v234, s[44:47], s75 offen lds\n\t" \
  "s_add_u32 m0, s79, 12288\n\t" \
  "s_add_u32 s75, s63, s62\n\t" \
  "s_add_u32 s75, s75, 128\n\t" \
  "buffer_load_dwordx4 v234, s[44:47], s75 offen lds\n\t" \
  "s_add_u32 m0, s79, 16384\n\t" \
  "s_add_u32 s75, s63, s62\n\t" \
  "s_add_u32 s75, s75, 36992\n\t" \
  "buffer_load_dwordx4 v234, s[44:47], s75 offen lds\n\t" \
  "s_add_u32 m0, s79, 20480\n\t" \
  "s_add_u32 s75, s67, 128\n\t" \
  "buffer_load_dwordx4 v234, s[44:47], s75 offen lds\n\t" \
  "s_add_u32 m0, s79, 49152\n\t" \
  "s_add_u32 s75, s64, s62\n\t" \
  "s_add_u32 s75, s75, 36992\n\t" \
  "buffer_load_dwordx4 v234, s[44:47], s75 offen lds\n\t" \
  "s_add_u32 m0, s79, 53248\n\t" \
  "s_add_u32 s75, s65, s62\n\t" \
  "s_add_u32 s75, s75, 36992\n\t" \
  "buffer_load_dwordx4 v234, s[44:47], s75 offen lds\n\t" \
  "s_waitcnt vmcnt(11)\n\t" \
  "s_barrier\n\t" \
  "s_add_u32 m0, s79, 57344\n\t" \
  "s_add_u32 s75, s66, s62\n\t" \
  "s_add_u32 s75, s75, 36992\n\t" \
  "buffer_load_dwordx4 v234, s[44:47], s75 offen lds\n\t" \
  "s_add_u32 m0, s79, 135168\n\t" \
  "s_add_u32 s75, s68, 128\n\t" \
  "buffer_load_dwordx4 v234, s[44:47], s75 offen lds\n\t" \
  "s_add_u32 m0, s79, 139264\n\t" \
  "s_add_u32 s75, s69, 128\n\t" \
  "buffer_load_dwordx4 v234, s[44:47], s75 offen lds\n\t" \
  "s_add_u32 m0, s79, 143360\n\t" \
  "s_add_u32 s75, s70, 128\n\t" \
  "buffer_load_dwordx4 v234, s[44:47], s75 offen lds\n\t" \
  "v_mov_b32 v191, v136\n\t" \
  "v_mov_b32 v192, %[mean]\n\t" \
  "v_mov_b32 v193, %[mean]\n\t" \
  "s_nop 1\n\t" \
  "v_permlane16_swap_b32 v136, v191\n\t" \
  "v_permlane16_swap_b32 v192, v193\n\t" \
  "s_bitcmp1_b32 %[wi], 0\n\t" \
  "s_cselect_b64 vcc, -1, 0\n\t" \
  "v_cndmask_b32 v194, v136, v191, vcc\n\t" \
  "v_cndmask_b32 v198, v192, v193, vcc\n\t" \
  "v_cndmask_b32 v188, %[rho0], %[rho1], vcc\n\t" \
  "v_sub_f32 v196, 1.0, v194\n\t" \
  "v_mul_f32 v198, v196, v198\n\t" \
  "v_mul_f32 v188, v196, v188\n\t" \
  "s_bitcmp1_b32 %[sl], 0\n\t" \
  "s_cselect_b64 vcc, -1, 0\n\t" \
  "v_cndmask_b32 v195, v136, v191, vcc\n\t" \
  "v_cndmask_b32 v199, v192, v193, vcc\n\t" \
  "v_cndmask_b32 v189, %[rho0], %[rho1], vcc\n\t" \
  "v_sub_f32 v197, 1.0, v195\n\t" \
  "v_mul_f32 v199, v197, v199\n\t" \
  "v_mul_f32 v189, v197, v189\n\t" \
  "s_waitcnt vmcnt(9)\n\t" \
  "s_barrier\n\t" \
  "s_setprio 2\n\t" \
  "ds_write_b128 v220, v[124:127] offset:0\n\t" \
  "ds_write_b128 v221, v[112:115] offset:0\n\t" \
  "ds_write_b128 v220, v[128:131] offset:1024\n\t" \
  "ds_write_b128 v221, v[116:119] offset:1024\n\t" \
  "ds_write_b128 v220, v[132:135] offset:2048\n\t" \
  "ds_write_b128 v221, v[120:123] offset:2048\n\t" \
  "s_setprio 0\n\t" \
  "v_lshlrev_b32 v164, 16, v100\n\t" \
  "v_and_b32 v165, 0xffff0000, v100\n\t" \
  "v_lshlrev_b32 v166, 16, v101\n\t" \
  "v_and_b32 v167, 0xffff0000, v101\n\t" \
  "v_lshlrev_b32 v168, 16, v102\n\t" \
  "v_and_b32 v169, 0xffff0000, v102\n\t" \
  "v_lshlrev_b32 v170, 16, v103\n\t" \
  "v_and_b32 v171, 0xffff0000, v103\n\t" \
  "v_lshlrev_b32 v172, 16, v104\n\t" \
  "v_and_b32 v173, 0xffff0000, v104\n\t" \
  "v_lshlrev_b32 v174, 16, v105\n\t" \
  "v_and_b32 v175, 0xffff0000, v105\n\t" \
  "v_lshlrev_b32 v176, 16, v106\n\t" \
  "v_and_b32 v177, 0xffff0000, v106\n\t" \
  "v_lshlrev_b32 v178, 16, v107\n\t" \
  "v_and_b32 v179, 0xffff0000, v107\n\t" \
  "s_waitcnt vmcnt(3)\n\t" \
  "s_barrier\n\t" \
  "s_setprio 2\n\t" \
  "ds_read_b64_tr_b16 v[140:141], v222 offset:0\n\t" \
  "ds_read_b64_tr_b16 v[142:143], v222 offset:512\n\t" \
  "ds_read_b64 v[152:153], v223 offset:0\n\t" \
  "ds_read_b64 v[154:155], v223 offset:32\n\t" \
  "ds_read_b64_tr_b16 v[144:145], v222 offset:1024\n\t" \
  "ds_read_b64_tr_b16 v[146:147], v222 offset:1536\n\t" \
  "ds_read_b64 v[156:157], v223 offset:1024\n\t" \
  "ds_read_b64 v[158:159], v223 offset:1056\n\t" \
  "ds_read_b64_tr_b16 v[148:149], v222 offset:2048\n\t" \
  "ds_read_b64_tr_b16 v[150:151], v222 offset:2560\n\t" \
  "ds_read_b64 v[160:161], v223 offset:2048\n\t" \
  "ds_read_b64 v[162:163], v223 offset:2080\n\t" \
  "s_setprio 0\n\t" \
  "v_lshlrev_b32 v180, 16, v108\n\t" \
  "v_and_b32 v181, 0xffff0000, v108\n\t" \
  "v_lshlrev_b32 v182, 16, v109\n\t" \
  "v_and_b32 v183, 0xffff0000, v109\n\t" \
  "v_lshlrev_b32 v184, 16, v110\n\t" \
  "v_and_b32 v185, 0xffff0000, v110\n\t" \
  "v_lshlrev_b32 v186, 16, v111\n\t" \
  "v_and_b32 v187, 0xffff0000, v111\n\t" \
  "v_fma_f32 v164, v164, v196, v198\n\t" \
  "v_fma_f32 v165, v165, v196, v198\n\t" \
  "v_fma_f32 v166, v166, v196, v198\n\t" \
  "v_fma_f32 v167, v167, v196, v198\n\t" \
  "v_fma_f32 v168, v168, v196, v198\n\t" \
  "v_fma_f32 v169, v169, v196, v198\n\t" \
  "v_fma_f32 v170, v170, v196, v198\n\t" \
  "v_fma_f32 v171, v171, v196, v198\n\t" \
  "v_fma_f32 v172, v172, v196, v198\n\t" \
  "v_fma_f32 v173, v173, v196, v198\n\t" \
  "v_fma_f32 v174, v174, v196, v198\n\t" \
  "v_fma_f32 v175, v175, v196, v198\n\t" \
  "v_fma_f32 v176, v176, v196, v198\n\t" \
  "v_fma_f32 v177, v177, v196, v198\n\t" \
  "v_fma_f32 v178, v178, v196, v198\n\t" \
  "v_fma_f32 v179, v179, v196, v198\n\t" \
  "v_fma_f32 v180, v180, v197, v199\n\t" \
  "v_fma_f32 v181, v181, v197, v199\n\t" \
  "v_fma_f32 v182, v182, v197, v199\n\t" \
  "v_fma_f32 v183, v183, v197, v199\n\t" \
  "v_fma_f32 v184, v184, v197, v199\n\t" \
  "v_fma_f32 v185, v185, v197, v199\n\t" \
  "v_fma_f32 v186, v186, v197, v199\n\t" \
  "v_fma_f32 v187, v187, v197, v199\n\t" \
  "s_waitcnt vmcnt(0)\n\t" \
  "s_barrier\n\t" \
  "s_mov_b64 s[40:41], %[out]\n\t" \
  "s_mov_b32 s42, 2359296\n\t" \
  "s_mov_b32 s82, 0\n\t" \
  "s_xor_b32 s54, %[spi], 8192\n\t" \
  "s_add_u32 s57, s54, 4096\n\t" \
  "s_xor_b32 s60, %[sci], 2048\n\t" \
  "s_xor_b32 s55, %[spi], 16384\n\t" \
  "s_add_u32 s58, s55, 4096\n\t" \
  "s_xor_b32 s61, %[sci], 4096\n\t" \
  "s_xor_b32 s56, %[spi], 24576\n\t" \
  "s_add_u32 s59, s56, 4096\n\t" \
  "s_xor_b32 s62, %[sci], 6144\n\t" \
  "s_waitcnt lgkmcnt(0)\n\t" \
  "v_lshlrev_b32 v191, 16, v152\n\t" \
  "v_and_b32 v152, 0xffff0000, v152\n\t" \
  "v_lshlrev_b32 v192, 16, v140\n\t" \
  "v_and_b32 v140, 0xffff0000, v140\n\t" \
  "v_mul_f32 v191, v194, v191\n\t" \
  "v_mul_f32 v152, v194, v152\n\t" \
  "v_fmac_f32 v164, v191, v192\n\t" \
  "v_fmac_f32 v165, v152, v140\n\t" \
  "v_lshlrev_b32 v191, 16, v153\n\t" \
  "v_and_b32 v153, 0xffff0000, v153\n\t" \
  "v_lshlrev_b32 v192, 16, v141\n\t" \
  "v_and_b32 v141, 0xffff0000, v141\n\t" \
  "v_mul_f32 v191, v194, v191\n\t" \
  "v_mul_f32 v153, v194, v153\n\t" \
  "v_fmac_f32 v166, v191, v192\n\t" \
  "v_fmac_f32 v167, v153, v141\n\t" \
  "v_lshlrev_b32 v191, 16, v154\n\t" \
  "v_and_b32 v154, 0xffff0000, v154\n\t" \
  "v_lshlrev_b32 v192, 16, v142\n\t" \
  "v_and_b32 v142, 0xffff0000, v142\n\t" \
  "v_mul_f32 v191, v194, v191\n\t" \
  "v_mul_f32 v154, v194, v154\n\t" \
  "v_fmac_f32 v168, v191, v192\n\t" \
  "v_fmac_f32 v169, v154, v142\n\t" \
  "v_lshlrev_b32 v191, 16, v155\n\t" \
  "v_and_b32 v155, 0xffff0000, v155\n\t" \
  "v_lshlrev_b32 v192, 16, v143\n\t" \
  "v_and_b32 v143, 0xffff0000, v143\n\t" \
  "v_mul_f32 v191, v194, v191\n\t" \
  "v_mul_f32 v155, v194, v155\n\t" \
  "v_fmac_f32 v170, v191, v192\n\t" \
  "v_fmac_f32 v171, v155, v143\n\t" \
  "v_lshlrev_b32 v191, 16, v156\n\t" \
  "v_and_b32 v156, 0xffff0000, v156\n\t" \
  "v_lshlrev_b32 v192, 16, v144\n\t" \
  "v_and_b32 v144, 0xffff0000, v144\n\t" \
  "v_mul_f32 v191, v194, v191\n\t" \
  "v_mul_f32 v156, v194, v156\n\t" \
  "v_fmac_f32 v172, v191, v192\n\t" \
  "v_fmac_f32 v173, v156, v144\n\t" \
  "v_lshlrev_b32 v191, 16, v157\n\t" \
  "v_and_b32 v157, 0xffff0000, v157\n\t" \
  "v_lshlrev_b32 v192, 16, v145\n\t" \
  "v_and_b32 v145, 0xffff0000, v145\n\t" \
  "v_mul_f32 v191, v194, v191\n\t" \
  "v_mul_f32 v157, v194, v157\n\t" \
  "v_fmac_f32 v174, v191, v192\n\t" \
  "v_fmac_f32 v175, v157, v145\n\t" \
  "v_lshlrev_b32 v191, 16, v158\n\t" \
  "v_and_b32 v158, 0xffff0000, v158\n\t" \
  "v_lshlrev_b32 v192, 16, v146\n\t" \
  "v_and_b32 v146, 0xffff0000, v146\n\t" \
  "v_mul_f32 v191, v194, v191\n\t" \
  "v_mul_f32 v158, v194, v158\n\t" \
  "v_fmac_f32 v176, v191, v192\n\t" \
  "v_fmac_f32 v177, v158, v146\n\t" \
  "v_lshlrev_b32 v191, 16, v159\n\t" \
  "v_and_b32 v159, 0xffff0000, v159\n\t" \
  "v_lshlrev_b32 v192, 16, v147\n\t" \
  "v_and_b32 v147, 0xffff0000, v147\n\t" \
  "v_mul_f32 v191, v194, v191\n\t" \
  "v_mul_f32 v159, v194, v159\n\t" \
  "v_fmac_f32 v178, v191, v192\n\t" \
  "v_fmac_f32 v179, v159, v147\n\t" \
  "v_lshlrev_b32 v191, 16, v160\n\t" \
  "v_and_b32 v160, 0xffff0000, v160\n\t" \
  "v_lshlrev_b32 v192, 16, v148\n\t" \
  "v_and_b32 v148, 0xffff0000, v148\n\t" \
  "v_mul_f32 v191, v195, v191\n\t" \
  "v_mul_f32 v160, v195, v160\n\t" \
  "v_fmac_f32 v180, v191, v192\n\t" \
  "v_fmac_f32 v181, v160, v148\n\t" \
  "v_lshlrev_b32 v191, 16, v161\n\t" \
  "v_and_b32 v161, 0xffff0000, v161\n\t" \
  "v_lshlrev_b32 v192, 16, v149\n\t" \
  "v_and_b32 v149, 0xffff0000, v149\n\t" \
  "v_mul_f32 v191, v195, v191\n\t" \
  "v_mul_f32 v161, v195, v161\n\t" \
  "v_fmac_f32 v182, v191, v192\n\t" \
  "v_fmac_f32 v183, v161, v149\n\t" \
  "v_lshlrev_b32 v191, 16, v162\n\t" \
  "v_and_b32 v162, 0xffff0000, v162\n\t" \
  "v_lshlrev_b32 v192, 16, v150\n\t" \
  "v_and_b32 v150, 0xffff0000, v150\n\t" \
  "v_mul_f32 v191, v195, v191\n\t" \
  "v_mul_f32 v162, v195, v162\n\t" \
  "v_fmac_f32 v184, v191, v192\n\t" \
  "v_fmac_f32 v185, v162, v150\n\t" \
  "v_lshlrev_b32 v191, 16, v163\n\t" \
  "v_and_b32 v163, 0xffff0000, v163\n\t" \
  "v_lshlrev_b32 v192, 16, v151\n\t" \
  "v_and_b32 v151, 0xffff0000, v151\n\t" \
  "v_mul_f32 v191, v195, v191\n\t" \
  "v_mul_f32 v163, v195, v163\n\t" \
  "v_fmac_f32 v186, v191, v192\n\t" \
  "v_fmac_f32 v187, v163, v151\n\t" \
  "s_barrier\n\t" \
  "ds_read_b128 v[140:143], v137\n\t" \
  "ds_read_b128 v[144:147], v137 offset:4096\n\t" \
  "buffer_load_dwordx4 v[200:203], %[ap4], s[48:51], 0 offen offset:0 sc1\n\t" \
  "buffer_load_dword v216, %[ac4], s[48:51], 0 offen offset:0 sc1\n\t" \
  "buffer_load_dwordx4 v[204:207], %[ap4], s[48:51], 0 offen offset:1024 sc1\n\t" \
  "buffer_load_dword v217, %[ac4], s[48:51], 0 offen offset:1024 sc1\n\t" \
  "buffer_load_dwordx4 v[208:211], %[ap4], s[48:51], 0 offen offset:2048 sc1\n\t" \
  "buffer_load_dword v218, %[ac4], s[48:51], 0 offen offset:2048 sc1\n\t" \
  "buffer_load_dwordx4 v[212:215], %[ap4], s[48:51], 0 offen offset:3072 sc1\n\t" \
  "buffer_load_dword v219, %[ac4], s[48:51], 0 offen offset:3072 sc1\n\t" \
  "buffer_load_dwordx4 v[100:103], v232, s[48:51], s54 offen sc1\n\t" \
  "buffer_load_dword v124, v232, s[48:51], s60 offen offset:0 sc1\n\t" \
  "buffer_load_dwordx4 v[104:107], v232, s[48:51], s55 offen sc1\n\t" \
  "buffer_load_dword v125, v232, s[48:51], s61 offen offset:0 sc1\n\t" \
  "buffer_load_dwordx4 v[108:111], v232, s[48:51], s56 offen sc1\n\t" \
  "buffer_load_dword v126, v232, s[48:51], s62 offen offset:0 sc1\n\t" \
  "s_waitcnt lgkmcnt(0)\n\t" \
  "v_lshlrev_b32 v191, 16, v140\n\t" \
  "v_and_b32 v192, 0xffff0000, v140\n\t" \
  "v_fmac_f32 v164, v188, v191\n\t" \
  "v_fmac_f32 v165, v188, v192\n\t" \
  "v_lshlrev_b32 v191, 16, v141\n\t" \
  "v_and_b32 v192, 0xffff0000, v141\n\t" \
  "v_fmac_f32 v166, v188, v191\n\t" \
  "v_fmac_f32 v167, v188, v192\n\t" \
  "v_lshlrev_b32 v191, 16, v142\n\t" \
  "v_and_b32 v192, 0xffff0000, v142\n\t" \
  "v_fmac_f32 v168, v188, v191\n\t" \
  "v_fmac_f32 v169, v188, v192\n\t" \
  "v_lshlrev_b32 v191, 16, v143\n\t" \
  "v_and_b32 v192, 0xffff0000, v143\n\t" \
  "v_fmac_f32 v170, v188, v191\n\t" \
  "v_fmac_f32 v171, v188, v192\n\t" \
  "v_lshlrev_b32 v191, 16, v144\n\t" \
  "v_and_b32 v192, 0xffff0000, v144\n\t" \
  "v_fmac_f32 v172, v188, v191\n\t" \
  "v_fmac_f32 v173, v188, v192\n\t" \
  "v_lshlrev_b32 v191, 16, v145\n\t" \
  "v_and_b32 v192, 0xffff0000, v145\n\t" \
  "v_fmac_f32 v174, v188, v191\n\t" \
  "v_fmac_f32 v175, v188, v192\n\t" \
  "v_lshlrev_b32 v191, 16, v146\n\t" \
  "v_and_b32 v192, 0xffff0000, v146\n\t" \
  "v_fmac_f32 v176, v188, v191\n\t" \
  "v_fmac_f32 v177, v188, v192\n\t" \
  "v_lshlrev_b32 v191, 16, v147\n\t" \
  "v_and_b32 v192, 0xffff0000, v147\n\t" \
  "v_fmac_f32 v178, v188, v191\n\t" \
  "v_fmac_f32 v179, v188, v192\n\t" \
  "s_waitcnt vmcnt(6)\n\t" \
  "L_V4_%=:\n\t" \
  "v_add3_u32 v191, v200, v201, v237\n\t" \
  "v_add3_u32 v191, v191, v202, v203\n\t" \
  "v_xor_b32 v192, v191, v216\n\t" \
  "v_add3_u32 v191, v204, v205, v237\n\t" \
  "v_add3_u32 v191, v191, v206, v207\n\t" \
  "v_bitop3_b32 v192, v191, v217, v192 bitop3:0xbe\n\t" \
  "v_add3_u32 v191, v208, v209, v237\n\t" \
  "v_add3_u32 v191, v191, v210, v211\n\t" \
  "v_bitop3_b32 v192, v191, v218, v192 bitop3:0xbe\n\t" \
  "v_add3_u32 v191, v212, v213, v237\n\t" \
  "v_add3_u32 v191, v191, v214, v215\n\t" \
  "v_bitop3_b32 v192, v191, v219, v192 bitop3:0xbe\n\t" \
  "v_cmp_ne_u32 vcc, 0, v192\n\t" \
  "s_cbranch_vccnz L_W4_%=\n\t" \
  "L_X4_%=:\n\t" \
  "v_lshlrev_b32 v148, 16, v200\n\t" \
  "v_and_b32 v149, 0xffff0000, v200\n\t" \
  "v_lshlrev_b32 v191, 16, v204\n\t" \
  "v_and_b32 v192, 0xffff0000, v204\n\t" \
  "v_add_f32 v148, v148, v191\n\t" \
  "v_add_f32 v149, v149, v192\n\t" \
  "v_lshlrev_b32 v191, 16, v208\n\t" \
  "v_and_b32 v192, 0xffff0000, v208\n\t" \
  "v_add_f32 v148, v148, v191\n\t" \
  "v_add_f32 v149, v149, v192\n\t" \
  "v_lshlrev_b32 v191, 16, v212\n\t" \
  "v_and_b32 v192, 0xffff0000, v212\n\t" \
  "v_add_f32 v148, v148, v191\n\t" \
  "v_add_f32 v149, v149, v192\n\t" \
  "v_lshlrev_b32 v150, 16, v201\n\t" \
  "v_and_b32 v151, 0xffff0000, v201\n\t" \
  "v_lshlrev_b32 v191, 16, v205\n\t" \
  "v_and_b32 v192, 0xffff0000, v205\n\t" \
  "v_add_f32 v150, v150, v191\n\t" \
  "v_add_f32 v151, v151, v192\n\t" \
  "v_lshlrev_b32 v191, 16, v209\n\t" \
  "v_and_b32 v192, 0xffff0000, v209\n\t" \
  "v_add_f32 v150, v150, v191\n\t" \
  "v_add_f32 v151, v151, v192\n\t" \
  "v_lshlrev_b32 v191, 16, v213\n\t" \
  "v_and_b32 v192, 0xffff0000, v213\n\t" \
  "v_add_f32 v150, v150, v191\n\t" \
  "v_add_f32 v151, v151, v192\n\t" \
  "v_lshlrev_b32 v152, 16, v202\n\t" \
  "v_and_b32 v153, 0xffff0000, v202\n\t" \
  "v_lshlrev_b32 v191, 16, v206\n\t" \
  "v_and_b32 v192, 0xffff0000, v206\n\t" \
  "v_add_f32 v152, v152, v191\n\t" \
  "v_add_f32 v153, v153, v192\n\t" \
  "v_lshlrev_b32 v191, 16, v210\n\t" \
  "v_and_b32 v192, 0xffff0000, v210\n\t" \
  "v_add_f32 v152, v152, v191\n\t" \
  "v_add_f32 v153, v153, v192\n\t" \
  "v_lshlrev_b32 v191, 16, v214\n\t" \
  "v_and_b32 v192, 0xffff0000, v214\n\t" \
  "v_add_f32 v152, v152, v191\n\t" \
  "v_add_f32 v153, v153, v192\n\t" \
  "v_lshlrev_b32 v154, 16, v203\n\t" \
  "v_and_b32 v155, 0xffff0000, v203\n\t" \
  "v_lshlrev_b32 v191, 16, v207\n\t" \
  "v_and_b32 v192, 0xffff0000, v207\n\t" \
  "v_add_f32 v154, v154, v191\n\t" \
  "v_add_f32 v155, v155, v192\n\t" \
  "v_lshlrev_b32 v191, 16, v211\n\t" \
  "v_and_b32 v192, 0xffff0000, v211\n\t" \
  "v_add_f32 v154, v154, v191\n\t" \
  "v_add_f32 v155, v155, v192\n\t" \
  "v_lshlrev_b32 v191, 16, v215\n\t" \
  "v_and_b32 v192, 0xffff0000, v215\n\t" \
  "v_add_f32 v154, v154, v191\n\t" \
  "v_add_f32 v155, v155, v192\n\t" \
  "buffer_load_dwordx4 v[112:115], v232, s[48:51], s57 offen sc1\n\t" \
  "buffer_load_dword v127, v232, s[48:51], s60 offen offset:1024 sc1\n\t" \
  "buffer_load_dwordx4 v[116:119], v232, s[48:51], s58 offen sc1\n\t" \
  "buffer_load_dword v128, v232, s[48:51], s61 offen offset:1024 sc1\n\t" \
  "buffer_load_dwordx4 v[120:123], v232, s[48:51], s59 offen sc1\n\t" \
  "buffer_load_dword v129, v232, s[48:51], s62 offen offset:1024 sc1\n\t" \
  "v_mov_b32 v156, v148\n\t" \
  "v_mov_b32 v157, v149\n\t" \
  "v_mov_b32 v158, v150\n\t" \
  "v_mov_b32 v159, v151\n\t" \
  "v_mov_b32 v160, v152\n\t" \
  "v_mov_b32 v161, v153\n\t" \
  "v_mov_b32 v162, v154\n\t" \
  "v_mov_b32 v163, v155\n\t" \
  "s_nop 1\n\t" \
  "v_permlane16_swap_b32 v148, v156\n\t" \
  "v_permlane16_swap_b32 v149, v157\n\t" \
  "v_permlane16_swap_b32 v150, v158\n\t" \
  "v_permlane16_swap_b32 v151, v159\n\t" \
  "v_permlane16_swap_b32 v152, v160\n\t" \
  "v_permlane16_swap_b32 v153, v161\n\t" \
  "v_permlane16_swap_b32 v154, v162\n\t" \
  "v_permlane16_swap_b32 v155, v163\n\t" \
  "v_add_f32 v148, v148, v156\n\t" \
  "v_add_f32 v149, v149, v157\n\t" \
  "v_add_f32 v150, v150, v158\n\t" \
  "v_add_f32 v151, v151, v159\n\t" \
  "v_add_f32 v152, v152, v160\n\t" \
  "v_add_f32 v153, v153, v161\n\t" \
  "v_add_f32 v154, v154, v162\n\t" \
  "v_add_f32 v155, v155, v163\n\t" \
  "v_mov_b32 v156, v148\n\t" \
  "v_mov_b32 v157, v149\n\t" \
  "v_mov_b32 v158, v150\n\t" \
  "v_mov_b32 v159, v151\n\t" \
  "v_mov_b32 v160, v152\n\t" \
  "v_mov_b32 v161, v153\n\t" \
  "v_mov_b32 v162, v154\n\t" \
  "v_mov_b32 v163, v155\n\t" \
  "s_nop 1\n\t" \
  "v_permlane32_swap_b32 v148, v156\n\t" \
  "v_permlane32_swap_b32 v149, v157\n\t" \
  "v_permlane32_swap_b32 v150, v158\n\t" \
  "v_permlane32_swap_b32 v151, v159\n\t" \
  "v_permlane32_swap_b32 v152, v160\n\t" \
  "v_permlane32_swap_b32 v153, v161\n\t" \
  "v_permlane32_swap_b32 v154, v162\n\t" \
  "v_permlane32_swap_b32 v155, v163\n\t" \
  "v_add_f32 v148, v148, v156\n\t" \
  "v_add_f32 v149, v149, v157\n\t" \
  "v_add_f32 v150, v150, v158\n\t" \
  "v_add_f32 v151, v151, v159\n\t" \
  "v_add_f32 v152, v152, v160\n\t" \
  "v_add_f32 v153, v153, v161\n\t" \
  "v_add_f32 v154, v154, v162\n\t" \
  "v_add_f32 v155, v155, v163\n\t" \
  "v_fmac_f32 v180, v189, v148\n\t" \
  "v_fmac_f32 v181, v189, v149\n\t" \
  "v_fmac_f32 v182, v189, v150\n\t" \
  "v_fmac_f32 v183, v189, v151\n\t" \
  "v_fmac_f32 v184, v189, v152\n\t" \
  "v_fmac_f32 v185, v189, v153\n\t" \
  "v_fmac_f32 v186, v189, v154\n\t" \
  "v_fmac_f32 v187, v189, v155\n\t" \
  "v_cvt_pk_bf16_f32 v180, v180, v181\n\t" \
  "v_cvt_pk_bf16_f32 v181, v182, v183\n\t" \
  "v_cvt_pk_bf16_f32 v182, v184, v185\n\t" \
  "v_cvt_pk_bf16_f32 v183, v186, v187\n\t" \
  "s_waitcnt vmcnt(6)\n\t" \
  "L_V0_%=:\n\t" \
  "v_add3_u32 v191, v100, v101, v237\n\t" \
  "v_add3_u32 v191, v191, v102, v103\n\t" \
  "v_xor_b32 v192, v191, v124\n\t" \
  "v_add3_u32 v191, v104, v105, v237\n\t" \
  "v_add3_u32 v191, v191, v106, v107\n\t" \
  "v_bitop3_b32 v192, v191, v125, v192 bitop3:0xbe\n\t" \
  "v_add3_u32 v191, v108, v109, v237\n\t" \
  "v_add3_u32 v191, v191, v110, v111\n\t" \
  "v_bitop3_b32 v192, v191, v126, v192 bitop3:0xbe\n\t" \
  "v_cmp_ne_u32 vcc, 0, v192\n\t" \
  "s_cbranch_vccnz L_W0_%=\n\t" \
  "L_X0_%=:\n\t" \
  "v_lshlrev_b32 v191, 16, v100\n\t" \
  "v_and_b32 v192, 0xffff0000, v100\n\t" \
  "v_fmac_f32 v164, v188, v191\n\t" \
  "v_fmac_f32 v165, v188, v192\n\t" \
  "v_lshlrev_b32 v191, 16, v101\n\t" \
  "v_and_b32 v192, 0xffff0000, v101\n\t" \
  "v_fmac_f32 v166, v188, v191\n\t" \
  "v_fmac_f32 v167, v188, v192\n\t" \
  "v_lshlrev_b32 v191, 16, v102\n\t" \
  "v_and_b32 v192, 0xffff0000, v102\n\t" \
  "v_fmac_f32 v168, v188, v191\n\t" \
  "v_fmac_f32 v169, v188, v192\n\t" \
  "v_lshlrev_b32 v191, 16, v103\n\t" \
  "v_and_b32 v192, 0xffff0000, v103\n\t" \
  "v_fmac_f32 v170, v188, v191\n\t" \
  "v_fmac_f32 v171, v188, v192\n\t" \
  "v_lshlrev_b32 v191, 16, v104\n\t" \
  "v_and_b32 v192, 0xffff0000, v104\n\t" \
  "v_fmac_f32 v164, v188, v191\n\t" \
  "v_fmac_f32 v165, v188, v192\n\t" \
  "v_lshlrev_b32 v191, 16, v105\n\t" \
  "v_and_b32 v192, 0xffff0000, v105\n\t" \
  "v_fmac_f32 v166, v188, v191\n\t" \
  "v_fmac_f32 v167, v188, v192\n\t" \
  "v_lshlrev_b32 v191, 16, v106\n\t" \
  "v_and_b32 v192, 0xffff0000, v106\n\t" \
  "v_fmac_f32 v168, v188, v191\n\t" \
  "v_fmac_f32 v169, v188, v192\n\t" \
  "v_lshlrev_b32 v191, 16, v107\n\t" \
  "v_and_b32 v192, 0xffff0000, v107\n\t" \
  "v_fmac_f32 v170, v188, v191\n\t" \
  "v_fmac_f32 v171, v188, v192\n\t" \
  "v_lshlrev_b32 v191, 16, v108\n\t" \
  "v_and_b32 v192, 0xffff0000, v108\n\t" \
  "v_fmac_f32 v164, v188, v191\n\t" \
  "v_fmac_f32 v165, v188, v192\n\t" \
  "v_lshlrev_b32 v191, 16, v109\n\t" \
  "v_and_b32 v192, 0xffff0000, v109\n\t" \
  "v_fmac_f32 v166, v188, v191\n\t" \
  "v_fmac_f32 v167, v188, v192\n\t" \
  "v_lshlrev_b32 v191, 16, v110\n\t" \
  "v_and_b32 v192, 0xffff0000, v110\n\t" \
  "v_fmac_f32 v168, v188, v191\n\t" \
  "v_fmac_f32 v169, v188, v192\n\t" \
  "v_lshlrev_b32 v191, 16, v111\n\t" \
  "v_and_b32 v192, 0xffff0000, v111\n\t" \
  "v_fmac_f32 v170, v188, v191\n\t" \
  "v_fmac_f32 v171, v188, v192\n\t" \
  "v_cvt_pk_bf16_f32 v164, v164, v165\n\t" \
  "v_cvt_pk_bf16_f32 v165, v166, v167\n\t" \
  "v_cvt_pk_bf16_f32 v166, v168, v169\n\t" \
  "v_cvt_pk_bf16_f32 v167, v170, v171\n\t" \
  "ds_write_b64 v224, v[164:165] offset:0\n\t" \
  "ds_write_b64 v224, v[166:167] offset:32\n\t" \
  "ds_read_b128 v[140:143], v225 offset:0\n\t" \
  "s_waitcnt vmcnt(0)\n\t" \
  "L_V1_%=:\n\t" \
  "v_add3_u32 v191, v112, v113, v237\n\t" \
  "v_add3_u32 v191, v191, v114, v115\n\t" \
  "v_xor_b32 v192, v191, v127\n\t" \
  "v_add3_u32 v191, v116, v117, v237\n\t" \
  "v_add3_u32 v191, v191, v118, v119\n\t" \
  "v_bitop3_b32 v192, v191, v128, v192 bitop3:0xbe\n\t" \
  "v_add3_u32 v191, v120, v121, v237\n\t" \
  "v_add3_u32 v191, v191, v122, v123\n\t" \
  "v_bitop3_b32 v192, v191, v129, v192 bitop3:0xbe\n\t" \
  "v_cmp_ne_u32 vcc, 0, v192\n\t" \
  "s_cbranch_vccnz L_W1_%=\n\t" \
  "L_X1_%=:\n\t" \
  "v_lshlrev_b32 v191, 16, v112\n\t" \
  "v_and_b32 v192, 0xffff0000, v112\n\t" \
  "v_fmac_f32 v172, v188, v191\n\t" \
  "v_fmac_f32 v173, v188, v192\n\t" \
  "v_lshlrev_b32 v191, 16, v113\n\t" \
  "v_and_b32 v192, 0xffff0000, v113\n\t" \
  "v_fmac_f32 v174, v188, v191\n\t" \
  "v_fmac_f32 v175, v188, v192\n\t" \
  "v_lshlrev_b32 v191, 16, v114\n\t" \
  "v_and_b32 v192, 0xffff0000, v114\n\t" \
  "v_fmac_f32 v176, v188, v191\n\t" \
  "v_fmac_f32 v177, v188, v192\n\t" \
  "v_lshlrev_b32 v191, 16, v115\n\t" \
  "v_and_b32 v192, 0xffff0000, v115\n\t" \
  "v_fmac_f32 v178, v188, v191\n\t" \
  "v_fmac_f32 v179, v188, v192\n\t" \
  "v_lshlrev_b32 v191, 16, v116\n\t" \
  "v_and_b32 v192, 0xffff0000, v116\n\t" \
  "v_fmac_f32 v172, v188, v191\n\t" \
  "v_fmac_f32 v173, v188, v192\n\t" \
  "v_lshlrev_b32 v191, 16, v117\n\t" \
  "v_and_b32 v192, 0xffff0000, v117\n\t" \
  "v_fmac_f32 v174, v188, v191\n\t" \
  "v_fmac_f32 v175, v188, v192\n\t" \
  "v_lshlrev_b32 v191, 16, v118\n\t" \
  "v_and_b32 v192, 0xffff0000, v118\n\t" \
  "v_fmac_f32 v176, v188, v191\n\t" \
  "v_fmac_f32 v177, v188, v192\n\t" \
  "v_lshlrev_b32 v191, 16, v119\n\t" \
  "v_and_b32 v192, 0xffff0000, v119\n\t" \
  "v_fmac_f32 v178, v188, v191\n\t" \
  "v_fmac_f32 v179, v188, v192\n\t" \
  "v_lshlrev_b32 v191, 16, v120\n\t" \
  "v_and_b32 v192, 0xffff0000, v120\n\t" \
  "v_fmac_f32 v172, v188, v191\n\t" \
  "v_fmac_f32 v173, v188, v192\n\t" \
  "v_lshlrev_b32 v191, 16, v121\n\t" \
  "v_and_b32 v192, 0xffff0000, v121\n\t" \
  "v_fmac_f32 v174, v188, v191\n\t" \
  "v_fmac_f32 v175, v188, v192\n\t" \
  "v_lshlrev_b32 v191, 16, v122\n\t" \
  "v_and_b32 v192, 0xffff0000, v122\n\t" \
  "v_fmac_f32 v176, v188, v191\n\t" \
  "v_fmac_f32 v177, v188, v192\n\t" \
  "v_lshlrev_b32 v191, 16, v123\n\t" \
  "v_and_b32 v192, 0xffff0000, v123\n\t" \
  "v_fmac_f32 v178, v188, v191\n\t" \
  "v_fmac_f32 v179, v188, v192\n\t" \
  "v_cvt_pk_bf16_f32 v172, v172, v173\n\t" \
  "v_cvt_pk_bf16_f32 v173, v174, v175\n\t" \
  "v_cvt_pk_bf16_f32 v174, v176, v177\n\t" \
  "v_cvt_pk_bf16_f32 v175, v178, v179\n\t" \
  "ds_write_b64 v224, v[172:173] offset:1536\n\t" \
  "ds_write_b64 v224, v[174:175] offset:1568\n\t" \
  "ds_read_b128 v[144:147], v225 offset:1536\n\t" \
  "s_waitcnt lgkmcnt(3)\n\t" \
  "buffer_store_dwordx4 v[140:143], %[aq], s[40:43], 0 offen \n\t" \
  "s_lshl_b32 s75, %[wi], 4\n\t" \
  "s_lshl_b64 exec, 0xffff, s75\n\t" \
  "buffer_store_dwordx2 v[180:181], %[oo], s[40:43], 0 offen \n\t" \
  "buffer_store_dwordx2 v[182:183], %[oo], s[40:43], 0 offen offset:32 \n\t" \
  "s_mov_b64 exec, -1\n\t" \
  "s_waitcnt lgkmcnt(0)\n\t" \
  "buffer_store_dwordx4 v[144:147], %[aq], s[40:43], 0 offen offset:128 \n\t" \
  "s_branch L_Z_%=\n\t" \
  "L_W4_%=:\n\t" \
  "s_add_u32 s82, s82, 1\n\t" \
  "s_cmp_gt_u32 s82, 400000\n\t" \
  "s_cbranch_scc1 L_X4_%=\n\t" \
  "s_sleep 1\n\t" \
  "buffer_load_dwordx4 v[200:203], %[ap4], s[48:51], 0 offen offset:0 sc1\n\t" \
  "buffer_load_dword v216, %[ac4], s[48:51], 0 offen offset:0 sc1\n\t" \
  "buffer_load_dwordx4 v[204:207], %[ap4], s[48:51], 0 offen offset:1024 sc1\n\t" \
  "buffer_load_dword v217, %[ac4], s[48:51], 0 offen offset:1024 sc1\n\t" \
  "buffer_load_dwordx4 v[208:211], %[ap4], s[48:51], 0 offen offset:2048 sc1\n\t" \
  "buffer_load_dword v218, %[ac4], s[48:51], 0 offen offset:2048 sc1\n\t" \
  "buffer_load_dwordx4 v[212:215], %[ap4], s[48:51], 0 offen offset:3072 sc1\n\t" \
  "buffer_load_dword v219, %[ac4], s[48:51], 0 offen offset:3072 sc1\n\t" \
  "s_waitcnt vmcnt(0)\n\t" \
  "s_branch L_V4_%=\n\t" \
  "L_W0_%=:\n\t" \
  "s_add_u32 s82, s82, 1\n\t" \
  "s_cmp_gt_u32 s82, 400000\n\t" \
  "s_cbranch_scc1 L_X0_%=\n\t" \
  "s_sleep 1\n\t" \
  "buffer_load_dwordx4 v[100:103], v232, s[48:51], s54 offen sc1\n\t" \
  "buffer_load_dword v124, v232, s[48:51], s60 offen offset:0 sc1\n\t" \
  "buffer_load_dwordx4 v[104:107], v232, s[48:51], s55 offen sc1\n\t" \
  "buffer_load_dword v125, v232, s[48:51], s61 offen offset:0 sc1\n\t" \
  "buffer_load_dwordx4 v[108:111], v232, s[48:51], s56 offen sc1\n\t" \
  "buffer_load_dword v126, v232, s[48:51], s62 offen offset:0 sc1\n\t" \
  "s_waitcnt vmcnt(0)\n\t" \
  "s_branch L_V0_%=\n\t" \
  "L_W1_%=:\n\t" \
  "s_add_u32 s82, s82, 1\n\t" \
  "s_cmp_gt_u32 s82, 400000\n\t" \
  "s_cbranch_scc1 L_X1_%=\n\t" \
  "s_sleep 1\n\t" \
  "buffer_load_dwordx4 v[112:115], v232, s[48:51], s57 offen sc1\n\t" \
  "buffer_load_dword v127, v232, s[48:51], s60 offen offset:1024 sc1\n\t" \
  "buffer_load_dwordx4 v[116:119], v232, s[48:51], s58 offen sc1\n\t" \
  "buffer_load_dword v128, v232, s[48:51], s61 offen offset:1024 sc1\n\t" \
  "buffer_load_dwordx4 v[120:123], v232, s[48:51], s59 offen sc1\n\t" \
  "buffer_load_dword v129, v232, s[48:51], s62 offen offset:1024 sc1\n\t" \
  "s_waitcnt vmcnt(0)\n\t" \
  "s_branch L_V1_%=\n\t" \
  "L_Z_%=:\n\t" \
  ""
#define CORE_PRE 10
#define CORE_P_CLOB "s40","s41","s42","s43","s75","s76","s79","s82"
#define CORE_C_CLOB "v0","v1","v2","v3","v4","v5","v6","v7","v8","v9","v10","v11","v12","v13","v14","v15","v16","v17","v18","v19","v20","v21","v22","v23","v24","v25","v26","v27","v28","v29","v30","v31","v32","v33","v34","v35","v36","v37","v38","v39","v40","v41","v42","v43","v44","v45","v46","v47","v48","v49","v50","v51","v52","v53","v54","v55","v56","v57","v58","v59","v60","v61","v62","v63","v64","v65","v66","v67","v68","v69","v70","v71","v72","v73","v74","v75","v76","v77","v78","v79","v80","v81","v82","v83","v84","v85","v86","v87","v88","v89","v90","v91","v92","v93","v94","v95","v96","v97","v98","v99","v100","v101","v102","v103","v104","v105","v106","v107","v108","v109","v110","v111","v112","v113","v114","v115","v116","v117","v118","v119","v120","v121","v122","v123","v124","v125","v126","v127","v128","v129","v130","v131","v132","v133","v134","v135","v136","v137","v138","v139","v140","v141","v142","v143","v144","v145","v146","v147","v148","v149","v150","v151","v152","v153","v154","v155","v156","v157","v158","v159","v160","v161","v162","v163","v164","v165","v166","v167","v168","v169","v170","v171","v172","v173","v174","v175","v176","v177","v178","v179","v180","v181","v182","v183","v184","v185","v186","v187","v188","v189","v190","v191","v192","v193","v194","v195","v196","v197","v198","v199","v200","v201","v202","v203","v204","v205","v206","v207","v208","v209","v210","v211","v212","v213","v214","v215","v216","v217","v218","v219","v220","v221","v222","v223","v224","v225","v226","v227","v228","v229","v230","v231","v232","v233","v234","v235","v236","v237","v238","v239","v240","v241","v242","v243","v244","v245","v246"
#define CORE_L_CLOB "v100","v101","v102","v103","v104","v105","v106","v107","v108","v109","v110","v111","v112","v113","v114","v115","v116","v117","v118","v119","v120","v121","v122","v123","v124","v125","v126","v127","v128","v129","v130","v131","v132","v133","v134","v135","v136","v137","v138","v139","v140","v141","v142","v143","v144","v145","v146","v147","v148","v149","v150","v151","v152","v153","v154","v155","v156","v157","v158","v159","v160","v161","v162","v163","v164","v165","v166","v167","v168","v169","v170","v171","v172","v173","v174","v175","v176","v177","v178","v179","v180","v181","v182","v183","v184","v185","v186","v187","v188","v189","v190","v191","v192","v193","v194","v195","v196","v197","v198","v199","v200","v201","v202","v203","v204","v205","v206","v207","v208","v209","v210","v211","v212","v213","v214","v215","v216","v217","v218","v219","v220","v221","v222","v223","v224","v225","v226","v227","v228","v229","v230","v231","v232","v233","v234","v235","v236","v237","v238","v239","v240","v241","v242","v243","v244","v245","v246","v247"
#define CORE_S_CLOB "s40","s41","s42","s43","s44","s45","s46","s47","s48","s49","s50","s51","s54","s55","s56","s57","s58","s59","s60","s61","s62","s63","s64","s65","s66","s67","s68","s69","s70","s71","s72","s73","s74","s75","s76","s77","s78","s79","s80","s81","s82"
#define CORE_SC_CLOB "s48","s49","s50","s51","s71","s75","s77","s78","s82","s94","s95"
#define P0_0 \
  "s_load_dword %[tag], %[ws], 0x0\n\t" \
  "v_xor_b32 v85, 32, %[aw]\n\t" \
  "v_add_u32 v86, 32768, %[aw]\n\t" \
  "v_add_u32 v87, 32768, v85\n\t" \
  "v_mov_b32 v84, %[aw]\n\t" \
  "v_mov_b32 v80, 0x3f803f80\n\t" \
  "v_mov_b32 v81, 0x3f803f80\n\t" \
  "v_mov_b32 v82, 0x3f803f80\n\t" \
  "v_mov_b32 v83, 0x3f803f80\n\t" \
  "v_xor_b32 v88, 0, %[ax]\n\t" \
  "v_xor_b32 v89, 2048, %[ax]\n\t" \
  "v_xor_b32 v92, 64, %[ax]\n\t" \
  "v_xor_b32 v93, 2112, %[ax]\n\t" \
  "s_waitcnt vmcnt(0) lgkmcnt(0)\n\t" \
  "s_add_u32 %[tag], %[tag], 1\n\t" \
  "s_barrier\n\t" \
  "s_cmp_lg_u32 %[w1k], 0\n\t" \
  "s_cbranch_scc1 L_F1_%=\n\t" \
  "v_mov_b32 v96, %[tag]\n\t" \
  "s_mov_b64 exec, 0xffff\n\t" \
  "global_store_dword %[vf1], v96, %[ws] sc1\n\t" \
  "s_mov_b64 exec, -1\n\t" \
  "L_F1_%=:\n\t" \
  "ds_read_b128 v[0:3], v88 offset:0\n\t" \
  "ds_read_b64_tr_b16 v[48:49], v84 offset:0\n\t" \
  "ds_read_b64_tr_b16 v[50:51], v84 offset:512\n\t" \
  "ds_read_b128 v[4:7], v89 offset:0\n\t" \
  "ds_read_b64_tr_b16 v[52:53], v85 offset:0\n\t" \
  "ds_read_b64_tr_b16 v[54:55], v85 offset:512\n\t" \
  "ds_read_b128 v[8:11], v92 offset:0\n\t" \
  "ds_read_b64_tr_b16 v[56:57], v84 offset:4096\n\t" \
  "ds_read_b64_tr_b16 v[58:59], v84 offset:4608\n\t" \
  "ds_read_b128 v[12:15], v93 offset:0\n\t" \
  "ds_read_b64_tr_b16 v[60:61], v85 offset:4096\n\t" \
  "ds_read_b64_tr_b16 v[62:63], v85 offset:4608\n\t" \
  "s_waitcnt lgkmcnt(6)\n\t" \
  "v_mfma_f32_16x16x32_bf16 %[acc0], v[48:51], v[0:3], 0\n\t" \
  "ds_read_b128 v[16:19], v88 offset:12288\n\t" \
  "ds_read_b64_tr_b16 v[64:65], v84 offset:8192\n\t" \
  "v_mfma_f32_16x16x32_bf16 %[acc2], v[48:51], v[4:7], 0\n\t" \
  "ds_read_b64_tr_b16 v[66:67], v84 offset:8704\n\t" \
  "ds_read_b128 v[20:23], v89 offset:12288\n\t" \
  "v_mfma_f32_16x16x32_bf16 %[e0], v[48:51], v[80:83], 0\n\t" \
  "v_mfma_f32_16x16x32_bf16 %[acc1], v[52:55], v[0:3], 0\n\t" \
  "ds_read_b64_tr_b16 v[68:69], v85 offset:8192\n\t" \
  "ds_read_b64_tr_b16 v[70:71], v85 offset:8704\n\t" \
  "v_mfma_f32_16x16x32_bf16 %[acc3], v[52:55], v[4:7], 0\n\t" \
  "s_waitcnt lgkmcnt(6)\n\t" \
  "v_mfma_f32_16x16x32_bf16 %[acc0], v[56:59], v[8:11], %[acc0]\n\t" \
  "ds_read_b128 v[24:27], v92 offset:12288\n\t" \
  "ds_read_b64_tr_b16 v[72:73], v84 offset:12288\n\t" \
  "v_mfma_f32_16x16x32_bf16 %[acc2], v[56:59], v[12:15], %[acc2]\n\t" \
  "ds_read_b64_tr_b16 v[74:75], v84 offset:12800\n\t" \
  "ds_read_b128 v[28:31], v93 offset:12288\n\t" \
  "v_mfma_f32_16x16x32_bf16 %[e0], v[56:59], v[80:83], %[e0]\n\t" \
  "v_mfma_f32_16x16x32_bf16 %[acc1], v[60:63], v[8:11], %[acc1]\n\t" \
  "ds_read_b64_tr_b16 v[76:77], v85 offset:12288\n\t" \
  "ds_read_b64_tr_b16 v[78:79], v85 offset:12800\n\t" \
  "v_mfma_f32_16x16x32_bf16 %[acc3], v[60:63], v[12:15], %[acc3]\n\t" \
  "s_waitcnt lgkmcnt(6)\n\t" \
  "v_mfma_f32_16x16x32_bf16 %[acc0], v[64:67], v[16:19], %[acc0]\n\t" \
  "ds_read_b128 v[32:35], v88 offset:24576\n\t" \
  "ds_read_b64_tr_b16 v[48:49], v84 offset:16384\n\t" \
  "v_mfma_f32_16x16x32_bf16 %[acc2], v[64:67], v[20:23], %[acc2]\n\t" \
  "ds_read_b64_tr_b16 v[50:51], v84 offset:16896\n\t" \
  "ds_read_b128 v[36:39], v89 offset:24576\n\t" \
  "v_mfma_f32_16x16x32_bf16 %[e0], v[64:67], v[80:83], %[e0]\n\t" \
  "v_mfma_f32_16x16x32_bf16 %[acc1], v[68:71], v[16:19], %[acc1]\n\t" \
  "ds_read_b64_tr_b16 v[52:53], v85 offset:16384\n\t" \
  "ds_read_b64_tr_b16 v[54:55], v85 offset:16896\n\t" \
  "v_mfma_f32_16x16x32_bf16 %[acc3], v[68:71], v[20:23], %[acc3]\n\t" \
  "s_waitcnt lgkmcnt(6)\n\t" \
  "v_mfma_f32_16x16x32_bf16 %[acc0], v[72:75], v[24:27], %[acc0]\n\t" \
  "ds_read_b128 v[40:43], v92 offset:24576\n\t" \
  "ds_read_b64_tr_b16 v[56:57], v84 offset:20480\n\t" \
  "v_mfma_f32_16x16x32_bf16 %[acc2], v[72:75], v[28:31], %[acc2]\n\t" \
  "ds_read_b64_tr_b16 v[58:59], v84 offset:20992\n\t" \
  "ds_read_b128 v[44:47], v93 offset:24576\n\t" \
  "v_mfma_f32_16x16x32_bf16 %[e0], v[72:75], v[80:83], %[e0]\n\t" \
  "v_mfma_f32_16x16x32_bf16 %[acc1], v[76:79], v[24:27], %[acc1]\n\t" \
  "ds_read_b64_tr_b16 v[60:61], v85 offset:20480\n\t" \
  "ds_read_b64_tr_b16 v[62:63], v85 offset:20992\n\t" \
  "v_mfma_f32_16x16x32_bf16 %[acc3], v[76:79], v[28:31], %[acc3]\n\t" \
  "s_waitcnt lgkmcnt(6)\n\t" \
  "v_mfma_f32_16x16x32_bf16 %[acc0], v[48:51], v[32:35], %[acc0]\n\t" \
  "v_mfma_f32_16x16x32_bf16 %[acc2], v[48:51], v[36:39], %[acc2]\n\t" \
  "v_mfma_f32_16x16x32_bf16 %[e0], v[48:51], v[80:83], %[e0]\n\t" \
  "v_mfma_f32_16x16x32_bf16 %[acc1], v[52:55], v[32:35], %[acc1]\n\t" \
  "v_mfma_f32_16x16x32_bf16 %[acc3], v[52:55], v[36:39], %[acc3]\n\t" \
  "s_waitcnt lgkmcnt(0)\n\t" \
  "v_mfma_f32_16x16x32_bf16 %[acc0], v[56:59], v[40:43], %[acc0]\n\t" \
  "v_mfma_f32_16x16x32_bf16 %[acc2], v[56:59], v[44:47], %[acc2]\n\t" \
  "v_mfma_f32_16x16x32_bf16 %[e0], v[56:59], v[80:83], %[e0]\n\t" \
  "v_mfma_f32_16x16x32_bf16 %[acc1], v[60:63], v[40:43], %[acc1]\n\t" \
  "v_mfma_f32_16x16x32_bf16 %[acc3], v[60:63], v[44:47], %[acc3]\n\t" \
  "s_barrier\n\t" \
  "global_load_dwordx2 %[skw], %[sko], %[sk]\n\t" \
  "ds_read_b128 v[0:3], v88 offset:36864\n\t" \
  "ds_read_b64_tr_b16 v[64:65], v84 offset:24576\n\t" \
  "ds_read_b64_tr_b16 v[66:67], v84 offset:25088\n\t" \
  "ds_read_b128 v[4:7], v89 offset:36864\n\t" \
  "ds_read_b64_tr_b16 v[68:69], v85 offset:24576\n\t" \
  "ds_read_b64_tr_b16 v[70:71], v85 offset:25088\n\t" \
  "ds_read_b128 v[8:11], v92 offset:36864\n\t" \
  "ds_read_b64_tr_b16 v[72:73], v84 offset:28672\n\t" \
  "ds_read_b64_tr_b16 v[74:75], v84 offset:29184\n\t" \
  "ds_read_b128 v[12:15], v93 offset:36864\n\t" \
  "ds_read_b64_tr_b16 v[76:77], v85 offset:28672\n\t" \
  "ds_read_b64_tr_b16 v[78:79], v85 offset:29184\n\t" \
  "s_waitcnt lgkmcnt(6)\n\t" \
  "v_mfma_f32_16x16x32_bf16 %[acc0], v[64:67], v[0:3], %[acc0]\n\t" \
  "ds_read_b128 v[16:19], v88 offset:49152\n\t" \
  "ds_read_b64_tr_b16 v[48:49], v86 offset:0\n\t" \
  "v_mfma_f32_16x16x32_bf16 %[acc2], v[64:67], v[4:7], %[acc2]\n\t" \
  "ds_read_b64_tr_b16 v[50:51], v86 offset:512\n\t" \
  "ds_read_b128 v[20:23], v89 offset:49152\n\t" \
  "v_mfma_f32_16x16x32_bf16 %[e0], v[64:67], v[80:83], %[e0]\n\t" \
  "v_mfma_f32_16x16x32_bf16 %[acc1], v[68:71], v[0:3], %[acc1]\n\t" \
  "ds_read_b64_tr_b16 v[52:53], v87 offset:0\n\t" \
  "ds_read_b64_tr_b16 v[54:55], v87 offset:512\n\t" \
  "v_mfma_f32_16x16x32_bf16 %[acc3], v[68:71], v[4:7], %[acc3]\n\t" \
  "s_waitcnt lgkmcnt(6)\n\t" \
  "v_mfma_f32_16x16x32_bf16 %[acc0], v[72:75], v[8:11], %[acc0]\n\t" \
  "ds_read_b128 v[24:27], v92 offset:49152\n\t" \
  "ds_read_b64_tr_b16 v[56:57], v86 offset:4096\n\t" \
  "v_mfma_f32_16x16x32_bf16 %[acc2], v[72:75], v[12:15], %[acc2]\n\t" \
  "ds_read_b64_tr_b16 v[58:59], v86 offset:4608\n\t" \
  "ds_read_b128 v[28:31], v93 offset:49152\n\t" \
  "v_mfma_f32_16x16x32_bf16 %[e0], v[72:75], v[80:83], %[e0]\n\t" \
  "v_mfma_f32_16x16x32_bf16 %[acc1], v[76:79], v[8:11], %[acc1]\n\t" \
  "ds_read_b64_tr_b16 v[60:61], v87 offset:4096\n\t" \
  "ds_read_b64_tr_b16 v[62:63], v87 offset:4608\n\t" \
  "v_mfma_f32_16x16x32_bf16 %[acc3], v[76:79], v[12:15], %[acc3]\n\t" \
  "s_waitcnt lgkmcnt(6)\n\t" \
  "v_mfma_f32_16x16x32_bf16 %[acc0], v[48:51], v[16:19], %[acc0]\n\t" \
  "ds_read_b128 v[32:35], v88 offset:61440\n\t" \
  "ds_read_b64_tr_b16 v[64:65], v86 offset:8192\n\t" \
  "v_mfma_f32_16x16x32_bf16 %[acc2], v[48:51], v[20:23], %[acc2]\n\t" \
  "ds_read_b64_tr_b16 v[66:67], v86 offset:8704\n\t" \
  "ds_read_b128 v[36:39], v89 offset:61440\n\t" \
  "v_mfma_f32_16x16x32_bf16 %[e0], v[48:51], v[80:83], %[e0]\n\t" \
  "v_mfma_f32_16x16x32_bf16 %[acc1], v[52:55], v[16:19], %[acc1]\n\t" \
  "ds_read_b64_tr_b16 v[68:69], v87 offset:8192\n\t" \
  "ds_read_b64_tr_b16 v[70:71], v87 offset:8704\n\t" \
  "v_mfma_f32_16x16x32_bf16 %[acc3], v[52:55], v[20:23], %[acc3]\n\t" \
  "s_waitcnt lgkmcnt(6)\n\t" \
  "v_mfma_f32_16x16x32_bf16 %[acc0], v[56:59], v[24:27], %[acc0]\n\t" \
  "ds_read_b128 v[40:43], v92 offset:61440\n\t" \
  "ds_read_b64_tr_b16 v[72:73], v86 offset:12288\n\t" \
  "v_mfma_f32_16x16x32_bf16 %[acc2], v[56:59], v[28:31], %[acc2]\n\t" \
  "ds_read_b64_tr_b16 v[74:75], v86 offset:12800\n\t" \
  "ds_read_b128 v[44:47], v93 offset:61440\n\t" \
  "v_mfma_f32_16x16x32_bf16 %[e0], v[56:59], v[80:83], %[e0]\n\t" \
  "v_mfma_f32_16x16x32_bf16 %[acc1], v[60:63], v[24:27], %[acc1]\n\t" \
  "ds_read_b64_tr_b16 v[76:77], v87 offset:12288\n\t" \
  "ds_read_b64_tr_b16 v[78:79], v87 offset:12800\n\t" \
  "v_mfma_f32_16x16x32_bf16 %[acc3], v[60:63], v[28:31], %[acc3]\n\t" \
  "s_waitcnt lgkmcnt(6)\n\t" \
  "v_mfma_f32_16x16x32_bf16 %[acc0], v[64:67], v[32:35], %[acc0]\n\t" \
  "v_mfma_f32_16x16x32_bf16 %[acc2], v[64:67], v[36:39], %[acc2]\n\t" \
  "v_mfma_f32_16x16x32_bf16 %[e0], v[64:67], v[80:83], %[e0]\n\t" \
  "v_mfma_f32_16x16x32_bf16 %[acc1], v[68:71], v[32:35], %[acc1]\n\t" \
  "v_mfma_f32_16x16x32_bf16 %[acc3], v[68:71], v[36:39], %[acc3]\n\t" \
  "s_waitcnt lgkmcnt(0)\n\t" \
  "v_mfma_f32_16x16x32_bf16 %[acc0], v[72:75], v[40:43], %[acc0]\n\t" \
  "v_mfma_f32_16x16x32_bf16 %[acc2], v[72:75], v[44:47], %[acc2]\n\t" \
  "v_mfma_f32_16x16x32_bf16 %[e0], v[72:75], v[80:83], %[e0]\n\t" \
  "v_mfma_f32_16x16x32_bf16 %[acc1], v[76:79], v[40:43], %[acc1]\n\t" \
  "v_mfma_f32_16x16x32_bf16 %[acc3], v[76:79], v[44:47], %[acc3]\n\t" \
  "s_barrier\n\t" \
  "ds_read_b128 v[0:3], v88 offset:0\n\t" \
  "ds_read_b64_tr_b16 v[48:49], v86 offset:16384\n\t" \
  "ds_read_b64_tr_b16 v[50:51], v86 offset:16896\n\t" \
  "ds_read_b128 v[4:7], v89 offset:0\n\t" \
  "ds_read_b64_tr_b16 v[52:53], v87 offset:16384\n\t" \
  "ds_read_b64_tr_b16 v[54:55], v87 offset:16896\n\t" \
  "ds_read_b128 v[8:11], v92 offset:0\n\t" \
  "ds_read_b64_tr_b16 v[56:57], v86 offset:20480\n\t" \
  "ds_read_b64_tr_b16 v[58:59], v86 offset:20992\n\t" \
  "ds_read_b128 v[12:15], v93 offset:0\n\t" \
  "ds_read_b64_tr_b16 v[60:61], v87 offset:20480\n\t" \
  "ds_read_b64_tr_b16 v[62:63], v87 offset:20992\n\t" \
  "s_waitcnt lgkmcnt(6)\n\t" \
  "v_mfma_f32_16x16x32_bf16 %[acc0], v[48:51], v[0:3], %[acc0]\n\t" \
  "ds_read_b128 v[16:19], v88 offset:12288\n\t" \
  "ds_read_b64_tr_b16 v[64:65], v86 offset:24576\n\t" \
  "v_mfma_f32_16x16x32_bf16 %[acc2], v[48:51], v[4:7], %[acc2]\n\t" \
  "ds_read_b64_tr_b16 v[66:67], v86 offset:25088\n\t" \
  "ds_read_b128 v[20:23], v89 offset:12288\n\t" \
  "v_mfma_f32_16x16x32_bf16 %[e0], v[48:51], v[80:83], %[e0]\n\t" \
  "v_mfma_f32_16x16x32_bf16 %[acc1], v[52:55], v[0:3], %[acc1]\n\t" \
  "ds_read_b64_tr_b16 v[68:69], v87 offset:24576\n\t" \
  "ds_read_b64_tr_b16 v[70:71], v87 offset:25088\n\t" \
  "v_mfma_f32_16x16x32_bf16 %[acc3], v[52:55], v[4:7], %[acc3]\n\t" \
  "s_waitcnt lgkmcnt(6)\n\t" \
  "v_mfma_f32_16x16x32_bf16 %[acc0], v[56:59], v[8:11], %[acc0]\n\t" \
  "ds_read_b128 v[24:27], v92 offset:12288\n\t" \
  "ds_read_b64_tr_b16 v[72:73], v86 offset:28672\n\t" \
  "v_mfma_f32_16x16x32_bf16 %[acc2], v[56:59], v[12:15], %[acc2]\n\t" \
  "ds_read_b64_tr_b16 v[74:75], v86 offset:29184\n\t" \
  "ds_read_b128 v[28:31], v93 offset:12288\n\t" \
  "v_mfma_f32_16x16x32_bf16 %[e0], v[56:59], v[80:83], %[e0]\n\t" \
  "v_mfma_f32_16x16x32_bf16 %[acc1], v[60:63], v[8:11], %[acc1]\n\t" \
  "ds_read_b64_tr_b16 v[76:77], v87 offset:28672\n\t" \
  "ds_read_b64_tr_b16 v[78:79], v87 offset:29184\n\t" \
  "v_mfma_f32_16x16x32_bf16 %[acc3], v[60:63], v[12:15], %[acc3]\n\t" \
  "s_waitcnt lgkmcnt(6)\n\t" \
  "v_mfma_f32_16x16x32_bf16 %[acc0], v[64:67], v[16:19], %[acc0]\n\t" \
  "ds_read_b128 v[32:35], v88 offset:24576\n\t" \
  "ds_read_b64_tr_b16 v[48:49], v86 offset:32768\n\t" \
  "v_mfma_f32_16x16x32_bf16 %[acc2], v[64:67], v[20:23], %[acc2]\n\t" \
  "ds_read_b64_tr_b16 v[50:51], v86 offset:33280\n\t" \
  "ds_read_b128 v[36:39], v89 offset:24576\n\t" \
  "v_mfma_f32_16x16x32_bf16 %[e0], v[64:67], v[80:83], %[e0]\n\t" \
  "v_mfma_f32_16x16x32_bf16 %[acc1], v[68:71], v[16:19], %[acc1]\n\t" \
  "ds_read_b64_tr_b16 v[52:53], v87 offset:32768\n\t" \
  "ds_read_b64_tr_b16 v[54:55], v87 offset:33280\n\t" \
  "v_mfma_f32_16x16x32_bf16 %[acc3], v[68:71], v[20:23], %[acc3]\n\t" \
  "s_waitcnt lgkmcnt(6)\n\t" \
  "v_mfma_f32_16x16x32_bf16 %[acc0], v[72:75], v[24:27], %[acc0]\n\t" \
  "ds_read_b128 v[40:43], v92 offset:24576\n\t" \
  "ds_read_b64_tr_b16 v[56:57], v86 offset:36864\n\t" \
  "v_mfma_f32_16x16x32_bf16 %[acc2], v[72:75], v[28:31], %[acc2]\n\t" \
  "ds_read_b64_tr_b16 v[58:59], v86 offset:37376\n\t" \
  "ds_read_b128 v[44:47], v93 offset:24576\n\t" \
  "v_mfma_f32_16x16x32_bf16 %[e0], v[72:75], v[80:83], %[e0]\n\t" \
  "v_mfma_f32_16x16x32_bf16 %[acc1], v[76:79], v[24:27], %[acc1]\n\t" \
  "ds_read_b64_tr_b16 v[60:61], v87 offset:36864\n\t" \
  "ds_read_b64_tr_b16 v[62:63], v87 offset:37376\n\t" \
  "v_mfma_f32_16x16x32_bf16 %[acc3], v[76:79], v[28:31], %[acc3]\n\t" \
  "s_waitcnt lgkmcnt(6)\n\t" \
  "v_mfma_f32_16x16x32_bf16 %[acc0], v[48:51], v[32:35], %[acc0]\n\t" \
  "v_mfma_f32_16x16x32_bf16 %[acc2], v[48:51], v[36:39], %[acc2]\n\t" \
  "v_mfma_f32_16x16x32_bf16 %[e0], v[48:51], v[80:83], %[e0]\n\t" \
  "v_mfma_f32_16x16x32_bf16 %[acc1], v[52:55], v[32:35], %[acc1]\n\t" \
  "v_mfma_f32_16x16x32_bf16 %[acc3], v[52:55], v[36:39], %[acc3]\n\t" \
  "s_waitcnt lgkmcnt(0)\n\t" \
  "v_mfma_f32_16x16x32_bf16 %[acc0], v[56:59], v[40:43], %[acc0]\n\t" \
  "v_mfma_f32_16x16x32_bf16 %[acc2], v[56:59], v[44:47], %[acc2]\n\t" \
  "v_mfma_f32_16x16x32_bf16 %[e0], v[56:59], v[80:83], %[e0]\n\t" \
  "v_mfma_f32_16x16x32_bf16 %[acc1], v[60:63], v[40:43], %[acc1]\n\t" \
  "v_mfma_f32_16x16x32_bf16 %[acc3], v[60:63], v[44:47], %[acc3]\n\t" \
  "s_waitcnt vmcnt(0)\n\t" \
  "s_nop 6\n\t" \
  ""
#define P0_1 \
  "s_load_dword %[tag], %[ws], 0x0\n\t" \
  "v_xor_b32 v85, 32, %[aw]\n\t" \
  "v_add_u32 v86, 32768, %[aw]\n\t" \
  "v_add_u32 v87, 32768, v85\n\t" \
  "v_mov_b32 v84, %[aw]\n\t" \
  "v_mov_b32 v80, 0x3f803f80\n\t" \
  "v_mov_b32 v81, 0x3f803f80\n\t" \
  "v_mov_b32 v82, 0x3f803f80\n\t" \
  "v_mov_b32 v83, 0x3f803f80\n\t" \
  "v_xor_b32 v88, 0, %[ax]\n\t" \
  "v_xor_b32 v89, 2048, %[ax]\n\t" \
  "v_xor_b32 v92, 64, %[ax]\n\t" \
  "v_xor_b32 v93, 2112, %[ax]\n\t" \
  "s_waitcnt vmcnt(0) lgkmcnt(0)\n\t" \
  "s_add_u32 %[tag], %[tag], 1\n\t" \
  "s_barrier\n\t" \
  "ds_read_b128 v[0:3], v88 offset:0\n\t" \
  "ds_read_b64_tr_b16 v[48:49], v84 offset:0\n\t" \
  "ds_read_b64_tr_b16 v[50:51], v84 offset:512\n\t" \
  "ds_read_b128 v[4:7], v89 offset:0\n\t" \
  "ds_read_b64_tr_b16 v[52:53], v85 offset:0\n\t" \
  "ds_read_b64_tr_b16 v[54:55], v85 offset:512\n\t" \
  "ds_read_b128 v[8:11], v92 offset:0\n\t" \
  "ds_read_b64_tr_b16 v[56:57], v84 offset:4096\n\t" \
  "ds_read_b64_tr_b16 v[58:59], v84 offset:4608\n\t" \
  "ds_read_b128 v[12:15], v93 offset:0\n\t" \
  "ds_read_b64_tr_b16 v[60:61], v85 offset:4096\n\t" \
  "ds_read_b64_tr_b16 v[62:63], v85 offset:4608\n\t" \
  "s_waitcnt lgkmcnt(6)\n\t" \
  "v_mfma_f32_16x16x32_bf16 %[acc0], v[48:51], v[0:3], 0\n\t" \
  "ds_read_b128 v[16:19], v88 offset:12288\n\t" \
  "ds_read_b64_tr_b16 v[64:65], v84 offset:8192\n\t" \
  "v_mfma_f32_16x16x32_bf16 %[acc2], v[48:51], v[4:7], 0\n\t" \
  "ds_read_b64_tr_b16 v[66:67], v84 offset:8704\n\t" \
  "ds_read_b128 v[20:23], v89 offset:12288\n\t" \
  "v_mfma_f32_16x16x32_bf16 %[acc1], v[52:55], v[0:3], 0\n\t" \
  "ds_read_b64_tr_b16 v[68:69], v85 offset:8192\n\t" \
  "ds_read_b64_tr_b16 v[70:71], v85 offset:8704\n\t" \
  "v_mfma_f32_16x16x32_bf16 %[acc3], v[52:55], v[4:7], 0\n\t" \
  "v_mfma_f32_16x16x32_bf16 %[e0], v[52:55], v[80:83], 0\n\t" \
  "s_waitcnt lgkmcnt(6)\n\t" \
  "v_mfma_f32_16x16x32_bf16 %[acc0], v[56:59], v[8:11], %[acc0]\n\t" \
  "ds_read_b128 v[24:27], v92 offset:12288\n\t" \
  "ds_read_b64_tr_b16 v[72:73], v84 offset:12288\n\t" \
  "v_mfma_f32_16x16x32_bf16 %[acc2], v[56:59], v[12:15], %[acc2]\n\t" \
  "ds_read_b64_tr_b16 v[74:75], v84 offset:12800\n\t" \
  "ds_read_b128 v[28:31], v93 offset:12288\n\t" \
  "v_mfma_f32_16x16x32_bf16 %[acc1], v[60:63], v[8:11], %[acc1]\n\t" \
  "ds_read_b64_tr_b16 v[76:77], v85 offset:12288\n\t" \
  "ds_read_b64_tr_b16 v[78:79], v85 offset:12800\n\t" \
  "v_mfma_f32_16x16x32_bf16 %[acc3], v[60:63], v[12:15], %[acc3]\n\t" \
  "v_mfma_f32_16x16x32_bf16 %[e0], v[60:63], v[80:83], %[e0]\n\t" \
  "s_waitcnt lgkmcnt(6)\n\t" \
  "v_mfma_f32_16x16x32_bf16 %[acc0], v[64:67], v[16:19], %[acc0]\n\t" \
  "ds_read_b128 v[32:35], v88 offset:24576\n\t" \
  "ds_read_b64_tr_b16 v[48:49], v84 offset:16384\n\t" \
  "v_mfma_f32_16x16x32_bf16 %[acc2], v[64:67], v[20:23], %[acc2]\n\t" \
  "ds_read_b64_tr_b16 v[50:51], v84 offset:16896\n\t" \
  "ds_read_b128 v[36:39], v89 offset:24576\n\t" \
  "v_mfma_f32_16x16x32_bf16 %[acc1], v[68:71], v[16:19], %[acc1]\n\t" \
  "ds_read_b64_tr_b16 v[52:53], v85 offset:16384\n\t" \
  "ds_read_b64_tr_b16 v[54:55], v85 offset:16896\n\t" \
  "v_mfma_f32_16x16x32_bf16 %[acc3], v[68:71], v[20:23], %[acc3]\n\t" \
  "v_mfma_f32_16x16x32_bf16 %[e0], v[68:71], v[80:83], %[e0]\n\t" \
  "s_waitcnt lgkmcnt(6)\n\t" \
  "v_mfma_f32_16x16x32_bf16 %[acc0], v[72:75], v[24:27], %[acc0]\n\t" \
  "ds_read_b128 v[40:43], v92 offset:24576\n\t" \
  "ds_read_b64_tr_b16 v[56:57], v84 offset:20480\n\t" \
  "v_mfma_f32_16x16x32_bf16 %[acc2], v[72:75], v[28:31], %[acc2]\n\t" \
  "ds_read_b64_tr_b16 v[58:59], v84 offset:20992\n\t" \
  "ds_read_b128 v[44:47], v93 offset:24576\n\t" \
  "v_mfma_f32_16x16x32_bf16 %[acc1], v[76:79], v[24:27], %[acc1]\n\t" \
  "ds_read_b64_tr_b16 v[60:61], v85 offset:20480\n\t" \
  "ds_read_b64_tr_b16 v[62:63], v85 offset:20992\n\t" \
  "v_mfma_f32_16x16x32_bf16 %[acc3], v[76:79], v[28:31], %[acc3]\n\t" \
  "v_mfma_f32_16x16x32_bf16 %[e0], v[76:79], v[80:83], %[e0]\n\t" \
  "s_waitcnt lgkmcnt(6)\n\t" \
  "v_mfma_f32_16x16x32_bf16 %[acc0], v[48:51], v[32:35], %[acc0]\n\t" \
  "v_mfma_f32_16x16x32_bf16 %[acc2], v[48:51], v[36:39], %[acc2]\n\t" \
  "v_mfma_f32_16x16x32_bf16 %[acc1], v[52:55], v[32:35], %[acc1]\n\t" \
  "v_mfma_f32_16x16x32_bf16 %[acc3], v[52:55], v[36:39], %[acc3]\n\t" \
  "v_mfma_f32_16x16x32_bf16 %[e0], v[52:55], v[80:83], %[e0]\n\t" \
  "s_waitcnt lgkmcnt(0)\n\t" \
  "v_mfma_f32_16x16x32_bf16 %[acc0], v[56:59], v[40:43], %[acc0]\n\t" \
  "v_mfma_f32_16x16x32_bf16 %[acc2], v[56:59], v[44:47], %[acc2]\n\t" \
  "v_mfma_f32_16x16x32_bf16 %[acc1], v[60:63], v[40:43], %[acc1]\n\t" \
  "v_mfma_f32_16x16x32_bf16 %[acc3], v[60:63], v[44:47], %[acc3]\n\t" \
  "v_mfma_f32_16x16x32_bf16 %[e0], v[60:63], v[80:83], %[e0]\n\t" \
  "s_barrier\n\t" \
  "global_load_dwordx2 %[skw], %[sko], %[sk]\n\t" \
  "ds_read_b128 v[0:3], v88 offset:36864\n\t" \
  "ds_read_b64_tr_b16 v[64:65], v84 offset:24576\n\t" \
  "ds_read_b64_tr_b16 v[66:67], v84 offset:25088\n\t" \
  "ds_read_b128 v[4:7], v89 offset:36864\n\t" \
  "ds_read_b64_tr_b16 v[68:69], v85 offset:24576\n\t" \
  "ds_read_b64_tr_b16 v[70:71], v85 offset:25088\n\t" \
  "ds_read_b128 v[8:11], v92 offset:36864\n\t" \
  "ds_read_b64_tr_b16 v[72:73], v84 offset:28672\n\t" \
  "ds_read_b64_tr_b16 v[74:75], v84 offset:29184\n\t" \
  "ds_read_b128 v[12:15], v93 offset:36864\n\t" \
  "ds_read_b64_tr_b16 v[76:77], v85 offset:28672\n\t" \
  "ds_read_b64_tr_b16 v[78:79], v85 offset:29184\n\t" \
  "s_waitcnt lgkmcnt(6)\n\t" \
  "v_mfma_f32_16x16x32_bf16 %[acc0], v[64:67], v[0:3], %[acc0]\n\t" \
  "ds_read_b128 v[16:19], v88 offset:49152\n\t" \
  "ds_read_b64_tr_b16 v[48:49], v86 offset:0\n\t" \
  "v_mfma_f32_16x16x32_bf16 %[acc2], v[64:67], v[4:7], %[acc2]\n\t" \
  "ds_read_b64_tr_b16 v[50:51], v86 offset:512\n\t" \
  "ds_read_b128 v[20:23], v89 offset:49152\n\t" \
  "v_mfma_f32_16x16x32_bf16 %[acc1], v[68:71], v[0:3], %[acc1]\n\t" \
  "ds_read_b64_tr_b16 v[52:53], v87 offset:0\n\t" \
  "ds_read_b64_tr_b16 v[54:55], v87 offset:512\n\t" \
  "v_mfma_f32_16x16x32_bf16 %[acc3], v[68:71], v[4:7], %[acc3]\n\t" \
  "v_mfma_f32_16x16x32_bf16 %[e0], v[68:71], v[80:83], %[e0]\n\t" \
  "s_waitcnt lgkmcnt(6)\n\t" \
  "v_mfma_f32_16x16x32_bf16 %[acc0], v[72:75], v[8:11], %[acc0]\n\t" \
  "ds_read_b128 v[24:27], v92 offset:49152\n\t" \
  "ds_read_b64_tr_b16 v[56:57], v86 offset:4096\n\t" \
  "v_mfma_f32_16x16x32_bf16 %[acc2], v[72:75], v[12:15], %[acc2]\n\t" \
  "ds_read_b64_tr_b16 v[58:59], v86 offset:4608\n\t" \
  "ds_read_b128 v[28:31], v93 offset:49152\n\t" \
  "v_mfma_f32_16x16x32_bf16 %[acc1], v[76:79], v[8:11], %[acc1]\n\t" \
  "ds_read_b64_tr_b16 v[60:61], v87 offset:4096\n\t" \
  "ds_read_b64_tr_b16 v[62:63], v87 offset:4608\n\t" \
  "v_mfma_f32_16x16x32_bf16 %[acc3], v[76:79], v[12:15], %[acc3]\n\t" \
  "v_mfma_f32_16x16x32_bf16 %[e0], v[76:79], v[80:83], %[e0]\n\t" \
  "s_waitcnt lgkmcnt(6)\n\t" \
  "v_mfma_f32_16x16x32_bf16 %[acc0], v[48:51], v[16:19], %[acc0]\n\t" \
  "ds_read_b128 v[32:35], v88 offset:61440\n\t" \
  "ds_read_b64_tr_b16 v[64:65], v86 offset:8192\n\t" \
  "v_mfma_f32_16x16x32_bf16 %[acc2], v[48:51], v[20:23], %[acc2]\n\t" \
  "ds_read_b64_tr_b16 v[66:67], v86 offset:8704\n\t" \
  "ds_read_b128 v[36:39], v89 offset:61440\n\t" \
  "v_mfma_f32_16x16x32_bf16 %[acc1], v[52:55], v[16:19], %[acc1]\n\t" \
  "ds_read_b64_tr_b16 v[68:69], v87 offset:8192\n\t" \
  "ds_read_b64_tr_b16 v[70:71], v87 offset:8704\n\t" \
  "v_mfma_f32_16x16x32_bf16 %[acc3], v[52:55], v[20:23], %[acc3]\n\t" \
  "v_mfma_f32_16x16x32_bf16 %[e0], v[52:55], v[80:83], %[e0]\n\t" \
  "s_waitcnt lgkmcnt(6)\n\t" \
  "v_mfma_f32_16x16x32_bf16 %[acc0], v[56:59], v[24:27], %[acc0]\n\t" \
  "ds_read_b128 v[40:43], v92 offset:61440\n\t" \
  "ds_read_b64_tr_b16 v[72:73], v86 offset:12288\n\t" \
  "v_mfma_f32_16x16x32_bf16 %[acc2], v[56:59], v[28:31], %[acc2]\n\t" \
  "ds_read_b64_tr_b16 v[74:75], v86 offset:12800\n\t" \
  "ds_read_b128 v[44:47], v93 offset:61440\n\t" \
  "v_mfma_f32_16x16x32_bf16 %[acc1], v[60:63], v[24:27], %[acc1]\n\t" \
  "ds_read_b64_tr_b16 v[76:77], v87 offset:12288\n\t" \
  "ds_read_b64_tr_b16 v[78:79], v87 offset:12800\n\t" \
  "v_mfma_f32_16x16x32_bf16 %[acc3], v[60:63], v[28:31], %[acc3]\n\t" \
  "v_mfma_f32_16x16x32_bf16 %[e0], v[60:63], v[80:83], %[e0]\n\t" \
  "s_waitcnt lgkmcnt(6)\n\t" \
  "v_mfma_f32_16x16x32_bf16 %[acc0], v[64:67], v[32:35], %[acc0]\n\t" \
  "v_mfma_f32_16x16x32_bf16 %[acc2], v[64:67], v[36:39], %[acc2]\n\t" \
  "v_mfma_f32_16x16x32_bf16 %[acc1], v[68:71], v[32:35], %[acc1]\n\t" \
  "v_mfma_f32_16x16x32_bf16 %[acc3], v[68:71], v[36:39], %[acc3]\n\t" \
  "v_mfma_f32_16x16x32_bf16 %[e0], v[68:71], v[80:83], %[e0]\n\t" \
  "s_waitcnt lgkmcnt(0)\n\t" \
  "v_mfma_f32_16x16x32_bf16 %[acc0], v[72:75], v[40:43], %[acc0]\n\t" \
  "v_mfma_f32_16x16x32_bf16 %[acc2], v[72:75], v[44:47], %[acc2]\n\t" \
  "v_mfma_f32_16x16x32_bf16 %[acc1], v[76:79], v[40:43], %[acc1]\n\t" \
  "v_mfma_f32_16x16x32_bf16 %[acc3], v[76:79], v[44:47], %[acc3]\n\t" \
  "v_mfma_f32_16x16x32_bf16 %[e0], v[76:79], v[80:83], %[e0]\n\t" \
  "s_barrier\n\t" \
  "ds_read_b128 v[0:3], v88 offset:0\n\t" \
  "ds_read_b64_tr_b16 v[48:49], v86 offset:16384\n\t" \
  "ds_read_b64_tr_b16 v[50:51], v86 offset:16896\n\t" \
  "ds_read_b128 v[4:7], v89 offset:0\n\t" \
  "ds_read_b64_tr_b16 v[52:53], v87 offset:16384\n\t" \
  "ds_read_b64_tr_b16 v[54:55], v87 offset:16896\n\t" \
  "ds_read_b128 v[8:11], v92 offset:0\n\t" \
  "ds_read_b64_tr_b16 v[56:57], v86 offset:20480\n\t" \
  "ds_read_b64_tr_b16 v[58:59], v86 offset:20992\n\t" \
  "ds_read_b128 v[12:15], v93 offset:0\n\t" \
  "ds_read_b64_tr_b16 v[60:61], v87 offset:20480\n\t" \
  "ds_read_b64_tr_b16 v[62:63], v87 offset:20992\n\t" \
  "s_waitcnt lgkmcnt(6)\n\t" \
  "v_mfma_f32_16x16x32_bf16 %[acc0], v[48:51], v[0:3], %[acc0]\n\t" \
  "ds_read_b128 v[16:19], v88 offset:12288\n\t" \
  "ds_read_b64_tr_b16 v[64:65], v86 offset:24576\n\t" \
  "v_mfma_f32_16x16x32_bf16 %[acc2], v[48:51], v[4:7], %[acc2]\n\t" \
  "ds_read_b64_tr_b16 v[66:67], v86 offset:25088\n\t" \
  "ds_read_b128 v[20:23], v89 offset:12288\n\t" \
  "v_mfma_f32_16x16x32_bf16 %[acc1], v[52:55], v[0:3], %[acc1]\n\t" \
  "ds_read_b64_tr_b16 v[68:69], v87 offset:24576\n\t" \
  "ds_read_b64_tr_b16 v[70:71], v87 offset:25088\n\t" \
  "v_mfma_f32_16x16x32_bf16 %[acc3], v[52:55], v[4:7], %[acc3]\n\t" \
  "v_mfma_f32_16x16x32_bf16 %[e0], v[52:55], v[80:83], %[e0]\n\t" \
  "s_waitcnt lgkmcnt(6)\n\t" \
  "v_mfma_f32_16x16x32_bf16 %[acc0], v[56:59], v[8:11], %[acc0]\n\t" \
  "ds_read_b128 v[24:27], v92 offset:12288\n\t" \
  "ds_read_b64_tr_b16 v[72:73], v86 offset:28672\n\t" \
  "v_mfma_f32_16x16x32_bf16 %[acc2], v[56:59], v[12:15], %[acc2]\n\t" \
  "ds_read_b64_tr_b16 v[74:75], v86 offset:29184\n\t" \
  "ds_read_b128 v[28:31], v93 offset:12288\n\t" \
  "v_mfma_f32_16x16x32_bf16 %[acc1], v[60:63], v[8:11], %[acc1]\n\t" \
  "ds_read_b64_tr_b16 v[76:77], v87 offset:28672\n\t" \
  "ds_read_b64_tr_b16 v[78:79], v87 offset:29184\n\t" \
  "v_mfma_f32_16x16x32_bf16 %[acc3], v[60:63], v[12:15], %[acc3]\n\t" \
  "v_mfma_f32_16x16x32_bf16 %[e0], v[60:63], v[80:83], %[e0]\n\t" \
  "s_waitcnt lgkmcnt(6)\n\t" \
  "v_mfma_f32_16x16x32_bf16 %[acc0], v[64:67], v[16:19], %[acc0]\n\t" \
  "ds_read_b128 v[32:35], v88 offset:24576\n\t" \
  "ds_read_b64_tr_b16 v[48:49], v86 offset:32768\n\t" \
  "v_mfma_f32_16x16x32_bf16 %[acc2], v[64:67], v[20:23], %[acc2]\n\t" \
  "ds_read_b64_tr_b16 v[50:51], v86 offset:33280\n\t" \
  "ds_read_b128 v[36:39], v89 offset:24576\n\t" \
  "v_mfma_f32_16x16x32_bf16 %[acc1], v[68:71], v[16:19], %[acc1]\n\t" \
  "ds_read_b64_tr_b16 v[52:53], v87 offset:32768\n\t" \
  "ds_read_b64_tr_b16 v[54:55], v87 offset:33280\n\t" \
  "v_mfma_f32_16x16x32_bf16 %[acc3], v[68:71], v[20:23], %[acc3]\n\t" \
  "v_mfma_f32_16x16x32_bf16 %[e0], v[68:71], v[80:83], %[e0]\n\t" \
  "s_waitcnt lgkmcnt(6)\n\t" \
  "v_mfma_f32_16x16x32_bf16 %[acc0], v[72:75], v[24:27], %[acc0]\n\t" \
  "ds_read_b128 v[40:43], v92 offset:24576\n\t" \
  "ds_read_b64_tr_b16 v[56:57], v86 offset:36864\n\t" \
  "v_mfma_f32_16x16x32_bf16 %[acc2], v[72:75], v[28:31], %[acc2]\n\t" \
  "ds_read_b64_tr_b16 v[58:59], v86 offset:37376\n\t" \
  "ds_read_b128 v[44:47], v93 offset:24576\n\t" \
  "v_mfma_f32_16x16x32_bf16 %[acc1], v[76:79], v[24:27], %[acc1]\n\t" \
  "ds_read_b64_tr_b16 v[60:61], v87 offset:36864\n\t" \
  "ds_read_b64_tr_b16 v[62:63], v87 offset:37376\n\t" \
  "v_mfma_f32_16x16x32_bf16 %[acc3], v[76:79], v[28:31], %[acc3]\n\t" \
  "v_mfma_f32_16x16x32_bf16 %[e0], v[76:79], v[80:83], %[e0]\n\t" \
  "s_waitcnt lgkmcnt(6)\n\t" \
  "v_mfma_f32_16x16x32_bf16 %[acc0], v[48:51], v[32:35], %[acc0]\n\t" \
  "v_mfma_f32_16x16x32_bf16 %[acc2], v[48:51], v[36:39], %[acc2]\n\t" \
  "v_mfma_f32_16x16x32_bf16 %[acc1], v[52:55], v[32:35], %[acc1]\n\t" \
  "v_mfma_f32_16x16x32_bf16 %[acc3], v[52:55], v[36:39], %[acc3]\n\t" \
  "v_mfma_f32_16x16x32_bf16 %[e0], v[52:55], v[80:83], %[e0]\n\t" \
  "s_waitcnt lgkmcnt(0)\n\t" \
  "v_mfma_f32_16x16x32_bf16 %[acc0], v[56:59], v[40:43], %[acc0]\n\t" \
  "v_mfma_f32_16x16x32_bf16 %[acc2], v[56:59], v[44:47], %[acc2]\n\t" \
  "v_mfma_f32_16x16x32_bf16 %[acc1], v[60:63], v[40:43], %[acc1]\n\t" \
  "v_mfma_f32_16x16x32_bf16 %[acc3], v[60:63], v[44:47], %[acc3]\n\t" \
  "v_mfma_f32_16x16x32_bf16 %[e0], v[60:63], v[80:83], %[e0]\n\t" \
  "s_waitcnt vmcnt(0)\n\t" \
  "s_nop 6\n\t" \
  ""
#define P0_2 \
  "s_load_dword %[tag], %[ws], 0x0\n\t" \
  "v_xor_b32 v85, 32, %[aw]\n\t" \
  "v_add_u32 v86, 32768, %[aw]\n\t" \
  "v_add_u32 v87, 32768, v85\n\t" \
  "v_mov_b32 v84, %[aw]\n\t" \
  "v_mov_b32 v80, 0x3f803f80\n\t" \
  "v_mov_b32 v81, 0x3f803f80\n\t" \
  "v_mov_b32 v82, 0x3f803f80\n\t" \
  "v_mov_b32 v83, 0x3f803f80\n\t" \
  "v_xor_b32 v88, 0, %[ax]\n\t" \
  "v_xor_b32 v89, 2048, %[ax]\n\t" \
  "v_xor_b32 v90, 0, %[ax2]\n\t" \
  "v_xor_b32 v91, 2048, %[ax2]\n\t" \
  "v_xor_b32 v92, 64, %[ax]\n\t" \
  "v_xor_b32 v93, 2112, %[ax]\n\t" \
  "v_xor_b32 v94, 64, %[ax2]\n\t" \
  "v_xor_b32 v95, 2112, %[ax2]\n\t" \
  "s_waitcnt vmcnt(0) lgkmcnt(0)\n\t" \
  "s_add_u32 %[tag], %[tag], 1\n\t" \
  "s_barrier\n\t" \
  "ds_read_b128 v[0:3], v88 offset:0\n\t" \
  "ds_read_b64_tr_b16 v[48:49], v84 offset:0\n\t" \
  "ds_read_b64_tr_b16 v[50:51], v84 offset:512\n\t" \
  "ds_read_b128 v[4:7], v89 offset:0\n\t" \
  "ds_read_b64_tr_b16 v[52:53], v85 offset:0\n\t" \
  "ds_read_b64_tr_b16 v[54:55], v85 offset:512\n\t" \
  "ds_read_b128 v[8:11], v90 offset:0\n\t" \
  "ds_read_b128 v[12:15], v91 offset:0\n\t" \
  "ds_read_b128 v[16:19], v92 offset:0\n\t" \
  "ds_read_b64_tr_b16 v[56:57], v84 offset:4096\n\t" \
  "s_waitcnt lgkmcnt(4)\n\t" \
  "v_mfma_f32_16x16x32_bf16 %[acc0], v[48:51], v[0:3], 0\n\t" \
  "ds_read_b64_tr_b16 v[58:59], v84 offset:4608\n\t" \
  "v_mfma_f32_16x16x32_bf16 %[acc2], v[48:51], v[4:7], 0\n\t" \
  "ds_read_b128 v[20:23], v93 offset:0\n\t" \
  "v_mfma_f32_16x16x32_bf16 %[e0], v[80:83], v[0:3], 0\n\t" \
  "ds_read_b64_tr_b16 v[60:61], v85 offset:4096\n\t" \
  "v_mfma_f32_16x16x32_bf16 %[e1], v[0:3], v[0:3], 0\n\t" \
  "ds_read_b64_tr_b16 v[62:63], v85 offset:4608\n\t" \
  "v_mfma_f32_16x16x32_bf16 %[acc1], v[52:55], v[0:3], 0\n\t" \
  "ds_read_b128 v[24:27], v94 offset:0\n\t" \
  "v_mfma_f32_16x16x32_bf16 %[acc3], v[52:55], v[4:7], 0\n\t" \
  "ds_read_b128 v[28:31], v95 offset:0\n\t" \
  "s_waitcnt lgkmcnt(5)\n\t" \
  "v_mfma_f32_16x16x32_bf16 %[e2], v[80:83], v[8:11], 0\n\t" \
  "ds_read_b128 v[32:35], v88 offset:12288\n\t" \
  "v_mfma_f32_16x16x32_bf16 %[e3], v[8:11], v[8:11], 0\n\t" \
  "v_mfma_f32_16x16x32_bf16 %[e4], v[80:83], v[12:15], 0\n\t" \
  "ds_read_b64_tr_b16 v[64:65], v84 offset:8192\n\t" \
  "v_mfma_f32_16x16x32_bf16 %[e5], v[12:15], v[12:15], 0\n\t" \
  "v_mfma_f32_16x16x32_bf16 %[acc0], v[56:59], v[16:19], %[acc0]\n\t" \
  "ds_read_b64_tr_b16 v[66:67], v84 offset:8704\n\t" \
  "s_waitcnt lgkmcnt(4)\n\t" \
  "v_mfma_f32_16x16x32_bf16 %[acc2], v[56:59], v[20:23], %[acc2]\n\t" \
  "ds_read_b128 v[36:39], v89 offset:12288\n\t" \
  "v_mfma_f32_16x16x32_bf16 %[e0], v[80:83], v[16:19], %[e0]\n\t" \
  "ds_read_b64_tr_b16 v[68:69], v85 offset:8192\n\t" \
  "v_mfma_f32_16x16x32_bf16 %[e1], v[16:19], v[16:19], %[e1]\n\t" \
  "ds_read_b64_tr_b16 v[70:71], v85 offset:8704\n\t" \
  "v_mfma_f32_16x16x32_bf16 %[acc1], v[60:63], v[16:19], %[acc1]\n\t" \
  "ds_read_b128 v[40:43], v90 offset:12288\n\t" \
  "v_mfma_f32_16x16x32_bf16 %[acc3], v[60:63], v[20:23], %[acc3]\n\t" \
  "ds_read_b128 v[44:47], v91 offset:12288\n\t" \
  "v_mfma_f32_16x16x32_bf16 %[e2], v[80:83], v[24:27], %[e2]\n\t" \
  "ds_read_b128 v[0:3], v92 offset:12288\n\t" \
  "v_mfma_f32_16x16x32_bf16 %[e3], v[24:27], v[24:27], %[e3]\n\t" \
  "s_waitcnt lgkmcnt(5)\n\t" \
  "v_mfma_f32_16x16x32_bf16 %[e4], v[80:83], v[28:31], %[e4]\n\t" \
  "ds_read_b64_tr_b16 v[72:73], v84 offset:12288\n\t" \
  "v_mfma_f32_16x16x32_bf16 %[e5], v[28:31], v[28:31], %[e5]\n\t" \
  "v_mfma_f32_16x16x32_bf16 %[acc0], v[64:67], v[32:35], %[acc0]\n\t" \
  "ds_read_b64_tr_b16 v[74:75], v84 offset:12800\n\t" \
  "v_mfma_f32_16x16x32_bf16 %[acc2], v[64:67], v[36:39], %[acc2]\n\t" \
  "ds_read_b128 v[4:7], v93 offset:12288\n\t" \
  "v_mfma_f32_16x16x32_bf16 %[e0], v[80:83], v[32:35], %[e0]\n\t" \
  "ds_read_b64_tr_b16 v[76:77], v85 offset:12288\n\t" \
  "v_mfma_f32_16x16x32_bf16 %[e1], v[32:35], v[32:35], %[e1]\n\t" \
  "ds_read_b64_tr_b16 v[78:79], v85 offset:12800\n\t" \
  "s_waitcnt lgkmcnt(6)\n\t" \
  "v_mfma_f32_16x16x32_bf16 %[acc1], v[68:71], v[32:35], %[acc1]\n\t" \
  "ds_read_b128 v[8:11], v94 offset:12288\n\t" \
  "v_mfma_f32_16x16x32_bf16 %[acc3], v[68:71], v[36:39], %[acc3]\n\t" \
  "ds_read_b128 v[12:15], v95 offset:12288\n\t" \
  "v_mfma_f32_16x16x32_bf16 %[e2], v[80:83], v[40:43], %[e2]\n\t" \
  "ds_read_b128 v[16:19], v88 offset:24576\n\t" \
  "v_mfma_f32_16x16x32_bf16 %[e3], v[40:43], v[40:43], %[e3]\n\t" \
  "v_mfma_f32_16x16x32_bf16 %[e4], v[80:83], v[44:47], %[e4]\n\t" \
  "ds_read_b64_tr_b16 v[48:49], v84 offset:16384\n\t" \
  "v_mfma_f32_16x16x32_bf16 %[e5], v[44:47], v[44:47], %[e5]\n\t" \
  "s_waitcnt lgkmcnt(4)\n\t" \
  "v_mfma_f32_16x16x32_bf16 %[acc0], v[72:75], v[0:3], %[acc0]\n\t" \
  "ds_read_b64_tr_b16 v[50:51], v84 offset:16896\n\t" \
  "v_mfma_f32_16x16x32_bf16 %[acc2], v[72:75], v[4:7], %[acc2]\n\t" \
  "ds_read_b128 v[20:23], v89 offset:24576\n\t" \
  "v_mfma_f32_16x16x32_bf16 %[e0], v[80:83], v[0:3], %[e0]\n\t" \
  "ds_read_b64_tr_b16 v[52:53], v85 offset:16384\n\t" \
  "v_mfma_f32_16x16x32_bf16 %[e1], v[0:3], v[0:3], %[e1]\n\t" \
  "ds_read_b64_tr_b16 v[54:55], v85 offset:16896\n\t" \
  "v_mfma_f32_16x16x32_bf16 %[acc1], v[76:79], v[0:3], %[acc1]\n\t" \
  "ds_read_b128 v[24:27], v90 offset:24576\n\t" \
  "v_mfma_f32_16x16x32_bf16 %[acc3], v[76:79], v[4:7], %[acc3]\n\t" \
  "ds_read_b128 v[28:31], v91 offset:24576\n\t" \
  "s_waitcnt lgkmcnt(5)\n\t" \
  "v_mfma_f32_16x16x32_bf16 %[e2], v[80:83], v[8:11], %[e2]\n\t" \
  "ds_read_b128 v[32:35], v92 offset:24576\n\t" \
  "v_mfma_f32_16x16x32_bf16 %[e3], v[8:11], v[8:11], %[e3]\n\t" \
  "v_mfma_f32_16x16x32_bf16 %[e4], v[80:83], v[12:15], %[e4]\n\t" \
  "ds_read_b64_tr_b16 v[56:57], v84 offset:20480\n\t" \
  "v_mfma_f32_16x16x32_bf16 %[e5], v[12:15], v[12:15], %[e5]\n\t" \
  "v_mfma_f32_16x16x32_bf16 %[acc0], v[48:51], v[16:19], %[acc0]\n\t" \
  "ds_read_b64_tr_b16 v[58:59], v84 offset:20992\n\t" \
  "s_waitcnt lgkmcnt(4)\n\t" \
  "v_mfma_f32_16x16x32_bf16 %[acc2], v[48:51], v[20:23], %[acc2]\n\t" \
  "ds_read_b128 v[36:39], v93 offset:24576\n\t" \
  "v_mfma_f32_16x16x32_bf16 %[e0], v[80:83], v[16:19], %[e0]\n\t" \
  "ds_read_b64_tr_b16 v[60:61], v85 offset:20480\n\t" \
  "v_mfma_f32_16x16x32_bf16 %[e1], v[16:19], v[16:19], %[e1]\n\t" \
  "ds_read_b64_tr_b16 v[62:63], v85 offset:20992\n\t" \
  "v_mfma_f32_16x16x32_bf16 %[acc1], v[52:55], v[16:19], %[acc1]\n\t" \
  "ds_read_b128 v[40:43], v94 offset:24576\n\t" \
  "v_mfma_f32_16x16x32_bf16 %[acc3], v[52:55], v[20:23], %[acc3]\n\t" \
  "ds_read_b128 v[44:47], v95 offset:24576\n\t" \
  "v_mfma_f32_16x16x32_bf16 %[e2], v[80:83], v[24:27], %[e2]\n\t" \
  "v_mfma_f32_16x16x32_bf16 %[e3], v[24:27], v[24:27], %[e3]\n\t" \
  "s_waitcnt lgkmcnt(4)\n\t" \
  "v_mfma_f32_16x16x32_bf16 %[e4], v[80:83], v[28:31], %[e4]\n\t" \
  "v_mfma_f32_16x16x32_bf16 %[e5], v[28:31], v[28:31], %[e5]\n\t" \
  "v_mfma_f32_16x16x32_bf16 %[acc0], v[56:59], v[32:35], %[acc0]\n\t" \
  "v_mfma_f32_16x16x32_bf16 %[acc2], v[56:59], v[36:39], %[acc2]\n\t" \
  "v_mfma_f32_16x16x32_bf16 %[e0], v[80:83], v[32:35], %[e0]\n\t" \
  "v_mfma_f32_16x16x32_bf16 %[e1], v[32:35], v[32:35], %[e1]\n\t" \
  "s_waitcnt lgkmcnt(0)\n\t" \
  "v_mfma_f32_16x16x32_bf16 %[acc1], v[60:63], v[32:35], %[acc1]\n\t" \
  "v_mfma_f32_16x16x32_bf16 %[acc3], v[60:63], v[36:39], %[acc3]\n\t" \
  "v_mfma_f32_16x16x32_bf16 %[e2], v[80:83], v[40:43], %[e2]\n\t" \
  "v_mfma_f32_16x16x32_bf16 %[e3], v[40:43], v[40:43], %[e3]\n\t" \
  "v_mfma_f32_16x16x32_bf16 %[e4], v[80:83], v[44:47], %[e4]\n\t" \
  "v_mfma_f32_16x16x32_bf16 %[e5], v[44:47], v[44:47], %[e5]\n\t" \
  "s_barrier\n\t" \
  "global_load_dwordx2 %[skw], %[sko], %[sk]\n\t" \
  "ds_read_b128 v[0:3], v88 offset:36864\n\t" \
  "ds_read_b64_tr_b16 v[64:65], v84 offset:24576\n\t" \
  "ds_read_b64_tr_b16 v[66:67], v84 offset:25088\n\t" \
  "ds_read_b128 v[4:7], v89 offset:36864\n\t" \
  "ds_read_b64_tr_b16 v[68:69], v85 offset:24576\n\t" \
  "ds_read_b64_tr_b16 v[70:71], v85 offset:25088\n\t" \
  "ds_read_b128 v[8:11], v90 offset:36864\n\t" \
  "ds_read_b128 v[12:15], v91 offset:36864\n\t" \
  "ds_read_b128 v[16:19], v92 offset:36864\n\t" \
  "ds_read_b64_tr_b16 v[72:73], v84 offset:28672\n\t" \
  "s_waitcnt lgkmcnt(4)\n\t" \
  "v_mfma_f32_16x16x32_bf16 %[acc0], v[64:67], v[0:3], %[acc0]\n\t" \
  "ds_read_b64_tr_b16 v[74:75], v84 offset:29184\n\t" \
  "v_mfma_f32_16x16x32_bf16 %[acc2], v[64:67], v[4:7], %[acc2]\n\t" \
  "ds_read_b128 v[20:23], v93 offset:36864\n\t" \
  "v_mfma_f32_16x16x32_bf16 %[e0], v[80:83], v[0:3], %[e0]\n\t" \
  "ds_read_b64_tr_b16 v[76:77], v85 offset:28672\n\t" \
  "v_mfma_f32_16x16x32_bf16 %[e1], v[0:3], v[0:3], %[e1]\n\t" \
  "ds_read_b64_tr_b16 v[78:79], v85 offset:29184\n\t" \
  "v_mfma_f32_16x16x32_bf16 %[acc1], v[68:71], v[0:3], %[acc1]\n\t" \
  "ds_read_b128 v[24:27], v94 offset:36864\n\t" \
  "v_mfma_f32_16x16x32_bf16 %[acc3], v[68:71], v[4:7], %[acc3]\n\t" \
  "ds_read_b128 v[28:31], v95 offset:36864\n\t" \
  "s_waitcnt lgkmcnt(5)\n\t" \
  "v_mfma_f32_16x16x32_bf16 %[e2], v[80:83], v[8:11], %[e2]\n\t" \
  "ds_read_b128 v[32:35], v88 offset:49152\n\t" \
  "v_mfma_f32_16x16x32_bf16 %[e3], v[8:11], v[8:11], %[e3]\n\t" \
  "v_mfma_f32_16x16x32_bf16 %[e4], v[80:83], v[12:15], %[e4]\n\t" \
  "ds_read_b64_tr_b16 v[48:49], v86 offset:0\n\t" \
  "v_mfma_f32_16x16x32_bf16 %[e5], v[12:15], v[12:15], %[e5]\n\t" \
  "v_mfma_f32_16x16x32_bf16 %[acc0], v[72:75], v[16:19], %[acc0]\n\t" \
  "ds_read_b64_tr_b16 v[50:51], v86 offset:512\n\t" \
  "s_waitcnt lgkmcnt(4)\n\t" \
  "v_mfma_f32_16x16x32_bf16 %[acc2], v[72:75], v[20:23], %[acc2]\n\t" \
  "ds_read_b128 v[36:39], v89 offset:49152\n\t" \
  "v_mfma_f32_16x16x32_bf16 %[e0], v[80:83], v[16:19], %[e0]\n\t" \
  "ds_read_b64_tr_b16 v[52:53], v87 offset:0\n\t" \
  "v_mfma_f32_16x16x32_bf16 %[e1], v[16:19], v[16:19], %[e1]\n\t" \
  "ds_read_b64_tr_b16 v[54:55], v87 offset:512\n\t" \
  "v_mfma_f32_16x16x32_bf16 %[acc1], v[76:79], v[16:19], %[acc1]\n\t" \
  "ds_read_b128 v[40:43], v90 offset:49152\n\t" \
  "v_mfma_f32_16x16x32_bf16 %[acc3], v[76:79], v[20:23], %[acc3]\n\t" \
  "ds_read_b128 v[44:47], v91 offset:49152\n\t" \
  "v_mfma_f32_16x16x32_bf16 %[e2], v[80:83], v[24:27], %[e2]\n\t" \
  "ds_read_b128 v[0:3], v92 offset:49152\n\t" \
  "v_mfma_f32_16x16x32_bf16 %[e3], v[24:27], v[24:27], %[e3]\n\t" \
  "s_waitcnt lgkmcnt(5)\n\t" \
  "v_mfma_f32_16x16x32_bf16 %[e4], v[80:83], v[28:31], %[e4]\n\t" \
  "ds_read_b64_tr_b16 v[56:57], v86 offset:4096\n\t" \
  "v_mfma_f32_16x16x32_bf16 %[e5], v[28:31], v[28:31], %[e5]\n\t" \
  "v_mfma_f32_16x16x32_bf16 %[acc0], v[48:51], v[32:35], %[acc0]\n\t" \
  "ds_read_b64_tr_b16 v[58:59], v86 offset:4608\n\t" \
  "v_mfma_f32_16x16x32_bf16 %[acc2], v[48:51], v[36:39], %[acc2]\n\t" \
  "ds_read_b128 v[4:7], v93 offset:49152\n\t" \
  "v_mfma_f32_16x16x32_bf16 %[e0], v[80:83], v[32:35], %[e0]\n\t" \
  "ds_read_b64_tr_b16 v[60:61], v87 offset:4096\n\t" \
  "v_mfma_f32_16x16x32_bf16 %[e1], v[32:35], v[32:35], %[e1]\n\t" \
  "ds_read_b64_tr_b16 v[62:63], v87 offset:4608\n\t" \
  "s_waitcnt lgkmcnt(6)\n\t" \
  "v_mfma_f32_16x16x32_bf16 %[acc1], v[52:55], v[32:35], %[acc1]\n\t" \
  "ds_read_b128 v[8:11], v94 offset:49152\n\t" \
  "v_mfma_f32_16x16x32_bf16 %[acc3], v[52:55], v[36:39], %[acc3]\n\t" \
  "ds_read_b128 v[12:15], v95 offset:49152\n\t" \
  "v_mfma_f32_16x16x32_bf16 %[e2], v[80:83], v[40:43], %[e2]\n\t" \
  "ds_read_b128 v[16:19], v88 offset:61440\n\t" \
  "v_mfma_f32_16x16x32_bf16 %[e3], v[40:43], v[40:43], %[e3]\n\t" \
  "v_mfma_f32_16x16x32_bf16 %[e4], v[80:83], v[44:47], %[e4]\n\t" \
  "ds_read_b64_tr_b16 v[64:65], v86 offset:8192\n\t" \
  "v_mfma_f32_16x16x32_bf16 %[e5], v[44:47], v[44:47], %[e5]\n\t" \
  "s_waitcnt lgkmcnt(4)\n\t" \
  "v_mfma_f32_16x16x32_bf16 %[acc0], v[56:59], v[0:3], %[acc0]\n\t" \
  "ds_read_b64_tr_b16 v[66:67], v86 offset:8704\n\t" \
  "v_mfma_f32_16x16x32_bf16 %[acc2], v[56:59], v[4:7], %[acc2]\n\t" \
  "ds_read_b128 v[20:23], v89 offset:61440\n\t" \
  "v_mfma_f32_16x16x32_bf16 %[e0], v[80:83], v[0:3], %[e0]\n\t" \
  "ds_read_b64_tr_b16 v[68:69], v87 offset:8192\n\t" \
  "v_mfma_f32_16x16x32_bf16 %[e1], v[0:3], v[0:3], %[e1]\n\t" \
  "ds_read_b64_tr_b16 v[70:71], v87 offset:8704\n\t" \
  "v_mfma_f32_16x16x32_bf16 %[acc1], v[60:63], v[0:3], %[acc1]\n\t" \
  "ds_read_b128 v[24:27], v90 offset:61440\n\t" \
  "v_mfma_f32_16x16x32_bf16 %[acc3], v[60:63], v[4:7], %[acc3]\n\t" \
  "ds_read_b128 v[28:31], v91 offset:61440\n\t" \
  "s_waitcnt lgkmcnt(5)\n\t" \
  "v_mfma_f32_16x16x32_bf16 %[e2], v[80:83], v[8:11], %[e2]\n\t" \
  "ds_read_b128 v[32:35], v92 offset:61440\n\t" \
  "v_mfma_f32_16x16x32_bf16 %[e3], v[8:11], v[8:11], %[e3]\n\t" \
  "v_mfma_f32_16x16x32_bf16 %[e4], v[80:83], v[12:15], %[e4]\n\t" \
  "ds_read_b64_tr_b16 v[72:73], v86 offset:12288\n\t" \
  "v_mfma_f32_16x16x32_bf16 %[e5], v[12:15], v[12:15], %[e5]\n\t" \
  "v_mfma_f32_16x16x32_bf16 %[acc0], v[64:67], v[16:19], %[acc0]\n\t" \
  "ds_read_b64_tr_b16 v[74:75], v86 offset:12800\n\t" \
  "s_waitcnt lgkmcnt(4)\n\t" \
  "v_mfma_f32_16x16x32_bf16 %[acc2], v[64:67], v[20:23], %[acc2]\n\t" \
  "ds_read_b128 v[36:39], v93 offset:61440\n\t" \
  "v_mfma_f32_16x16x32_bf16 %[e0], v[80:83], v[16:19], %[e0]\n\t" \
  "ds_read_b64_tr_b16 v[76:77], v87 offset:12288\n\t" \
  "v_mfma_f32_16x16x32_bf16 %[e1], v[16:19], v[16:19], %[e1]\n\t" \
  "ds_read_b64_tr_b16 v[78:79], v87 offset:12800\n\t" \
  "v_mfma_f32_16x16x32_bf16 %[acc1], v[68:71], v[16:19], %[acc1]\n\t" \
  "ds_read_b128 v[40:43], v94 offset:61440\n\t" \
  "v_mfma_f32_16x16x32_bf16 %[acc3], v[68:71], v[20:23], %[acc3]\n\t" \
  "ds_read_b128 v[44:47], v95 offset:61440\n\t" \
  "v_mfma_f32_16x16x32_bf16 %[e2], v[80:83], v[24:27], %[e2]\n\t" \
  "v_mfma_f32_16x16x32_bf16 %[e3], v[24:27], v[24:27], %[e3]\n\t" \
  "s_waitcnt lgkmcnt(4)\n\t" \
  "v_mfma_f32_16x16x32_bf16 %[e4], v[80:83], v[28:31], %[e4]\n\t" \
  "v_mfma_f32_16x16x32_bf16 %[e5], v[28:31], v[28:31], %[e5]\n\t" \
  "v_mfma_f32_16x16x32_bf16 %[acc0], v[72:75], v[32:35], %[acc0]\n\t" \
  "v_mfma_f32_16x16x32_bf16 %[acc2], v[72:75], v[36:39], %[acc2]\n\t" \
  "v_mfma_f32_16x16x32_bf16 %[e0], v[80:83], v[32:35], %[e0]\n\t" \
  "v_mfma_f32_16x16x32_bf16 %[e1], v[32:35], v[32:35], %[e1]\n\t" \
  "s_waitcnt lgkmcnt(0)\n\t" \
  "v_mfma_f32_16x16x32_bf16 %[acc1], v[76:79], v[32:35], %[acc1]\n\t" \
  "v_mfma_f32_16x16x32_bf16 %[acc3], v[76:79], v[36:39], %[acc3]\n\t" \
  "v_mfma_f32_16x16x32_bf16 %[e2], v[80:83], v[40:43], %[e2]\n\t" \
  "v_mfma_f32_16x16x32_bf16 %[e3], v[40:43], v[40:43], %[e3]\n\t" \
  "v_mfma_f32_16x16x32_bf16 %[e4], v[80:83], v[44:47], %[e4]\n\t" \
  "v_mfma_f32_16x16x32_bf16 %[e5], v[44:47], v[44:47], %[e5]\n\t" \
  "s_barrier\n\t" \
  "ds_read_b128 v[0:3], v88 offset:0\n\t" \
  "ds_read_b64_tr_b16 v[48:49], v86 offset:16384\n\t" \
  "ds_read_b64_tr_b16 v[50:51], v86 offset:16896\n\t" \
  "ds_read_b128 v[4:7], v89 offset:0\n\t" \
  "ds_read_b64_tr_b16 v[52:53], v87 offset:16384\n\t" \
  "ds_read_b64_tr_b16 v[54:55], v87 offset:16896\n\t" \
  "ds_read_b128 v[8:11], v90 offset:0\n\t" \
  "ds_read_b128 v[12:15], v91 offset:0\n\t" \
  "ds_read_b128 v[16:19], v92 offset:0\n\t" \
  "ds_read_b64_tr_b16 v[56:57], v86 offset:20480\n\t" \
  "s_waitcnt lgkmcnt(4)\n\t" \
  "v_mfma_f32_16x16x32_bf16 %[acc0], v[48:51], v[0:3], %[acc0]\n\t" \
  "ds_read_b64_tr_b16 v[58:59], v86 offset:20992\n\t" \
  "v_mfma_f32_16x16x32_bf16 %[acc2], v[48:51], v[4:7], %[acc2]\n\t" \
  "ds_read_b128 v[20:23], v93 offset:0\n\t" \
  "v_mfma_f32_16x16x32_bf16 %[e0], v[80:83], v[0:3], %[e0]\n\t" \
  "ds_read_b64_tr_b16 v[60:61], v87 offset:20480\n\t" \
  "v_mfma_f32_16x16x32_bf16 %[e1], v[0:3], v[0:3], %[e1]\n\t" \
  "ds_read_b64_tr_b16 v[62:63], v87 offset:20992\n\t" \
  "v_mfma_f32_16x16x32_bf16 %[acc1], v[52:55], v[0:3], %[acc1]\n\t" \
  "ds_read_b128 v[24:27], v94 offset:0\n\t" \
  "v_mfma_f32_16x16x32_bf16 %[acc3], v[52:55], v[4:7], %[acc3]\n\t" \
  "ds_read_b128 v[28:31], v95 offset:0\n\t" \
  "s_waitcnt lgkmcnt(5)\n\t" \
  "v_mfma_f32_16x16x32_bf16 %[e2], v[80:83], v[8:11], %[e2]\n\t" \
  "ds_read_b128 v[32:35], v88 offset:12288\n\t" \
  "v_mfma_f32_16x16x32_bf16 %[e3], v[8:11], v[8:11], %[e3]\n\t" \
  "v_mfma_f32_16x16x32_bf16 %[e4], v[80:83], v[12:15], %[e4]\n\t" \
  "ds_read_b64_tr_b16 v[64:65], v86 offset:24576\n\t" \
  "v_mfma_f32_16x16x32_bf16 %[e5], v[12:15], v[12:15], %[e5]\n\t" \
  "v_mfma_f32_16x16x32_bf16 %[acc0], v[56:59], v[16:19], %[acc0]\n\t" \
  "ds_read_b64_tr_b16 v[66:67], v86 offset:25088\n\t" \
  "s_waitcnt lgkmcnt(4)\n\t" \
  "v_mfma_f32_16x16x32_bf16 %[acc2], v[56:59], v[20:23], %[acc2]\n\t" \
  "ds_read_b128 v[36:39], v89 offset:12288\n\t" \
  "v_mfma_f32_16x16x32_bf16 %[e0], v[80:83], v[16:19], %[e0]\n\t" \
  "ds_read_b64_tr_b16 v[68:69], v87 offset:24576\n\t" \
  "v_mfma_f32_16x16x32_bf16 %[e1], v[16:19], v[16:19], %[e1]\n\t" \
  "ds_read_b64_tr_b16 v[70:71], v87 offset:25088\n\t" \
  "v_mfma_f32_16x16x32_bf16 %[acc1], v[60:63], v[16:19], %[acc1]\n\t" \
  "ds_read_b128 v[40:43], v90 offset:12288\n\t" \
  "v_mfma_f32_16x16x32_bf16 %[acc3], v[60:63], v[20:23], %[acc3]\n\t" \
  "ds_read_b128 v[44:47], v91 offset:12288\n\t" \
  "v_mfma_f32_16x16x32_bf16 %[e2], v[80:83], v[24:27], %[e2]\n\t" \
  "ds_read_b128 v[0:3], v92 offset:12288\n\t" \
  "v_mfma_f32_16x16x32_bf16 %[e3], v[24:27], v[24:27], %[e3]\n\t" \
  "s_waitcnt lgkmcnt(5)\n\t" \
  "v_mfma_f32_16x16x32_bf16 %[e4], v[80:83], v[28:31], %[e4]\n\t" \
  "ds_read_b64_tr_b16 v[72:73], v86 offset:28672\n\t" \
  "v_mfma_f32_16x16x32_bf16 %[e5], v[28:31], v[28:31], %[e5]\n\t" \
  "v_mfma_f32_16x16x32_bf16 %[acc0], v[64:67], v[32:35], %[acc0]\n\t" \
  "ds_read_b64_tr_b16 v[74:75], v86 offset:29184\n\t" \
  "v_mfma_f32_16x16x32_bf16 %[acc2], v[64:67], v[36:39], %[acc2]\n\t" \
  "ds_read_b128 v[4:7], v93 offset:12288\n\t" \
  "v_mfma_f32_16x16x32_bf16 %[e0], v[80:83], v[32:35], %[e0]\n\t" \
  "ds_read_b64_tr_b16 v[76:77], v87 offset:28672\n\t" \
  "v_mfma_f32_16x16x32_bf16 %[e1], v[32:35], v[32:35], %[e1]\n\t" \
  "ds_read_b64_tr_b16 v[78:79], v87 offset:29184\n\t" \
  "s_waitcnt lgkmcnt(6)\n\t" \
  "v_mfma_f32_16x16x32_bf16 %[acc1], v[68:71], v[32:35], %[acc1]\n\t" \
  "ds_read_b128 v[8:11], v94 offset:12288\n\t" \
  "v_mfma_f32_16x16x32_bf16 %[acc3], v[68:71], v[36:39], %[acc3]\n\t" \
  "ds_read_b128 v[12:15], v95 offset:12288\n\t" \
  "v_mfma_f32_16x16x32_bf16 %[e2], v[80:83], v[40:43], %[e2]\n\t" \
  "ds_read_b128 v[16:19], v88 offset:24576\n\t" \
  "v_mfma_f32_16x16x32_bf16 %[e3], v[40:43], v[40:43], %[e3]\n\t" \
  "v_mfma_f32_16x16x32_bf16 %[e4], v[80:83], v[44:47], %[e4]\n\t" \
  "ds_read_b64_tr_b16 v[48:49], v86 offset:32768\n\t" \
  "v_mfma_f32_16x16x32_bf16 %[e5], v[44:47], v[44:47], %[e5]\n\t" \
  "s_waitcnt lgkmcnt(4)\n\t" \
  "v_mfma_f32_16x16x32_bf16 %[acc0], v[72:75], v[0:3], %[acc0]\n\t" \
  "ds_read_b64_tr_b16 v[50:51], v86 offset:33280\n\t" \
  "v_mfma_f32_16x16x32_bf16 %[acc2], v[72:75], v[4:7], %[acc2]\n\t" \
  "ds_read_b128 v[20:23], v89 offset:24576\n\t" \
  "v_mfma_f32_16x16x32_bf16 %[e0], v[80:83], v[0:3], %[e0]\n\t" \
  "ds_read_b64_tr_b16 v[52:53], v87 offset:32768\n\t" \
  "v_mfma_f32_16x16x32_bf16 %[e1], v[0:3], v[0:3], %[e1]\n\t" \
  "ds_read_b64_tr_b16 v[54:55], v87 offset:33280\n\t" \
  "v_mfma_f32_16x16x32_bf16 %[acc1], v[76:79], v[0:3], %[acc1]\n\t" \
  "ds_read_b128 v[24:27], v90 offset:24576\n\t" \
  "v_mfma_f32_16x16x32_bf16 %[acc3], v[76:79], v[4:7], %[acc3]\n\t" \
  "ds_read_b128 v[28:31], v91 offset:24576\n\t" \
  "s_waitcnt lgkmcnt(5)\n\t" \
  "v_mfma_f32_16x16x32_bf16 %[e2], v[80:83], v[8:11], %[e2]\n\t" \
  "ds_read_b128 v[32:35], v92 offset:24576\n\t" \
  "v_mfma_f32_16x16x32_bf16 %[e3], v[8:11], v[8:11], %[e3]\n\t" \
  "v_mfma_f32_16x16x32_bf16 %[e4], v[80:83], v[12:15], %[e4]\n\t" \
  "ds_read_b64_tr_b16 v[56:57], v86 offset:36864\n\t" \
  "v_mfma_f32_16x16x32_bf16 %[e5], v[12:15], v[12:15], %[e5]\n\t" \
  "v_mfma_f32_16x16x32_bf16 %[acc0], v[48:51], v[16:19], %[acc0]\n\t" \
  "ds_read_b64_tr_b16 v[58:59], v86 offset:37376\n\t" \
  "s_waitcnt lgkmcnt(4)\n\t" \
  "v_mfma_f32_16x16x32_bf16 %[acc2], v[48:51], v[20:23], %[acc2]\n\t" \
  "ds_read_b128 v[36:39], v93 offset:24576\n\t" \
  "v_mfma_f32_16x16x32_bf16 %[e0], v[80:83], v[16:19], %[e0]\n\t" \
  "ds_read_b64_tr_b16 v[60:61], v87 offset:36864\n\t" \
  "v_mfma_f32_16x16x32_bf16 %[e1], v[16:19], v[16:19], %[e1]\n\t" \
  "ds_read_b64_tr_b16 v[62:63], v87 offset:37376\n\t" \
  "v_mfma_f32_16x16x32_bf16 %[acc1], v[52:55], v[16:19], %[acc1]\n\t" \
  "ds_read_b128 v[40:43], v94 offset:24576\n\t" \
  "v_mfma_f32_16x16x32_bf16 %[acc3], v[52:55], v[20:23], %[acc3]\n\t" \
  "ds_read_b128 v[44:47], v95 offset:24576\n\t" \
  "v_mfma_f32_16x16x32_bf16 %[e2], v[80:83], v[24:27], %[e2]\n\t" \
  "v_mfma_f32_16x16x32_bf16 %[e3], v[24:27], v[24:27], %[e3]\n\t" \
  "s_waitcnt lgkmcnt(4)\n\t" \
  "v_mfma_f32_16x16x32_bf16 %[e4], v[80:83], v[28:31], %[e4]\n\t" \
  "v_mfma_f32_16x16x32_bf16 %[e5], v[28:31], v[28:31], %[e5]\n\t" \
  "v_mfma_f32_16x16x32_bf16 %[acc0], v[56:59], v[32:35], %[acc0]\n\t" \
  "v_mfma_f32_16x16x32_bf16 %[acc2], v[56:59], v[36:39], %[acc2]\n\t" \
  "v_mfma_f32_16x16x32_bf16 %[e0], v[80:83], v[32:35], %[e0]\n\t" \
  "v_mfma_f32_16x16x32_bf16 %[e1], v[32:35], v[32:35], %[e1]\n\t" \
  "s_waitcnt lgkmcnt(0)\n\t" \
  "v_mfma_f32_16x16x32_bf16 %[acc1], v[60:63], v[32:35], %[acc1]\n\t" \
  "v_mfma_f32_16x16x32_bf16 %[acc3], v[60:63], v[36:39], %[acc3]\n\t" \
  "v_mfma_f32_16x16x32_bf16 %[e2], v[80:83], v[40:43], %[e2]\n\t" \
  "v_mfma_f32_16x16x32_bf16 %[e3], v[40:43], v[40:43], %[e3]\n\t" \
  "v_mfma_f32_16x16x32_bf16 %[e4], v[80:83], v[44:47], %[e4]\n\t" \
  "v_mfma_f32_16x16x32_bf16 %[e5], v[44:47], v[44:47], %[e5]\n\t" \
  "s_waitcnt vmcnt(0)\n\t" \
  "s_nop 6\n\t" \
  ""
#define P1_A \
  "v_xor_b32 v168, 0, %[aw]\n\t" \
  "v_add_u32 v172, 32768, v168\n\t" \
  "v_xor_b32 v169, 32, %[aw]\n\t" \
  "v_add_u32 v173, 32768, v169\n\t" \
  "v_xor_b32 v170, 64, %[aw]\n\t" \
  "v_add_u32 v174, 32768, v170\n\t" \
  "v_xor_b32 v171, 96, %[aw]\n\t" \
  "v_add_u32 v175, 32768, v171\n\t" \
  "s_mov_b32 s41, 0\n\t" \
  "s_waitcnt lgkmcnt(0)\n\t" \
  "s_barrier\n\t" \
  "s_lshl_b32 s40, %[c], 11\n\t" \
  "s_add_u32 s42, %[sh], 18432\n\t" \
  "s_add_u32 s40, s40, %[sh]\n\t" \
  "buffer_load_dword %[mq], %[vmq], %[rs], 0 offen sc1\n\t" \
  "buffer_load_dwordx4 v[0:3], %[vh], %[rs], s40 offen offset:0 sc1\n\t" \
  "buffer_load_dwordx4 v[4:7], %[vh], %[rs], s40 offen offset:1024 sc1\n\t" \
  "s_add_u32 s40, s40, 2048\n\t" \
  "s_cmp_ge_u32 s40, s42\n\t" \
  "s_cselect_b32 s40, %[sh], s40\n\t" \
  "buffer_load_dwordx4 v[8:11], %[vh], %[rs], s40 offen offset:0 sc1\n\t" \
  "buffer_load_dwordx4 v[12:15], %[vh], %[rs], s40 offen offset:1024 sc1\n\t" \
  "s_add_u32 s40, s40, 2048\n\t" \
  "s_cmp_ge_u32 s40, s42\n\t" \
  "s_cselect_b32 s40, %[sh], s40\n\t" \
  "buffer_load_dwordx4 v[16:19], %[vh], %[rs], s40 offen offset:0 sc1\n\t" \
  "buffer_load_dwordx4 v[20:23], %[vh], %[rs], s40 offen offset:1024 sc1\n\t" \
  "s_add_u32 s40, s40, 2048\n\t" \
  "s_cmp_ge_u32 s40, s42\n\t" \
  "s_cselect_b32 s40, %[sh], s40\n\t" \
  "buffer_load_dwordx4 v[24:27], %[vh], %[rs], s40 offen offset:0 sc1\n\t" \
  "buffer_load_dwordx4 v[28:31], %[vh], %[rs], s40 offen offset:1024 sc1\n\t" \
  "s_add_u32 s40, s40, 2048\n\t" \
  "s_cmp_ge_u32 s40, s42\n\t" \
  "s_cselect_b32 s40, %[sh], s40\n\t" \
  "buffer_load_dwordx4 v[32:35], %[vh], %[rs], s40 offen offset:0 sc1\n\t" \
  "buffer_load_dwordx4 v[36:39], %[vh], %[rs], s40 offen offset:1024 sc1\n\t" \
  "s_add_u32 s40, s40, 2048\n\t" \
  "s_cmp_ge_u32 s40, s42\n\t" \
  "s_cselect_b32 s40, %[sh], s40\n\t" \
  "buffer_load_dwordx4 v[40:43], %[vh], %[rs], s40 offen offset:0 sc1\n\t" \
  "buffer_load_dwordx4 v[44:47], %[vh], %[rs], s40 offen offset:1024 sc1\n\t" \
  "s_add_u32 s40, s40, 2048\n\t" \
  "s_cmp_ge_u32 s40, s42\n\t" \
  "s_cselect_b32 s40, %[sh], s40\n\t" \
  "buffer_load_dwordx4 v[48:51], %[vh], %[rs], s40 offen offset:0 sc1\n\t" \
  "buffer_load_dwordx4 v[52:55], %[vh], %[rs], s40 offen offset:1024 sc1\n\t" \
  "s_add_u32 s40, s40, 2048\n\t" \
  "s_cmp_ge_u32 s40, s42\n\t" \
  "s_cselect_b32 s40, %[sh], s40\n\t" \
  "buffer_load_dwordx4 v[56:59], %[vh], %[rs], s40 offen offset:0 sc1\n\t" \
  "buffer_load_dwordx4 v[60:63], %[vh], %[rs], s40 offen offset:1024 sc1\n\t" \
  "s_add_u32 s40, s40, 2048\n\t" \
  "s_cmp_ge_u32 s40, s42\n\t" \
  "s_cselect_b32 s40, %[sh], s40\n\t" \
  "buffer_load_dwordx4 v[64:67], %[vh], %[rs], s40 offen offset:0 sc1\n\t" \
  "buffer_load_dwordx4 v[68:71], %[vh], %[rs], s40 offen offset:1024 sc1\n\t" \
  "L_R_%=:\n\t" \
  "ds_read_b64 v[152:153], %[at] offset:0\n\t" \
  "ds_read_b64 v[154:155], %[at] offset:32\n\t" \
  "ds_read_b64 v[156:157], %[at] offset:64\n\t" \
  "ds_read_b64 v[158:159], %[at] offset:96\n\t" \
  "ds_read_b64_tr_b16 v[72:73], v168 offset:0\n\t" \
  "ds_read_b64_tr_b16 v[74:75], v168 offset:2048\n\t" \
  "ds_read_b64_tr_b16 v[76:77], v169 offset:0\n\t" \
  "ds_read_b64_tr_b16 v[78:79], v169 offset:2048\n\t" \
  "ds_read_b64_tr_b16 v[80:81], v170 offset:0\n\t" \
  "ds_read_b64_tr_b16 v[82:83], v170 offset:2048\n\t" \
  "ds_read_b64_tr_b16 v[84:85], v171 offset:0\n\t" \
  "ds_read_b64_tr_b16 v[86:87], v171 offset:2048\n\t" \
  "ds_read_b64_tr_b16 v[88:89], v168 offset:4096\n\t" \
  "ds_read_b64_tr_b16 v[90:91], v168 offset:6144\n\t" \
  "ds_read_b64_tr_b16 v[92:93], v169 offset:4096\n\t" \
  "s_waitcnt lgkmcnt(7)\n\t" \
  "ds_read_b64_tr_b16 v[94:95], v169 offset:6144\n\t" \
  "ds_read_b64_tr_b16 v[96:97], v170 offset:4096\n\t" \
  "ds_read_b64_tr_b16 v[98:99], v170 offset:6144\n\t" \
  "ds_read_b64_tr_b16 v[100:101], v171 offset:4096\n\t" \
  "ds_read_b64_tr_b16 v[102:103], v171 offset:6144\n\t" \
  "ds_read_b64_tr_b16 v[104:105], v168 offset:8192\n\t" \
  "ds_read_b64_tr_b16 v[106:107], v168 offset:10240\n\t" \
  "ds_read_b64_tr_b16 v[108:109], v169 offset:8192\n\t" \
  "s_waitcnt lgkmcnt(7)\n\t" \
  "ds_read_b64_tr_b16 v[110:111], v169 offset:10240\n\t" \
  "ds_read_b64_tr_b16 v[112:113], v170 offset:8192\n\t" \
  "ds_read_b64_tr_b16 v[114:115], v170 offset:10240\n\t" \
  "ds_read_b64_tr_b16 v[116:117], v171 offset:8192\n\t" \
  "ds_read_b64_tr_b16 v[118:119], v171 offset:10240\n\t" \
  "ds_read_b64_tr_b16 v[120:121], v168 offset:12288\n\t" \
  "ds_read_b64_tr_b16 v[122:123], v168 offset:14336\n\t" \
  "ds_read_b64_tr_b16 v[124:125], v169 offset:12288\n\t" \
  "s_waitcnt lgkmcnt(7)\n\t" \
  "ds_read_b64_tr_b16 v[126:127], v169 offset:14336\n\t" \
  "ds_read_b64_tr_b16 v[128:129], v170 offset:12288\n\t" \
  "ds_read_b64_tr_b16 v[130:131], v170 offset:14336\n\t" \
  "ds_read_b64_tr_b16 v[132:133], v171 offset:12288\n\t" \
  "ds_read_b64_tr_b16 v[134:135], v171 offset:14336\n\t" \
  "ds_read_b64_tr_b16 v[136:137], v168 offset:16384\n\t" \
  "ds_read_b64_tr_b16 v[138:139], v168 offset:18432\n\t" \
  "ds_read_b64_tr_b16 v[140:141], v169 offset:16384\n\t" \
  "s_waitcnt lgkmcnt(7)\n\t" \
  "ds_read_b64_tr_b16 v[142:143], v169 offset:18432\n\t" \
  "ds_read_b64_tr_b16 v[144:145], v170 offset:16384\n\t" \
  "ds_read_b64_tr_b16 v[146:147], v170 offset:18432\n\t" \
  "s_waitcnt vmcnt(17)\n\t" \
  "s_mov_b64 exec, 0xffff\n\t" \
  "buffer_store_dword %[vtag], %[vfu], %[rs], 0 offen sc1\n\t" \
  "s_mov_b64 exec, -1\n\t" \
  "s_nop 3\n\t" \
  "v_mfma_f32_16x16x32_bf16 %[acc0], v[72:75], v[0:3], 0\n\t" \
  "ds_read_b64_tr_b16 v[148:149], v171 offset:16384\n\t" \
  "ds_read_b64_tr_b16 v[150:151], v171 offset:18432\n\t" \
  "v_max3_u32 v176, %[mq], v0, v1\n\t" \
  "v_mfma_f32_16x16x32_bf16 %[acc1], v[76:79], v[0:3], 0\n\t" \
  "ds_read_b64_tr_b16 v[72:73], v168 offset:20480\n\t" \
  "ds_read_b64_tr_b16 v[74:75], v168 offset:22528\n\t" \
  "v_max3_u32 v176, v176, v2, v3\n\t" \
  "v_mfma_f32_16x16x32_bf16 %[acc2], v[80:83], v[0:3], 0\n\t" \
  "ds_read_b64_tr_b16 v[76:77], v169 offset:20480\n\t" \
  "s_waitcnt lgkmcnt(7)\n\t" \
  "ds_read_b64_tr_b16 v[78:79], v169 offset:22528\n\t" \
  "v_mfma_f32_16x16x32_bf16 %[acc3], v[84:87], v[0:3], 0\n\t" \
  "ds_read_b64_tr_b16 v[80:81], v170 offset:20480\n\t" \
  "ds_read_b64_tr_b16 v[82:83], v170 offset:22528\n\t" \
  "v_mfma_f32_16x16x32_bf16 %[s1], v[152:155], v[0:3], 0\n\t" \
  "ds_read_b64 v[160:161], %[at] offset:128\n\t" \
  "ds_read_b64 v[162:163], %[at] offset:160\n\t" \
  "s_waitcnt vmcnt(17)\n\t" \
  "v_mfma_f32_16x16x32_bf16 %[acc0], v[88:91], v[4:7], %[acc0]\n\t" \
  "ds_read_b64_tr_b16 v[84:85], v171 offset:20480\n\t" \
  "ds_read_b64_tr_b16 v[86:87], v171 offset:22528\n\t" \
  "v_max3_u32 v176, v176, v4, v5\n\t" \
  "v_mfma_f32_16x16x32_bf16 %[acc1], v[92:95], v[4:7], %[acc1]\n\t" \
  "ds_read_b64_tr_b16 v[88:89], v168 offset:24576\n\t" \
  "s_waitcnt lgkmcnt(7)\n\t" \
  "ds_read_b64_tr_b16 v[90:91], v168 offset:26624\n\t" \
  "v_max3_u32 v176, v176, v6, v7\n\t" \
  "v_mfma_f32_16x16x32_bf16 %[acc2], v[96:99], v[4:7], %[acc2]\n\t" \
  "ds_read_b64_tr_b16 v[92:93], v169 offset:24576\n\t" \
  "ds_read_b64_tr_b16 v[94:95], v169 offset:26624\n\t" \
  "v_mfma_f32_16x16x32_bf16 %[acc3], v[100:103], v[4:7], %[acc3]\n\t" \
  "ds_read_b64_tr_b16 v[96:97], v170 offset:24576\n\t" \
  "ds_read_b64_tr_b16 v[98:99], v170 offset:26624\n\t" \
  "v_mfma_f32_16x16x32_bf16 %[s1], v[156:159], v[4:7], %[s1]\n\t" \
  "ds_read_b64 v[164:165], %[at] offset:192\n\t" \
  "ds_read_b64 v[166:167], %[at] offset:224\n\t" \
  "s_waitcnt vmcnt(16)\n\t" \
  "v_mfma_f32_16x16x32_bf16 %[acc0], v[104:107], v[8:11], %[acc0]\n\t" \
  "ds_read_b64_tr_b16 v[100:101], v171 offset:24576\n\t" \
  "s_waitcnt lgkmcnt(7)\n\t" \
  "ds_read_b64_tr_b16 v[102:103], v171 offset:26624\n\t" \
  "v_max3_u32 v176, v176, v8, v9\n\t" \
  "v_mfma_f32_16x16x32_bf16 %[acc1], v[108:111], v[8:11], %[acc1]\n\t" \
  "ds_read_b64_tr_b16 v[104:105], v168 offset:28672\n\t" \
  "ds_read_b64_tr_b16 v[106:107], v168 offset:30720\n\t" \
  "v_max3_u32 v176, v176, v10, v11\n\t" \
  "v_mfma_f32_16x16x32_bf16 %[acc2], v[112:115], v[8:11], %[acc2]\n\t" \
  "ds_read_b64_tr_b16 v[108:109], v169 offset:28672\n\t" \
  "ds_read_b64_tr_b16 v[110:111], v169 offset:30720\n\t" \
  "v_cmp_eq_u32 vcc, -1, v176\n\t" \
  "v_mfma_f32_16x16x32_bf16 %[acc3], v[116:119], v[8:11], %[acc3]\n\t" \
  "ds_read_b64_tr_b16 v[112:113], v170 offset:28672\n\t" \
  "ds_read_b64_tr_b16 v[114:115], v170 offset:30720\n\t" \
  "s_cbranch_vccnz L_G_%=\n\t" \
  "v_mfma_f32_16x16x32_bf16 %[s1], v[160:163], v[8:11], %[s1]\n\t" \
  "ds_read_b64 v[152:153], %[at] offset:256\n\t" \
  "s_waitcnt lgkmcnt(7)\n\t" \
  "ds_read_b64 v[154:155], %[at] offset:288\n\t" \
  "s_waitcnt vmcnt(15)\n\t" \
  "v_mfma_f32_16x16x32_bf16 %[acc0], v[120:123], v[12:15], %[acc0]\n\t" \
  "ds_read_b64_tr_b16 v[116:117], v171 offset:28672\n\t" \
  "ds_read_b64_tr_b16 v[118:119], v171 offset:30720\n\t" \
  "v_max3_u32 v176, v176, v12, v13\n\t" \
  "v_mfma_f32_16x16x32_bf16 %[acc1], v[124:127], v[12:15], %[acc1]\n\t" \
  "ds_read_b64_tr_b16 v[120:121], v172 offset:0\n\t" \
  "ds_read_b64_tr_b16 v[122:123], v172 offset:2048\n\t" \
  "v_max3_u32 v176, v176, v14, v15\n\t" \
  "v_mfma_f32_16x16x32_bf16 %[acc2], v[128:131], v[12:15], %[acc2]\n\t" \
  "ds_read_b64_tr_b16 v[124:125], v173 offset:0\n\t" \
  "ds_read_b64_tr_b16 v[126:127], v173 offset:2048\n\t" \
  "v_mfma_f32_16x16x32_bf16 %[acc3], v[132:135], v[12:15], %[acc3]\n\t" \
  "ds_read_b64_tr_b16 v[128:129], v174 offset:0\n\t" \
  "s_waitcnt lgkmcnt(7)\n\t" \
  "ds_read_b64_tr_b16 v[130:131], v174 offset:2048\n\t" \
  "v_mfma_f32_16x16x32_bf16 %[s1], v[164:167], v[12:15], %[s1]\n\t" \
  "ds_read_b64 v[156:157], %[at] offset:320\n\t" \
  "ds_read_b64 v[158:159], %[at] offset:352\n\t" \
  "s_waitcnt vmcnt(14)\n\t" \
  "v_mfma_f32_16x16x32_bf16 %[acc0], v[136:139], v[16:19], %[acc0]\n\t" \
  "ds_read_b64_tr_b16 v[132:133], v175 offset:0\n\t" \
  "ds_read_b64_tr_b16 v[134:135], v175 offset:2048\n\t" \
  "v_max3_u32 v176, v176, v16, v17\n\t" \
  "v_mfma_f32_16x16x32_bf16 %[acc1], v[140:143], v[16:19], %[acc1]\n\t" \
  "ds_read_b64_tr_b16 v[136:137], v172 offset:4096\n\t" \
  "ds_read_b64_tr_b16 v[138:139], v172 offset:6144\n\t" \
  "v_max3_u32 v176, v176, v18, v19\n\t" \
  "v_mfma_f32_16x16x32_bf16 %[acc2], v[144:147], v[16:19], %[acc2]\n\t" \
  "ds_read_b64_tr_b16 v[140:141], v173 offset:4096\n\t" \
  "s_waitcnt lgkmcnt(7)\n\t" \
  "ds_read_b64_tr_b16 v[142:143], v173 offset:6144\n\t" \
  "v_mfma_f32_16x16x32_bf16 %[acc3], v[148:151], v[16:19], %[acc3]\n\t" \
  "ds_read_b64_tr_b16 v[144:145], v174 offset:4096\n\t" \
  "ds_read_b64_tr_b16 v[146:147], v174 offset:6144\n\t" \
  "v_mfma_f32_16x16x32_bf16 %[s1], v[152:155], v[16:19], %[s1]\n\t" \
  "ds_read_b64 v[160:161], %[at] offset:384\n\t" \
  "ds_read_b64 v[162:163], %[at] offset:416\n\t" \
  "s_waitcnt vmcnt(13)\n\t" \
  "v_mfma_f32_16x16x32_bf16 %[acc0], v[72:75], v[20:23], %[acc0]\n\t" \
  "ds_read_b64_tr_b16 v[148:149], v175 offset:4096\n\t" \
  "ds_read_b64_tr_b16 v[150:151], v175 offset:6144\n\t" \
  "v_max3_u32 v176, v176, v20, v21\n\t" \
  "v_mfma_f32_16x16x32_bf16 %[acc1], v[76:79], v[20:23], %[acc1]\n\t" \
  "ds_read_b64_tr_b16 v[72:73], v172 offset:8192\n\t" \
  "s_waitcnt lgkmcnt(7)\n\t" \
  "ds_read_b64_tr_b16 v[74:75], v172 offset:10240\n\t" \
  "v_max3_u32 v176, v176, v22, v23\n\t" \
  "v_mfma_f32_16x16x32_bf16 %[acc2], v[80:83], v[20:23], %[acc2]\n\t" \
  "ds_read_b64_tr_b16 v[76:77], v173 offset:8192\n\t" \
  "ds_read_b64_tr_b16 v[78:79], v173 offset:10240\n\t" \
  "v_cmp_eq_u32 vcc, -1, v176\n\t" \
  "v_mfma_f32_16x16x32_bf16 %[acc3], v[84:87], v[20:23], %[acc3]\n\t" \
  "ds_read_b64_tr_b16 v[80:81], v174 offset:8192\n\t" \
  "ds_read_b64_tr_b16 v[82:83], v174 offset:10240\n\t" \
  "s_cbranch_vccnz L_G_%=\n\t" \
  "v_mfma_f32_16x16x32_bf16 %[s1], v[156:159], v[20:23], %[s1]\n\t" \
  "ds_read_b64 v[164:165], %[at] offset:448\n\t" \
  "ds_read_b64 v[166:167], %[at] offset:480\n\t" \
  "s_waitcnt vmcnt(12)\n\t" \
  "v_mfma_f32_16x16x32_bf16 %[acc0], v[88:91], v[24:27], %[acc0]\n\t" \
  "ds_read_b64_tr_b16 v[84:85], v175 offset:8192\n\t" \
  "s_waitcnt lgkmcnt(7)\n\t" \
  "ds_read_b64_tr_b16 v[86:87], v175 offset:10240\n\t" \
  "v_max3_u32 v176, v176, v24, v25\n\t" \
  "v_mfma_f32_16x16x32_bf16 %[acc1], v[92:95], v[24:27], %[acc1]\n\t" \
  "ds_read_b64_tr_b16 v[88:89], v172 offset:12288\n\t" \
  "ds_read_b64_tr_b16 v[90:91], v172 offset:14336\n\t" \
  "v_max3_u32 v176, v176, v26, v27\n\t" \
  "v_mfma_f32_16x16x32_bf16 %[acc2], v[96:99], v[24:27], %[acc2]\n\t" \
  "ds_read_b64_tr_b16 v[92:93], v173 offset:12288\n\t" \
  "ds_read_b64_tr_b16 v[94:95], v173 offset:14336\n\t" \
  "v_mfma_f32_16x16x32_bf16 %[acc3], v[100:103], v[24:27], %[acc3]\n\t" \
  "ds_read_b64_tr_b16 v[96:97], v174 offset:12288\n\t" \
  "ds_read_b64_tr_b16 v[98:99], v174 offset:14336\n\t" \
  "v_mfma_f32_16x16x32_bf16 %[s1], v[160:163], v[24:27], %[s1]\n\t" \
  "ds_read_b64 v[152:153], %[at] offset:512\n\t" \
  "s_waitcnt lgkmcnt(7)\n\t" \
  "ds_read_b64 v[154:155], %[at] offset:544\n\t" \
  "s_waitcnt vmcnt(11)\n\t" \
  "v_mfma_f32_16x16x32_bf16 %[acc0], v[104:107], v[28:31], %[acc0]\n\t" \
  "ds_read_b64_tr_b16 v[100:101], v175 offset:12288\n\t" \
  "ds_read_b64_tr_b16 v[102:103], v175 offset:14336\n\t" \
  "v_max3_u32 v176, v176, v28, v29\n\t" \
  "v_mfma_f32_16x16x32_bf16 %[acc1], v[108:111], v[28:31], %[acc1]\n\t" \
  "ds_read_b64_tr_b16 v[104:105], v172 offset:16384\n\t" \
  "ds_read_b64_tr_b16 v[106:107], v172 offset:18432\n\t" \
  "v_max3_u32 v176, v176, v30, v31\n\t" \
  "v_mfma_f32_16x16x32_bf16 %[acc2], v[112:115], v[28:31], %[acc2]\n\t" \
  "ds_read_b64_tr_b16 v[108:109], v173 offset:16384\n\t" \
  "ds_read_b64_tr_b16 v[110:111], v173 offset:18432\n\t" \
  "v_mfma_f32_16x16x32_bf16 %[acc3], v[116:119], v[28:31], %[acc3]\n\t" \
  "ds_read_b64_tr_b16 v[112:113], v174 offset:16384\n\t" \
  "s_waitcnt lgkmcnt(7)\n\t" \
  "ds_read_b64_tr_b16 v[114:115], v174 offset:18432\n\t" \
  "v_mfma_f32_16x16x32_bf16 %[s1], v[164:167], v[28:31], %[s1]\n\t" \
  "ds_read_b64 v[156:157], %[at] offset:576\n\t" \
  "ds_read_b64 v[158:159], %[at] offset:608\n\t" \
  "s_waitcnt vmcnt(10)\n\t" \
  "v_mfma_f32_16x16x32_bf16 %[acc0], v[120:123], v[32:35], %[acc0]\n\t" \
  "ds_read_b64_tr_b16 v[116:117], v175 offset:16384\n\t" \
  "ds_read_b64_tr_b16 v[118:119], v175 offset:18432\n\t" \
  "v_max3_u32 v176, v176, v32, v33\n\t" \
  "v_mfma_f32_16x16x32_bf16 %[acc1], v[124:127], v[32:35], %[acc1]\n\t" \
  "ds_read_b64_tr_b16 v[120:121], v172 offset:20480\n\t" \
  "ds_read_b64_tr_b16 v[122:123], v172 offset:22528\n\t" \
  "v_max3_u32 v176, v176, v34, v35\n\t" \
  "v_mfma_f32_16x16x32_bf16 %[acc2], v[128:131], v[32:35], %[acc2]\n\t" \
  "ds_read_b64_tr_b16 v[124:125], v173 offset:20480\n\t" \
  "s_waitcnt lgkmcnt(7)\n\t" \
  "ds_read_b64_tr_b16 v[126:127], v173 offset:22528\n\t" \
  "v_cmp_eq_u32 vcc, -1, v176\n\t" \
  "v_mfma_f32_16x16x32_bf16 %[acc3], v[132:135], v[32:35], %[acc3]\n\t" \
  "ds_read_b64_tr_b16 v[128:129], v174 offset:20480\n\t" \
  "ds_read_b64_tr_b16 v[130:131], v174 offset:22528\n\t" \
  "s_cbranch_vccnz L_G_%=\n\t" \
  "v_mfma_f32_16x16x32_bf16 %[s1], v[152:155], v[32:35], %[s1]\n\t" \
  "ds_read_b64 v[160:161], %[at] offset:640\n\t" \
  "ds_read_b64 v[162:163], %[at] offset:672\n\t" \
  "s_waitcnt vmcnt(9)\n\t" \
  "v_mfma_f32_16x16x32_bf16 %[acc0], v[136:139], v[36:39], %[acc0]\n\t" \
  "ds_read_b64_tr_b16 v[132:133], v175 offset:20480\n\t" \
  "ds_read_b64_tr_b16 v[134:135], v175 offset:22528\n\t" \
  "v_max3_u32 v176, v176, v36, v37\n\t" \
  "v_mfma_f32_16x16x32_bf16 %[acc1], v[140:143], v[36:39], %[acc1]\n\t" \
  "ds_read_b64_tr_b16 v[136:137], v172 offset:24576\n\t" \
  "s_waitcnt lgkmcnt(7)\n\t" \
  "ds_read_b64_tr_b16 v[138:139], v172 offset:26624\n\t" \
  "v_max3_u32 v176, v176, v38, v39\n\t" \
  "v_mfma_f32_16x16x32_bf16 %[acc2], v[144:147], v[36:39], %[acc2]\n\t" \
  "ds_read_b64_tr_b16 v[140:141], v173 offset:24576\n\t" \
  "ds_read_b64_tr_b16 v[142:143], v173 offset:26624\n\t" \
  "v_mfma_f32_16x16x32_bf16 %[acc3], v[148:151], v[36:39], %[acc3]\n\t" \
  "ds_read_b64_tr_b16 v[144:145], v174 offset:24576\n\t" \
  "ds_read_b64_tr_b16 v[146:147], v174 offset:26624\n\t" \
  "v_mfma_f32_16x16x32_bf16 %[s1], v[156:159], v[36:39], %[s1]\n\t" \
  "ds_read_b64 v[164:165], %[at] offset:704\n\t" \
  "ds_read_b64 v[166:167], %[at] offset:736\n\t" \
  "s_waitcnt vmcnt(8)\n\t" \
  "v_mfma_f32_16x16x32_bf16 %[acc0], v[72:75], v[40:43], %[acc0]\n\t" \
  "ds_read_b64_tr_b16 v[148:149], v175 offset:24576\n\t" \
  "s_waitcnt lgkmcnt(7)\n\t" \
  "ds_read_b64_tr_b16 v[150:151], v175 offset:26624\n\t" \
  "v_max3_u32 v176, v176, v40, v41\n\t" \
  "v_mfma_f32_16x16x32_bf16 %[acc1], v[76:79], v[40:43], %[acc1]\n\t" \
  "ds_read_b64_tr_b16 v[72:73], v172 offset:28672\n\t" \
  "ds_read_b64_tr_b16 v[74:75], v172 offset:30720\n\t" \
  "v_max3_u32 v176, v176, v42, v43\n\t" \
  "v_mfma_f32_16x16x32_bf16 %[acc2], v[80:83], v[40:43], %[acc2]\n\t" \
  "ds_read_b64_tr_b16 v[76:77], v173 offset:28672\n\t" \
  "v_mfma_f32_16x16x32_bf16 %[acc3], v[84:87], v[40:43], %[acc3]\n\t" \
  "ds_read_b64_tr_b16 v[78:79], v173 offset:30720\n\t" \
  "v_mfma_f32_16x16x32_bf16 %[s1], v[160:163], v[40:43], %[s1]\n\t" \
  "ds_read_b64 v[152:153], %[at] offset:768\n\t" \
  "ds_read_b64 v[154:155], %[at] offset:800\n\t" \
  "s_waitcnt vmcnt(7)\n\t" \
  "v_mfma_f32_16x16x32_bf16 %[acc0], v[88:91], v[44:47], %[acc0]\n\t" \
  "ds_read_b64_tr_b16 v[80:81], v174 offset:28672\n\t" \
  "v_max3_u32 v176, v176, v44, v45\n\t" \
  "v_mfma_f32_16x16x32_bf16 %[acc1], v[92:95], v[44:47], %[acc1]\n\t" \
  "s_waitcnt lgkmcnt(7)\n\t" \
  "ds_read_b64_tr_b16 v[82:83], v174 offset:30720\n\t" \
  "v_max3_u32 v176, v176, v46, v47\n\t" \
  "v_mfma_f32_16x16x32_bf16 %[acc2], v[96:99], v[44:47], %[acc2]\n\t" \
  "ds_read_b64_tr_b16 v[84:85], v175 offset:28672\n\t" \
  "v_cmp_eq_u32 vcc, -1, v176\n\t" \
  "v_mfma_f32_16x16x32_bf16 %[acc3], v[100:103], v[44:47], %[acc3]\n\t" \
  "ds_read_b64_tr_b16 v[86:87], v175 offset:30720\n\t" \
  "s_cbranch_vccnz L_G_%=\n\t" \
  "v_mfma_f32_16x16x32_bf16 %[s1], v[164:167], v[44:47], %[s1]\n\t" \
  "ds_read_b64 v[156:157], %[at] offset:832\n\t" \
  "ds_read_b64 v[158:159], %[at] offset:864\n\t" \
  "s_waitcnt vmcnt(6)\n\t" \
  "v_mfma_f32_16x16x32_bf16 %[acc0], v[104:107], v[48:51], %[acc0]\n\t" \
  "ds_read_b64_tr_b16 v[88:89], v172 offset:32768\n\t" \
  "v_max3_u32 v176, v176, v48, v49\n\t" \
  "v_mfma_f32_16x16x32_bf16 %[acc1], v[108:111], v[48:51], %[acc1]\n\t" \
  "ds_read_b64_tr_b16 v[90:91], v172 offset:34816\n\t" \
  "v_max3_u32 v176, v176, v50, v51\n\t" \
  "v_mfma_f32_16x16x32_bf16 %[acc2], v[112:115], v[48:51], %[acc2]\n\t" \
  "ds_read_b64_tr_b16 v[92:93], v173 offset:32768\n\t" \
  "v_mfma_f32_16x16x32_bf16 %[acc3], v[116:119], v[48:51], %[acc3]\n\t" \
  "s_waitcnt lgkmcnt(7)\n\t" \
  "ds_read_b64_tr_b16 v[94:95], v173 offset:34816\n\t" \
  "v_mfma_f32_16x16x32_bf16 %[s1], v[152:155], v[48:51], %[s1]\n\t" \
  "ds_read_b64 v[160:161], %[at] offset:896\n\t" \
  "ds_read_b64 v[162:163], %[at] offset:928\n\t" \
  "s_waitcnt vmcnt(5)\n\t" \
  "v_mfma_f32_16x16x32_bf16 %[acc0], v[120:123], v[52:55], %[acc0]\n\t" \
  "ds_read_b64_tr_b16 v[96:97], v174 offset:32768\n\t" \
  "v_max3_u32 v176, v176, v52, v53\n\t" \
  "v_mfma_f32_16x16x32_bf16 %[acc1], v[124:127], v[52:55], %[acc1]\n\t" \
  "ds_read_b64_tr_b16 v[98:99], v174 offset:34816\n\t" \
  "v_max3_u32 v176, v176, v54, v55\n\t" \
  "v_mfma_f32_16x16x32_bf16 %[acc2], v[128:131], v[52:55], %[acc2]\n\t" \
  "ds_read_b64_tr_b16 v[100:101], v175 offset:32768\n\t" \
  "v_mfma_f32_16x16x32_bf16 %[acc3], v[132:135], v[52:55], %[acc3]\n\t" \
  "ds_read_b64_tr_b16 v[102:103], v175 offset:34816\n\t" \
  "s_waitcnt lgkmcnt(10)\n\t" \
  "v_mfma_f32_16x16x32_bf16 %[s1], v[156:159], v[52:55], %[s1]\n\t" \
  "ds_read_b64 v[164:165], %[at] offset:960\n\t" \
  "ds_read_b64 v[166:167], %[at] offset:992\n\t" \
  "s_waitcnt vmcnt(4)\n\t" \
  "v_mfma_f32_16x16x32_bf16 %[acc0], v[136:139], v[56:59], %[acc0]\n\t" \
  "ds_read_b64_tr_b16 v[104:105], v172 offset:36864\n\t" \
  "v_max3_u32 v176, v176, v56, v57\n\t" \
  "v_mfma_f32_16x16x32_bf16 %[acc1], v[140:143], v[56:59], %[acc1]\n\t" \
  "ds_read_b64_tr_b16 v[106:107], v172 offset:38912\n\t" \
  "v_max3_u32 v176, v176, v58, v59\n\t" \
  "v_mfma_f32_16x16x32_bf16 %[acc2], v[144:147], v[56:59], %[acc2]\n\t" \
  "ds_read_b64_tr_b16 v[108:109], v173 offset:36864\n\t" \
  "v_cmp_eq_u32 vcc, -1, v176\n\t" \
  "v_mfma_f32_16x16x32_bf16 %[acc3], v[148:151], v[56:59], %[acc3]\n\t" \
  "s_waitcnt lgkmcnt(7)\n\t" \
  "ds_read_b64_tr_b16 v[110:111], v173 offset:38912\n\t" \
  "s_cbranch_vccnz L_G_%=\n\t" \
  "v_mfma_f32_16x16x32_bf16 %[s1], v[160:163], v[56:59], %[s1]\n\t" \
  "ds_read_b64 v[152:153], %[at] offset:1024\n\t" \
  "ds_read_b64 v[154:155], %[at] offset:1056\n\t" \
  "s_waitcnt vmcnt(3)\n\t" \
  "v_mfma_f32_16x16x32_bf16 %[acc0], v[72:75], v[60:63], %[acc0]\n\t" \
  "ds_read_b64_tr_b16 v[112:113], v174 offset:36864\n\t" \
  "v_max3_u32 v176, v176, v60, v61\n\t" \
  "v_mfma_f32_16x16x32_bf16 %[acc1], v[76:79], v[60:63], %[acc1]\n\t" \
  "ds_read_b64_tr_b16 v[114:115], v174 offset:38912\n\t" \
  "v_max3_u32 v176, v176, v62, v63\n\t" \
  "v_mfma_f32_16x16x32_bf16 %[acc2], v[80:83], v[60:63], %[acc2]\n\t" \
  "ds_read_b64_tr_b16 v[116:117], v175 offset:36864\n\t" \
  "v_mfma_f32_16x16x32_bf16 %[acc3], v[84:87], v[60:63], %[acc3]\n\t" \
  "ds_read_b64_tr_b16 v[118:119], v175 offset:38912\n\t" \
  "s_waitcnt lgkmcnt(10)\n\t" \
  "v_mfma_f32_16x16x32_bf16 %[s1], v[164:167], v[60:63], %[s1]\n\t" \
  "ds_read_b64 v[156:157], %[at] offset:1088\n\t" \
  "ds_read_b64 v[158:159], %[at] offset:1120\n\t" \
  "s_waitcnt vmcnt(2)\n\t" \
  "v_mfma_f32_16x16x32_bf16 %[acc0], v[88:91], v[64:67], %[acc0]\n\t" \
  "v_max3_u32 v176, v176, v64, v65\n\t" \
  "v_mfma_f32_16x16x32_bf16 %[acc1], v[92:95], v[64:67], %[acc1]\n\t" \
  "v_max3_u32 v176, v176, v66, v67\n\t" \
  "v_mfma_f32_16x16x32_bf16 %[acc2], v[96:99], v[64:67], %[acc2]\n\t" \
  "v_mfma_f32_16x16x32_bf16 %[acc3], v[100:103], v[64:67], %[acc3]\n\t" \
  "s_waitcnt lgkmcnt(6)\n\t" \
  "v_mfma_f32_16x16x32_bf16 %[s1], v[152:155], v[64:67], %[s1]\n\t" \
  "s_waitcnt vmcnt(1)\n\t" \
  "v_mfma_f32_16x16x32_bf16 %[acc0], v[104:107], v[68:71], %[acc0]\n\t" \
  "v_max3_u32 v176, v176, v68, v69\n\t" \
  "v_mfma_f32_16x16x32_bf16 %[acc1], v[108:111], v[68:71], %[acc1]\n\t" \
  "v_max3_u32 v176, v176, v70, v71\n\t" \
  "s_waitcnt lgkmcnt(0)\n\t" \
  "v_mfma_f32_16x16x32_bf16 %[acc2], v[112:115], v[68:71], %[acc2]\n\t" \
  "v_cmp_eq_u32 vcc, -1, v176\n\t" \
  "v_mfma_f32_16x16x32_bf16 %[acc3], v[116:119], v[68:71], %[acc3]\n\t" \
  "s_cbranch_vccnz L_G_%=\n\t" \
  "v_mfma_f32_16x16x32_bf16 %[s1], v[156:159], v[68:71], %[s1]\n\t" \
  "L_D_%=:\n\t" \
  "s_setprio 0\n\t" \
  "s_branch L_X_%=\n\t" \
  "L_G_%=:\n\t" \
  "s_waitcnt vmcnt(0) lgkmcnt(0)\n\t" \
  "s_add_u32 s41, s41, 1000\n\t" \
  "L_H_%=:\n\t" \
  "s_mov_b32 exec_hi, 15\n\t" \
  "buffer_load_dwordx4 v[0:3], %[vg], %[rs], %[sh] offen sc1\n\t" \
  "s_add_u32 s41, s41, 1\n\t" \
  "s_waitcnt vmcnt(0)\n\t" \
  "v_max3_u32 v0, v0, v1, v2\n\t" \
  "v_max_u32 v0, v0, v3\n\t" \
  "v_cmp_eq_u32 vcc, -1, v0\n\t" \
  "s_mov_b64 exec, -1\n\t" \
  "s_cmp_gt_u32 s41, 400000\n\t" \
  "s_cbranch_scc1 L_D_%=\n\t" \
  "s_cmp_lg_u64 vcc, 0\n\t" \
  "s_cbranch_scc1 L_H_%=\n\t" \
  "s_lshl_b32 s40, %[c], 11\n\t" \
  "s_add_u32 s42, %[sh], 18432\n\t" \
  "s_add_u32 s40, s40, %[sh]\n\t" \
  "buffer_load_dword %[mq], %[vmq], %[rs], 0 offen sc1\n\t" \
  "buffer_load_dwordx4 v[0:3], %[vh], %[rs], s40 offen offset:0 sc1\n\t" \
  "buffer_load_dwordx4 v[4:7], %[vh], %[rs], s40 offen offset:1024 sc1\n\t" \
  "s_add_u32 s40, s40, 2048\n\t" \
  "s_cmp_ge_u32 s40, s42\n\t" \
  "s_cselect_b32 s40, %[sh], s40\n\t" \
  "buffer_load_dwordx4 v[8:11], %[vh], %[rs], s40 offen offset:0 sc1\n\t" \
  "buffer_load_dwordx4 v[12:15], %[vh], %[rs], s40 offen offset:1024 sc1\n\t" \
  "s_add_u32 s40, s40, 2048\n\t" \
  "s_cmp_ge_u32 s40, s42\n\t" \
  "s_cselect_b32 s40, %[sh], s40\n\t" \
  "buffer_load_dwordx4 v[16:19], %[vh], %[rs], s40 offen offset:0 sc1\n\t" \
  "buffer_load_dwordx4 v[20:23], %[vh], %[rs], s40 offen offset:1024 sc1\n\t" \
  "s_add_u32 s40, s40, 2048\n\t" \
  "s_cmp_ge_u32 s40, s42\n\t" \
  "s_cselect_b32 s40, %[sh], s40\n\t" \
  "buffer_load_dwordx4 v[24:27], %[vh], %[rs], s40 offen offset:0 sc1\n\t" \
  "buffer_load_dwordx4 v[28:31], %[vh], %[rs], s40 offen offset:1024 sc1\n\t" \
  "s_add_u32 s40, s40, 2048\n\t" \
  "s_cmp_ge_u32 s40, s42\n\t" \
  "s_cselect_b32 s40, %[sh], s40\n\t" \
  "buffer_load_dwordx4 v[32:35], %[vh], %[rs], s40 offen offset:0 sc1\n\t" \
  "buffer_load_dwordx4 v[36:39], %[vh], %[rs], s40 offen offset:1024 sc1\n\t" \
  "s_add_u32 s40, s40, 2048\n\t" \
  "s_cmp_ge_u32 s40, s42\n\t" \
  "s_cselect_b32 s40, %[sh], s40\n\t" \
  "buffer_load_dwordx4 v[40:43], %[vh], %[rs], s40 offen offset:0 sc1\n\t" \
  "buffer_load_dwordx4 v[44:47], %[vh], %[rs], s40 offen offset:1024 sc1\n\t" \
  "s_add_u32 s40, s40, 2048\n\t" \
  "s_cmp_ge_u32 s40, s42\n\t" \
  "s_cselect_b32 s40, %[sh], s40\n\t" \
  "buffer_load_dwordx4 v[48:51], %[vh], %[rs], s40 offen offset:0 sc1\n\t" \
  "buffer_load_dwordx4 v[52:55], %[vh], %[rs], s40 offen offset:1024 sc1\n\t" \
  "s_add_u32 s40, s40, 2048\n\t" \
  "s_cmp_ge_u32 s40, s42\n\t" \
  "s_cselect_b32 s40, %[sh], s40\n\t" \
  "buffer_load_dwordx4 v[56:59], %[vh], %[rs], s40 offen offset:0 sc1\n\t" \
  "buffer_load_dwordx4 v[60:63], %[vh], %[rs], s40 offen offset:1024 sc1\n\t" \
  "s_add_u32 s40, s40, 2048\n\t" \
  "s_cmp_ge_u32 s40, s42\n\t" \
  "s_cselect_b32 s40, %[sh], s40\n\t" \
  "buffer_load_dwordx4 v[64:67], %[vh], %[rs], s40 offen offset:0 sc1\n\t" \
  "buffer_load_dwordx4 v[68:71], %[vh], %[rs], s40 offen offset:1024 sc1\n\t" \
  "s_branch L_R_%=\n\t" \
  "L_X_%=:\n\t" \
  "s_nop 5\n\t" \
  ""
#define P1_B \
  "v_xor_b32 v168, 0, %[aw]\n\t" \
  "v_add_u32 v172, 32768, v168\n\t" \
  "v_xor_b32 v169, 32, %[aw]\n\t" \
  "v_add_u32 v173, 32768, v169\n\t" \
  "v_xor_b32 v170, 64, %[aw]\n\t" \
  "v_add_u32 v174, 32768, v170\n\t" \
  "v_xor_b32 v171, 96, %[aw]\n\t" \
  "v_add_u32 v175, 32768, v171\n\t" \
  "s_mov_b32 s41, 0\n\t" \
  "s_waitcnt lgkmcnt(0)\n\t" \
  "s_barrier\n\t" \
  "s_lshl_b32 s40, %[c], 11\n\t" \
  "s_add_u32 s42, %[sh], 18432\n\t" \
  "s_add_u32 s40, s40, %[sh]\n\t" \
  "buffer_load_dword %[mq], %[vmq], %[rs], 0 offen sc1\n\t" \
  "buffer_load_dwordx4 v[0:3], %[vh], %[rs], s40 offen offset:0 sc1\n\t" \
  "s_add_u32 s40, s40, 2048\n\t" \
  "s_cmp_ge_u32 s40, s42\n\t" \
  "s_cselect_b32 s40, %[sh], s40\n\t" \
  "buffer_load_dwordx4 v[4:7], %[vh], %[rs], s40 offen offset:0 sc1\n\t" \
  "s_add_u32 s40, s40, 2048\n\t" \
  "s_cmp_ge_u32 s40, s42\n\t" \
  "s_cselect_b32 s40, %[sh], s40\n\t" \
  "buffer_load_dwordx4 v[8:11], %[vh], %[rs], s40 offen offset:0 sc1\n\t" \
  "s_add_u32 s40, s40, 2048\n\t" \
  "s_cmp_ge_u32 s40, s42\n\t" \
  "s_cselect_b32 s40, %[sh], s40\n\t" \
  "buffer_load_dwordx4 v[12:15], %[vh], %[rs], s40 offen offset:0 sc1\n\t" \
  "s_add_u32 s40, s40, 2048\n\t" \
  "s_cmp_ge_u32 s40, s42\n\t" \
  "s_cselect_b32 s40, %[sh], s40\n\t" \
  "buffer_load_dwordx4 v[16:19], %[vh], %[rs], s40 offen offset:0 sc1\n\t" \
  "s_add_u32 s40, s40, 2048\n\t" \
  "s_cmp_ge_u32 s40, s42\n\t" \
  "s_cselect_b32 s40, %[sh], s40\n\t" \
  "buffer_load_dwordx4 v[20:23], %[vh], %[rs], s40 offen offset:0 sc1\n\t" \
  "s_add_u32 s40, s40, 2048\n\t" \
  "s_cmp_ge_u32 s40, s42\n\t" \
  "s_cselect_b32 s40, %[sh], s40\n\t" \
  "buffer_load_dwordx4 v[24:27], %[vh], %[rs], s40 offen offset:0 sc1\n\t" \
  "s_add_u32 s40, s40, 2048\n\t" \
  "s_cmp_ge_u32 s40, s42\n\t" \
  "s_cselect_b32 s40, %[sh], s40\n\t" \
  "buffer_load_dwordx4 v[28:31], %[vh], %[rs], s40 offen offset:0 sc1\n\t" \
  "s_add_u32 s40, s40, 2048\n\t" \
  "s_cmp_ge_u32 s40, s42\n\t" \
  "s_cselect_b32 s40, %[sh], s40\n\t" \
  "buffer_load_dwordx4 v[32:35], %[vh], %[rs], s40 offen offset:0 sc1\n\t" \
  "L_R_%=:\n\t" \
  "s_setprio 1\n\t" \
  "ds_read_b64 v[152:153], %[at] offset:0\n\t" \
  "ds_read_b64 v[154:155], %[at] offset:32\n\t" \
  "ds_read_b64 v[156:157], %[at] offset:128\n\t" \
  "ds_read_b64 v[158:159], %[at] offset:160\n\t" \
  "ds_read_b64_tr_b16 v[72:73], v168 offset:0\n\t" \
  "ds_read_b64_tr_b16 v[74:75], v168 offset:2048\n\t" \
  "ds_read_b64_tr_b16 v[76:77], v169 offset:0\n\t" \
  "ds_read_b64_tr_b16 v[78:79], v169 offset:2048\n\t" \
  "ds_read_b64_tr_b16 v[80:81], v170 offset:0\n\t" \
  "ds_read_b64_tr_b16 v[82:83], v170 offset:2048\n\t" \
  "ds_read_b64_tr_b16 v[84:85], v171 offset:0\n\t" \
  "ds_read_b64_tr_b16 v[86:87], v171 offset:2048\n\t" \
  "ds_read_b64_tr_b16 v[88:89], v168 offset:8192\n\t" \
  "ds_read_b64_tr_b16 v[90:91], v168 offset:10240\n\t" \
  "ds_read_b64_tr_b16 v[92:93], v169 offset:8192\n\t" \
  "s_waitcnt lgkmcnt(7)\n\t" \
  "ds_read_b64_tr_b16 v[94:95], v169 offset:10240\n\t" \
  "ds_read_b64_tr_b16 v[96:97], v170 offset:8192\n\t" \
  "ds_read_b64_tr_b16 v[98:99], v170 offset:10240\n\t" \
  "ds_read_b64_tr_b16 v[100:101], v171 offset:8192\n\t" \
  "ds_read_b64_tr_b16 v[102:103], v171 offset:10240\n\t" \
  "ds_read_b64_tr_b16 v[104:105], v168 offset:16384\n\t" \
  "ds_read_b64_tr_b16 v[106:107], v168 offset:18432\n\t" \
  "ds_read_b64_tr_b16 v[108:109], v169 offset:16384\n\t" \
  "s_waitcnt lgkmcnt(7)\n\t" \
  "ds_read_b64_tr_b16 v[110:111], v169 offset:18432\n\t" \
  "ds_read_b64_tr_b16 v[112:113], v170 offset:16384\n\t" \
  "ds_read_b64_tr_b16 v[114:115], v170 offset:18432\n\t" \
  "ds_read_b64_tr_b16 v[116:117], v171 offset:16384\n\t" \
  "ds_read_b64_tr_b16 v[118:119], v171 offset:18432\n\t" \
  "ds_read_b64_tr_b16 v[120:121], v168 offset:24576\n\t" \
  "ds_read_b64_tr_b16 v[122:123], v168 offset:26624\n\t" \
  "ds_read_b64_tr_b16 v[124:125], v169 offset:24576\n\t" \
  "s_waitcnt lgkmcnt(7)\n\t" \
  "ds_read_b64_tr_b16 v[126:127], v169 offset:26624\n\t" \
  "ds_read_b64_tr_b16 v[128:129], v170 offset:24576\n\t" \
  "ds_read_b64_tr_b16 v[130:131], v170 offset:26624\n\t" \
  "ds_read_b64_tr_b16 v[132:133], v171 offset:24576\n\t" \
  "ds_read_b64_tr_b16 v[134:135], v171 offset:26624\n\t" \
  "ds_read_b64_tr_b16 v[136:137], v172 offset:0\n\t" \
  "ds_read_b64_tr_b16 v[138:139], v172 offset:2048\n\t" \
  "ds_read_b64_tr_b16 v[140:141], v173 offset:0\n\t" \
  "s_waitcnt lgkmcnt(7)\n\t" \
  "ds_read_b64_tr_b16 v[142:143], v173 offset:2048\n\t" \
  "ds_read_b64_tr_b16 v[144:145], v174 offset:0\n\t" \
  "ds_read_b64_tr_b16 v[146:147], v174 offset:2048\n\t" \
  "s_waitcnt vmcnt(8)\n\t" \
  "s_mov_b64 exec, 0xffff\n\t" \
  "buffer_store_dword %[vtag], %[vfu], %[rs], 0 offen sc1\n\t" \
  "s_mov_b64 exec, -1\n\t" \
  "s_nop 3\n\t" \
  "v_mfma_f32_16x16x32_bf16 %[acc0], v[72:75], v[0:3], 0\n\t" \
  "ds_read_b64_tr_b16 v[148:149], v175 offset:0\n\t" \
  "ds_read_b64_tr_b16 v[150:151], v175 offset:2048\n\t" \
  "v_max3_u32 v176, %[mq], v0, v1\n\t" \
  "v_mfma_f32_16x16x32_bf16 %[acc1], v[76:79], v[0:3], 0\n\t" \
  "ds_read_b64_tr_b16 v[72:73], v172 offset:8192\n\t" \
  "ds_read_b64_tr_b16 v[74:75], v172 offset:10240\n\t" \
  "v_max3_u32 v176, v176, v2, v3\n\t" \
  "v_mfma_f32_16x16x32_bf16 %[acc2], v[80:83], v[0:3], 0\n\t" \
  "ds_read_b64_tr_b16 v[76:77], v173 offset:8192\n\t" \
  "s_waitcnt lgkmcnt(7)\n\t" \
  "ds_read_b64_tr_b16 v[78:79], v173 offset:10240\n\t" \
  "v_mfma_f32_16x16x32_bf16 %[acc3], v[84:87], v[0:3], 0\n\t" \
  "ds_read_b64_tr_b16 v[80:81], v174 offset:8192\n\t" \
  "ds_read_b64_tr_b16 v[82:83], v174 offset:10240\n\t" \
  "v_mfma_f32_16x16x32_bf16 %[s1], v[152:155], v[0:3], 0\n\t" \
  "ds_read_b64 v[160:161], %[at] offset:256\n\t" \
  "ds_read_b64 v[162:163], %[at] offset:288\n\t" \
  "s_waitcnt vmcnt(8)\n\t" \
  "v_mfma_f32_16x16x32_bf16 %[acc0], v[88:91], v[4:7], %[acc0]\n\t" \
  "ds_read_b64_tr_b16 v[84:85], v175 offset:8192\n\t" \
  "ds_read_b64_tr_b16 v[86:87], v175 offset:10240\n\t" \
  "v_max3_u32 v176, v176, v4, v5\n\t" \
  "v_mfma_f32_16x16x32_bf16 %[acc1], v[92:95], v[4:7], %[acc1]\n\t" \
  "ds_read_b64_tr_b16 v[88:89], v172 offset:16384\n\t" \
  "s_waitcnt lgkmcnt(7)\n\t" \
  "ds_read_b64_tr_b16 v[90:91], v172 offset:18432\n\t" \
  "v_max3_u32 v176, v176, v6, v7\n\t" \
  "v_mfma_f32_16x16x32_bf16 %[acc2], v[96:99], v[4:7], %[acc2]\n\t" \
  "ds_read_b64_tr_b16 v[92:93], v173 offset:16384\n\t" \
  "v_mfma_f32_16x16x32_bf16 %[acc3], v[100:103], v[4:7], %[acc3]\n\t" \
  "ds_read_b64_tr_b16 v[94:95], v173 offset:18432\n\t" \
  "v_mfma_f32_16x16x32_bf16 %[s1], v[156:159], v[4:7], %[s1]\n\t" \
  "ds_read_b64 v[164:165], %[at] offset:384\n\t" \
  "ds_read_b64 v[166:167], %[at] offset:416\n\t" \
  "s_waitcnt vmcnt(7)\n\t" \
  "v_mfma_f32_16x16x32_bf16 %[acc0], v[104:107], v[8:11], %[acc0]\n\t" \
  "ds_read_b64_tr_b16 v[96:97], v174 offset:16384\n\t" \
  "v_max3_u32 v176, v176, v8, v9\n\t" \
  "v_mfma_f32_16x16x32_bf16 %[acc1], v[108:111], v[8:11], %[acc1]\n\t" \
  "ds_read_b64_tr_b16 v[98:99], v174 offset:18432\n\t" \
  "v_max3_u32 v176, v176, v10, v11\n\t" \
  "v_mfma_f32_16x16x32_bf16 %[acc2], v[112:115], v[8:11], %[acc2]\n\t" \
  "ds_read_b64_tr_b16 v[100:101], v175 offset:16384\n\t" \
  "v_cmp_eq_u32 vcc, -1, v176\n\t" \
  "v_mfma_f32_16x16x32_bf16 %[acc3], v[116:119], v[8:11], %[acc3]\n\t" \
  "s_waitcnt lgkmcnt(7)\n\t" \
  "ds_read_b64_tr_b16 v[102:103], v175 offset:18432\n\t" \
  "s_cbranch_vccnz L_G_%=\n\t" \
  "v_mfma_f32_16x16x32_bf16 %[s1], v[160:163], v[8:11], %[s1]\n\t" \
  "ds_read_b64 v[152:153], %[at] offset:512\n\t" \
  "ds_read_b64 v[154:155], %[at] offset:544\n\t" \
  "s_waitcnt vmcnt(6)\n\t" \
  "v_mfma_f32_16x16x32_bf16 %[acc0], v[120:123], v[12:15], %[acc0]\n\t" \
  "ds_read_b64_tr_b16 v[104:105], v172 offset:24576\n\t" \
  "v_max3_u32 v176, v176, v12, v13\n\t" \
  "v_mfma_f32_16x16x32_bf16 %[acc1], v[124:127], v[12:15], %[acc1]\n\t" \
  "ds_read_b64_tr_b16 v[106:107], v172 offset:26624\n\t" \
  "v_max3_u32 v176, v176, v14, v15\n\t" \
  "v_mfma_f32_16x16x32_bf16 %[acc2], v[128:131], v[12:15], %[acc2]\n\t" \
  "ds_read_b64_tr_b16 v[108:109], v173 offset:24576\n\t" \
  "v_mfma_f32_16x16x32_bf16 %[acc3], v[132:135], v[12:15], %[acc3]\n\t" \
  "ds_read_b64_tr_b16 v[110:111], v173 offset:26624\n\t" \
  "s_waitcnt lgkmcnt(10)\n\t" \
  "v_mfma_f32_16x16x32_bf16 %[s1], v[164:167], v[12:15], %[s1]\n\t" \
  "ds_read_b64 v[156:157], %[at] offset:640\n\t" \
  "ds_read_b64 v[158:159], %[at] offset:672\n\t" \
  "s_waitcnt vmcnt(5)\n\t" \
  "v_mfma_f32_16x16x32_bf16 %[acc0], v[136:139], v[16:19], %[acc0]\n\t" \
  "ds_read_b64_tr_b16 v[112:113], v174 offset:24576\n\t" \
  "v_max3_u32 v176, v176, v16, v17\n\t" \
  "v_mfma_f32_16x16x32_bf16 %[acc1], v[140:143], v[16:19], %[acc1]\n\t" \
  "ds_read_b64_tr_b16 v[114:115], v174 offset:26624\n\t" \
  "v_max3_u32 v176, v176, v18, v19\n\t" \
  "v_mfma_f32_16x16x32_bf16 %[acc2], v[144:147], v[16:19], %[acc2]\n\t" \
  "ds_read_b64_tr_b16 v[116:117], v175 offset:24576\n\t" \
  "v_mfma_f32_16x16x32_bf16 %[acc3], v[148:151], v[16:19], %[acc3]\n\t" \
  "s_waitcnt lgkmcnt(7)\n\t" \
  "ds_read_b64_tr_b16 v[118:119], v175 offset:26624\n\t" \
  "v_mfma_f32_16x16x32_bf16 %[s1], v[152:155], v[16:19], %[s1]\n\t" \
  "ds_read_b64 v[160:161], %[at] offset:768\n\t" \
  "ds_read_b64 v[162:163], %[at] offset:800\n\t" \
  "s_waitcnt vmcnt(4)\n\t" \
  "v_mfma_f32_16x16x32_bf16 %[acc0], v[72:75], v[20:23], %[acc0]\n\t" \
  "ds_read_b64_tr_b16 v[120:121], v172 offset:32768\n\t" \
  "v_max3_u32 v176, v176, v20, v21\n\t" \
  "v_mfma_f32_16x16x32_bf16 %[acc1], v[76:79], v[20:23], %[acc1]\n\t" \
  "ds_read_b64_tr_b16 v[122:123], v172 offset:34816\n\t" \
  "v_max3_u32 v176, v176, v22, v23\n\t" \
  "v_mfma_f32_16x16x32_bf16 %[acc2], v[80:83], v[20:23], %[acc2]\n\t" \
  "ds_read_b64_tr_b16 v[124:125], v173 offset:32768\n\t" \
  "v_cmp_eq_u32 vcc, -1, v176\n\t" \
  "v_mfma_f32_16x16x32_bf16 %[acc3], v[84:87], v[20:23], %[acc3]\n\t" \
  "ds_read_b64_tr_b16 v[126:127], v173 offset:34816\n\t" \
  "s_cbranch_vccnz L_G_%=\n\t" \
  "s_waitcnt lgkmcnt(10)\n\t" \
  "v_mfma_f32_16x16x32_bf16 %[s1], v[156:159], v[20:23], %[s1]\n\t" \
  "ds_read_b64 v[164:165], %[at] offset:896\n\t" \
  "ds_read_b64 v[166:167], %[at] offset:928\n\t" \
  "s_waitcnt vmcnt(3)\n\t" \
  "v_mfma_f32_16x16x32_bf16 %[acc0], v[88:91], v[24:27], %[acc0]\n\t" \
  "ds_read_b64_tr_b16 v[128:129], v174 offset:32768\n\t" \
  "v_max3_u32 v176, v176, v24, v25\n\t" \
  "v_mfma_f32_16x16x32_bf16 %[acc1], v[92:95], v[24:27], %[acc1]\n\t" \
  "ds_read_b64_tr_b16 v[130:131], v174 offset:34816\n\t" \
  "v_max3_u32 v176, v176, v26, v27\n\t" \
  "v_mfma_f32_16x16x32_bf16 %[acc2], v[96:99], v[24:27], %[acc2]\n\t" \
  "ds_read_b64_tr_b16 v[132:133], v175 offset:32768\n\t" \
  "v_mfma_f32_16x16x32_bf16 %[acc3], v[100:103], v[24:27], %[acc3]\n\t" \
  "s_waitcnt lgkmcnt(7)\n\t" \
  "ds_read_b64_tr_b16 v[134:135], v175 offset:34816\n\t" \
  "v_mfma_f32_16x16x32_bf16 %[s1], v[160:163], v[24:27], %[s1]\n\t" \
  "ds_read_b64 v[152:153], %[at] offset:1024\n\t" \
  "ds_read_b64 v[154:155], %[at] offset:1056\n\t" \
  "s_waitcnt vmcnt(2)\n\t" \
  "v_mfma_f32_16x16x32_bf16 %[acc0], v[104:107], v[28:31], %[acc0]\n\t" \
  "v_max3_u32 v176, v176, v28, v29\n\t" \
  "v_mfma_f32_16x16x32_bf16 %[acc1], v[108:111], v[28:31], %[acc1]\n\t" \
  "v_max3_u32 v176, v176, v30, v31\n\t" \
  "v_mfma_f32_16x16x32_bf16 %[acc2], v[112:115], v[28:31], %[acc2]\n\t" \
  "v_mfma_f32_16x16x32_bf16 %[acc3], v[116:119], v[28:31], %[acc3]\n\t" \
  "s_waitcnt lgkmcnt(6)\n\t" \
  "v_mfma_f32_16x16x32_bf16 %[s1], v[164:167], v[28:31], %[s1]\n\t" \
  "s_waitcnt vmcnt(1)\n\t" \
  "v_mfma_f32_16x16x32_bf16 %[acc0], v[120:123], v[32:35], %[acc0]\n\t" \
  "v_max3_u32 v176, v176, v32, v33\n\t" \
  "v_mfma_f32_16x16x32_bf16 %[acc1], v[124:127], v[32:35], %[acc1]\n\t" \
  "v_max3_u32 v176, v176, v34, v35\n\t" \
  "s_waitcnt lgkmcnt(0)\n\t" \
  "v_mfma_f32_16x16x32_bf16 %[acc2], v[128:131], v[32:35], %[acc2]\n\t" \
  "v_cmp_eq_u32 vcc, -1, v176\n\t" \
  "v_mfma_f32_16x16x32_bf16 %[acc3], v[132:135], v[32:35], %[acc3]\n\t" \
  "s_cbranch_vccnz L_G_%=\n\t" \
  "v_mfma_f32_16x16x32_bf16 %[s1], v[152:155], v[32:35], %[s1]\n\t" \
  "L_D_%=:\n\t" \
  "s_setprio 0\n\t" \
  "s_branch L_X_%=\n\t" \
  "L_G_%=:\n\t" \
  "s_waitcnt vmcnt(0) lgkmcnt(0)\n\t" \
  "s_add_u32 s41, s41, 1000\n\t" \
  "L_H_%=:\n\t" \
  "s_mov_b64 exec, 0x3ffff\n\t" \
  "buffer_load_dwordx4 v[0:3], %[vg], %[rs], %[sh] offen sc1\n\t" \
  "s_add_u32 s41, s41, 1\n\t" \
  "s_waitcnt vmcnt(0)\n\t" \
  "v_max3_u32 v0, v0, v1, v2\n\t" \
  "v_max_u32 v0, v0, v3\n\t" \
  "v_cmp_eq_u32 vcc, -1, v0\n\t" \
  "s_mov_b64 exec, -1\n\t" \
  "s_cmp_gt_u32 s41, 400000\n\t" \
  "s_cbranch_scc1 L_D_%=\n\t" \
  "s_cmp_lg_u64 vcc, 0\n\t" \
  "s_cbranch_scc1 L_H_%=\n\t" \
  "s_lshl_b32 s40, %[c], 11\n\t" \
  "s_add_u32 s42, %[sh], 18432\n\t" \
  "s_add_u32 s40, s40, %[sh]\n\t" \
  "buffer_load_dword %[mq], %[vmq], %[rs], 0 offen sc1\n\t" \
  "buffer_load_dwordx4 v[0:3], %[vh], %[rs], s40 offen offset:0 sc1\n\t" \
  "s_add_u32 s40, s40, 2048\n\t" \
  "s_cmp_ge_u32 s40, s42\n\t" \
  "s_cselect_b32 s40, %[sh], s40\n\t" \
  "buffer_load_dwordx4 v[4:7], %[vh], %[rs], s40 offen offset:0 sc1\n\t" \
  "s_add_u32 s40, s40, 2048\n\t" \
  "s_cmp_ge_u32 s40, s42\n\t" \
  "s_cselect_b32 s40, %[sh], s40\n\t" \
  "buffer_load_dwordx4 v[8:11], %[vh], %[rs], s40 offen offset:0 sc1\n\t" \
  "s_add_u32 s40, s40, 2048\n\t" \
  "s_cmp_ge_u32 s40, s42\n\t" \
  "s_cselect_b32 s40, %[sh], s40\n\t" \
  "buffer_load_dwordx4 v[12:15], %[vh], %[rs], s40 offen offset:0 sc1\n\t" \
  "s_add_u32 s40, s40, 2048\n\t" \
  "s_cmp_ge_u32 s40, s42\n\t" \
  "s_cselect_b32 s40, %[sh], s40\n\t" \
  "buffer_load_dwordx4 v[16:19], %[vh], %[rs], s40 offen offset:0 sc1\n\t" \
  "s_add_u32 s40, s40, 2048\n\t" \
  "s_cmp_ge_u32 s40, s42\n\t" \
  "s_cselect_b32 s40, %[sh], s40\n\t" \
  "buffer_load_dwordx4 v[20:23], %[vh], %[rs], s40 offen offset:0 sc1\n\t" \
  "s_add_u32 s40, s40, 2048\n\t" \
  "s_cmp_ge_u32 s40, s42\n\t" \
  "s_cselect_b32 s40, %[sh], s40\n\t" \
  "buffer_load_dwordx4 v[24:27], %[vh], %[rs], s40 offen offset:0 sc1\n\t" \
  "s_add_u32 s40, s40, 2048\n\t" \
  "s_cmp_ge_u32 s40, s42\n\t" \
  "s_cselect_b32 s40, %[sh], s40\n\t" \
  "buffer_load_dwordx4 v[28:31], %[vh], %[rs], s40 offen offset:0 sc1\n\t" \
  "s_add_u32 s40, s40, 2048\n\t" \
  "s_cmp_ge_u32 s40, s42\n\t" \
  "s_cselect_b32 s40, %[sh], s40\n\t" \
  "buffer_load_dwordx4 v[32:35], %[vh], %[rs], s40 offen offset:0 sc1\n\t" \
  "s_branch L_R_%=\n\t" \
  "L_X_%=:\n\t" \
  "s_nop 5\n\t" \
  ""
#define CONV_0 \
  "v_mov_b32 v160, 0\n\t" \
  "v_mov_b32 v161, 0\n\t" \
  "v_mov_b32 v162, 0\n\t" \
  "v_mov_b32 v163, 0\n\t" \
  "v_mov_b32 v164, 0\n\t" \
  "v_mov_b32 v165, 0\n\t" \
  "v_mov_b32 v166, 0\n\t" \
  "v_mov_b32 v167, 0\n\t" \
  "v_mov_b32 v168, 0\n\t" \
  "v_mov_b32 v169, 0\n\t" \
  "v_mov_b32 v170, 0\n\t" \
  "v_mov_b32 v171, 0\n\t" \
  "v_mov_b32 v172, 0\n\t" \
  "v_mov_b32 v173, 0\n\t" \
  "v_mov_b32 v174, 0\n\t" \
  "v_mov_b32 v175, 0\n\t" \
  "ds_read_b64_tr_b16 v[0:1], %[ha] offset:32\n\t" \
  "ds_read_b64_tr_b16 v[2:3], %[ha] offset:0\n\t" \
  "ds_read_b64_tr_b16 v[4:5], %[ha] offset:288\n\t" \
  "ds_read_b64_tr_b16 v[6:7], %[ha] offset:256\n\t" \
  "ds_read_b64_tr_b16 v[8:9], %[ha] offset:544\n\t" \
  "ds_read_b64_tr_b16 v[10:11], %[ha] offset:512\n\t" \
  "ds_read_b64_tr_b16 v[12:13], %[ha] offset:800\n\t" \
  "ds_read_b64_tr_b16 v[14:15], %[ha] offset:768\n\t" \
  "s_waitcnt lgkmcnt(7)\n\t" \
  "ds_read_b64_tr_b16 v[16:17], %[ha] offset:1056\n\t" \
  "ds_read_b64_tr_b16 v[18:19], %[ha] offset:1024\n\t" \
  "ds_read_b64_tr_b16 v[20:21], %[ha] offset:1312\n\t" \
  "ds_read_b64_tr_b16 v[22:23], %[ha] offset:1280\n\t" \
  "ds_read_b64_tr_b16 v[24:25], %[ha] offset:1568\n\t" \
  "ds_read_b64_tr_b16 v[26:27], %[ha] offset:1536\n\t" \
  "ds_read_b64_tr_b16 v[28:29], %[ha] offset:1824\n\t" \
  "ds_read_b64_tr_b16 v[30:31], %[ha] offset:1792\n\t" \
  "s_waitcnt lgkmcnt(7)\n\t" \
  "ds_read_b64_tr_b16 v[32:33], %[ha] offset:2080\n\t" \
  "ds_read_b64_tr_b16 v[34:35], %[ha] offset:2048\n\t" \
  "ds_read_b64_tr_b16 v[36:37], %[ha] offset:2336\n\t" \
  "ds_read_b64_tr_b16 v[38:39], %[ha] offset:2304\n\t" \
  "ds_read_b64_tr_b16 v[40:41], %[ha] offset:2592\n\t" \
  "ds_read_b64_tr_b16 v[42:43], %[ha] offset:2560\n\t" \
  "ds_read_b64_tr_b16 v[44:45], %[ha] offset:2848\n\t" \
  "ds_read_b64_tr_b16 v[46:47], %[ha] offset:2816\n\t" \
  "s_waitcnt lgkmcnt(7)\n\t" \
  "ds_read_b64_tr_b16 v[48:49], %[ha] offset:3104\n\t" \
  "ds_read_b64_tr_b16 v[50:51], %[ha] offset:3072\n\t" \
  "ds_read_b64_tr_b16 v[52:53], %[ha] offset:3360\n\t" \
  "ds_read_b64_tr_b16 v[54:55], %[ha] offset:3328\n\t" \
  "ds_read_b64_tr_b16 v[56:57], %[ha] offset:3616\n\t" \
  "ds_read_b64_tr_b16 v[58:59], %[ha] offset:3584\n\t" \
  "ds_read_b64_tr_b16 v[60:61], %[ha] offset:3872\n\t" \
  "ds_read_b64_tr_b16 v[62:63], %[ha] offset:3840\n\t" \
  "s_waitcnt lgkmcnt(7)\n\t" \
  "s_waitcnt lgkmcnt(0)\n\t" \
  "s_cmp_lg_u32 %[nobar], 0\n\t" \
  "s_cbranch_scc1 L_A_%=\n\t" \
  "s_barrier\n\t" \
  "L_A_%=:\n\t" \
  "ds_read_b64 v[64:65], %[ua] offset:0\n\t" \
  "ds_read_b64 v[66:67], %[ua] offset:8\n\t" \
  "ds_read_b64 v[68:69], %[ua] offset:960\n\t" \
  "ds_read_b64 v[70:71], %[ua] offset:968\n\t" \
  "ds_read_b64 v[72:73], %[ua] offset:64\n\t" \
  "ds_read_b64 v[74:75], %[ua] offset:72\n\t" \
  "ds_read_u16_d16_hi v160, %[ke] offset:0\n\t" \
  "ds_read_b64 v[76:77], %[ua] offset:896\n\t" \
  "ds_read_b64 v[78:79], %[ua] offset:904\n\t" \
  "ds_read_b64 v[80:81], %[ua] offset:128\n\t" \
  "ds_read_b64 v[82:83], %[ua] offset:136\n\t" \
  "ds_read_u16_d16_hi v161, %[ke] offset:1152\n\t" \
  "s_waitcnt lgkmcnt(8)\n\t" \
  "v_mfma_f32_16x16x32_bf16 v[108:111], v[60:63], v[64:67], 0\n\t" \
  "ds_read_b64 v[84:85], %[ua] offset:832\n\t" \
  "ds_read_b64 v[86:87], %[ua] offset:840\n\t" \
  "v_mfma_f32_16x16x32_bf16 v[104:107], v[36:39], v[64:67], 0\n\t" \
  "v_mfma_f32_16x16x32_bf16 v[100:103], v[28:31], v[64:67], 0\n\t" \
  "v_mfma_f32_16x16x32_bf16 v[96:99], v[4:7], v[64:67], 0\n\t" \
  "v_mfma_f32_16x16x32_bf16 v[108:111], v[0:3], v[68:71], v[108:111]\n\t" \
  "ds_read_b64 v[88:89], %[ua] offset:192\n\t" \
  "ds_read_b64 v[90:91], %[ua] offset:200\n\t" \
  "s_waitcnt lgkmcnt(9)\n\t" \
  "v_mfma_f32_16x16x32_bf16 v[108:111], v[56:59], v[72:75], v[108:111]\n\t" \
  "ds_read_u16_d16_hi v162, %[ke] offset:2304\n\t" \
  "ds_read_b64 v[92:93], %[ua] offset:768\n\t" \
  "v_mfma_f32_16x16x32_bf16 v[104:107], v[32:35], v[72:75], v[104:107]\n\t" \
  "ds_read_b64 v[94:95], %[ua] offset:776\n\t" \
  "v_mfma_f32_16x16x32_bf16 v[100:103], v[24:27], v[72:75], v[100:103]\n\t" \
  "v_mfma_f32_16x16x32_bf16 v[96:99], v[0:3], v[72:75], v[96:99]\n\t" \
  "s_waitcnt lgkmcnt(8)\n\t" \
  "v_mfma_f32_16x16x32_bf16 v[108:111], v[4:7], v[76:79], v[108:111]\n\t" \
  "ds_read_b64 v[64:65], %[ua] offset:256\n\t" \
  "ds_read_b64 v[66:67], %[ua] offset:264\n\t" \
  "v_mfma_f32_16x16x32_bf16 v[108:111], v[52:55], v[80:83], v[108:111]\n\t" \
  "ds_read_b64 v[68:69], %[ua] offset:704\n\t" \
  "ds_read_b64 v[70:71], %[ua] offset:712\n\t" \
  "s_waitcnt lgkmcnt(9)\n\t" \
  "v_mfma_f32_16x16x32_bf16 v[104:107], v[28:31], v[80:83], v[104:107]\n\t" \
  "ds_read_b64 v[72:73], %[ua] offset:320\n\t" \
  "v_mfma_f32_16x16x32_bf16 v[100:103], v[20:23], v[80:83], v[100:103]\n\t" \
  "v_mfma_f32_16x16x32_bf16 v[108:111], v[8:11], v[84:87], v[108:111]\n\t" \
  "ds_read_b64 v[74:75], %[ua] offset:328\n\t" \
  "ds_read_u16_d16_hi v163, %[ke] offset:3456\n\t" \
  "s_waitcnt lgkmcnt(9)\n\t" \
  "v_mfma_f32_16x16x32_bf16 v[108:111], v[48:51], v[88:91], v[108:111]\n\t" \
  "ds_read_b64 v[76:77], %[ua] offset:640\n\t" \
  "ds_read_b64 v[78:79], %[ua] offset:648\n\t" \
  "v_mfma_f32_16x16x32_bf16 v[104:107], v[24:27], v[88:91], v[104:107]\n\t" \
  "v_mfma_f32_16x16x32_bf16 v[100:103], v[16:19], v[88:91], v[100:103]\n\t" \
  "ds_read_b64 v[80:81], %[ua] offset:384\n\t" \
  "s_waitcnt lgkmcnt(8)\n\t" \
  "v_mfma_f32_16x16x32_bf16 v[108:111], v[12:15], v[92:95], v[108:111]\n\t" \
  "ds_read_b64 v[82:83], %[ua] offset:392\n\t" \
  "ds_read_u16_d16_hi v164, %[ke] offset:384\n\t" \
  "v_mfma_f32_16x16x32_bf16 v[108:111], v[44:47], v[64:67], v[108:111]\n\t" \
  "ds_read_b64 v[84:85], %[ua] offset:576\n\t" \
  "ds_read_b64 v[86:87], %[ua] offset:584\n\t" \
  "v_mfma_f32_16x16x32_bf16 v[104:107], v[20:23], v[64:67], v[104:107]\n\t" \
  "v_mfma_f32_16x16x32_bf16 v[100:103], v[12:15], v[64:67], v[100:103]\n\t" \
  "s_waitcnt lgkmcnt(8)\n\t" \
  "v_mfma_f32_16x16x32_bf16 v[108:111], v[16:19], v[68:71], v[108:111]\n\t" \
  "ds_read_b64 v[88:89], %[ua] offset:448\n\t" \
  "ds_read_b64 v[90:91], %[ua] offset:456\n\t" \
  "v_mfma_f32_16x16x32_bf16 v[108:111], v[40:43], v[72:75], v[108:111]\n\t" \
  "ds_read_u16_d16_hi v165, %[ke] offset:1536\n\t" \
  "ds_read_b64 v[92:93], %[ua] offset:512\n\t" \
  "s_waitcnt lgkmcnt(9)\n\t" \
  "v_mfma_f32_16x16x32_bf16 v[104:107], v[16:19], v[72:75], v[104:107]\n\t" \
  "ds_read_b64 v[94:95], %[ua] offset:520\n\t" \
  "v_mfma_f32_16x16x32_bf16 v[100:103], v[8:11], v[72:75], v[100:103]\n\t" \
  "v_mfma_f32_16x16x32_bf16 v[108:111], v[20:23], v[76:79], v[108:111]\n\t" \
  "ds_read_b64 v[64:65], %[ua] offset:1152\n\t" \
  "ds_read_b64 v[66:67], %[ua] offset:1160\n\t" \
  "s_waitcnt lgkmcnt(9)\n\t" \
  "v_mfma_f32_16x16x32_bf16 v[108:111], v[36:39], v[80:83], v[108:111]\n\t" \
  "ds_read_u16_d16_hi v166, %[ke] offset:2688\n\t" \
  "ds_read_b64 v[68:69], %[ua] offset:2112\n\t" \
  "v_mfma_f32_16x16x32_bf16 v[104:107], v[12:15], v[80:83], v[104:107]\n\t" \
  "v_mfma_f32_16x16x32_bf16 v[100:103], v[4:7], v[80:83], v[100:103]\n\t" \
  "ds_read_b64 v[70:71], %[ua] offset:2120\n\t" \
  "s_waitcnt lgkmcnt(8)\n\t" \
  "v_mfma_f32_16x16x32_bf16 v[108:111], v[24:27], v[84:87], v[108:111]\n\t" \
  "ds_read_b64 v[72:73], %[ua] offset:1216\n\t" \
  "ds_read_b64 v[74:75], %[ua] offset:1224\n\t" \
  "v_mfma_f32_16x16x32_bf16 v[104:107], v[0:3], v[84:87], v[104:107]\n\t" \
  "v_mfma_f32_16x16x32_bf16 v[108:111], v[32:35], v[88:91], v[108:111]\n\t" \
  "ds_read_u16_d16_hi v167, %[ke] offset:3840\n\t" \
  "ds_read_b64 v[76:77], %[ua] offset:2048\n\t" \
  "v_mfma_f32_16x16x32_bf16 v[104:107], v[8:11], v[88:91], v[104:107]\n\t" \
  "s_waitcnt lgkmcnt(9)\n\t" \
  "v_mfma_f32_16x16x32_bf16 v[100:103], v[0:3], v[88:91], v[100:103]\n\t" \
  "ds_read_b64 v[78:79], %[ua] offset:2056\n\t" \
  "v_mfma_f32_16x16x32_bf16 v[108:111], v[28:31], v[92:95], v[108:111]\n\t" \
  "ds_read_b64 v[80:81], %[ua] offset:1280\n\t" \
  "ds_read_b64 v[82:83], %[ua] offset:1288\n\t" \
  "v_mfma_f32_16x16x32_bf16 v[104:107], v[4:7], v[92:95], v[104:107]\n\t" \
  "s_waitcnt lgkmcnt(9)\n\t" \
  "v_mfma_f32_16x16x32_bf16 v[124:127], v[60:63], v[64:67], 0\n\t" \
  "ds_read_u16_d16_hi v168, %[ke] offset:512\n\t" \
  "ds_read_b64 v[84:85], %[ua] offset:1984\n\t" \
  "v_mfma_f32_16x16x32_bf16 v[120:123], v[36:39], v[64:67], 0\n\t" \
  "v_mfma_f32_16x16x32_bf16 v[116:119], v[28:31], v[64:67], 0\n\t" \
  "ds_read_b64 v[86:87], %[ua] offset:1992\n\t" \
  "v_mfma_f32_16x16x32_bf16 v[112:115], v[4:7], v[64:67], 0\n\t" \
  "s_cbranch_scc1 L_B_%=\n\t" \
  "s_waitcnt vmcnt(0)\n\t" \
  "s_barrier\n\t" \
  "L_B_%=:\n\t" \
  "s_waitcnt lgkmcnt(8)\n\t" \
  "v_mfma_f32_16x16x32_bf16 v[124:127], v[0:3], v[68:71], v[124:127]\n\t" \
  "ds_read_b64 v[88:89], %[ua] offset:1344\n\t" \
  "ds_read_b64 v[90:91], %[ua] offset:1352\n\t" \
  "v_mfma_f32_16x16x32_bf16 v[124:127], v[56:59], v[72:75], v[124:127]\n\t" \
  "ds_read_b64 v[92:93], %[ua] offset:1920\n\t" \
  "ds_read_b64 v[94:95], %[ua] offset:1928\n\t" \
  "v_mfma_f32_16x16x32_bf16 v[120:123], v[32:35], v[72:75], v[120:123]\n\t" \
  "s_waitcnt lgkmcnt(9)\n\t" \
  "v_mfma_f32_16x16x32_bf16 v[116:119], v[24:27], v[72:75], v[116:119]\n\t" \
  "ds_read_b64 v[64:65], %[ua] offset:1408\n\t" \
  "v_mfma_f32_16x16x32_bf16 v[112:115], v[0:3], v[72:75], v[112:115]\n\t" \
  "v_mfma_f32_16x16x32_bf16 v[124:127], v[4:7], v[76:79], v[124:127]\n\t" \
  "ds_read_b64 v[66:67], %[ua] offset:1416\n\t" \
  "ds_read_u16_d16_hi v169, %[ke] offset:1664\n\t" \
  "s_waitcnt lgkmcnt(9)\n\t" \
  "v_mfma_f32_16x16x32_bf16 v[124:127], v[52:55], v[80:83], v[124:127]\n\t" \
  "ds_read_b64 v[68:69], %[ua] offset:1856\n\t" \
  "ds_read_b64 v[70:71], %[ua] offset:1864\n\t" \
  "v_mfma_f32_16x16x32_bf16 v[120:123], v[28:31], v[80:83], v[120:123]\n\t" \
  "v_mfma_f32_16x16x32_bf16 v[116:119], v[20:23], v[80:83], v[116:119]\n\t" \
  "ds_read_b64 v[72:73], %[ua] offset:1472\n\t" \
  "s_waitcnt lgkmcnt(8)\n\t" \
  "v_mfma_f32_16x16x32_bf16 v[124:127], v[8:11], v[84:87], v[124:127]\n\t" \
  "ds_read_b64 v[74:75], %[ua] offset:1480\n\t" \
  "ds_read_u16_d16_hi v170, %[ke] offset:2816\n\t" \
  "v_mfma_f32_16x16x32_bf16 v[124:127], v[48:51], v[88:91], v[124:127]\n\t" \
  "ds_read_b64 v[76:77], %[ua] offset:1792\n\t" \
  "ds_read_b64 v[78:79], %[ua] offset:1800\n\t" \
  "v_mfma_f32_16x16x32_bf16 v[120:123], v[24:27], v[88:91], v[120:123]\n\t" \
  "v_mfma_f32_16x16x32_bf16 v[116:119], v[16:19], v[88:91], v[116:119]\n\t" \
  "s_waitcnt lgkmcnt(8)\n\t" \
  "v_mfma_f32_16x16x32_bf16 v[124:127], v[12:15], v[92:95], v[124:127]\n\t" \
  "ds_read_b64 v[80:81], %[ua] offset:1536\n\t" \
  "ds_read_b64 v[82:83], %[ua] offset:1544\n\t" \
  "v_mfma_f32_16x16x32_bf16 v[124:127], v[44:47], v[64:67], v[124:127]\n\t" \
  "ds_read_b64 v[84:85], %[ua] offset:1728\n\t" \
  "ds_read_b64 v[86:87], %[ua] offset:1736\n\t" \
  "s_waitcnt lgkmcnt(9)\n\t" \
  "v_mfma_f32_16x16x32_bf16 v[120:123], v[20:23], v[64:67], v[120:123]\n\t" \
  "ds_read_u16_d16_hi v171, %[ke] offset:3968\n\t" \
  "v_mfma_f32_16x16x32_bf16 v[116:119], v[12:15], v[64:67], v[116:119]\n\t" \
  "v_mfma_f32_16x16x32_bf16 v[124:127], v[16:19], v[68:71], v[124:127]\n\t" \
  "ds_read_b64 v[88:89], %[ua] offset:1600\n\t" \
  "ds_read_b64 v[90:91], %[ua] offset:1608\n\t" \
  "s_waitcnt lgkmcnt(9)\n\t" \
  "v_mfma_f32_16x16x32_bf16 v[124:127], v[40:43], v[72:75], v[124:127]\n\t" \
  "ds_read_b64 v[92:93], %[ua] offset:1664\n\t" \
  "ds_read_b64 v[94:95], %[ua] offset:1672\n\t" \
  "v_mfma_f32_16x16x32_bf16 v[120:123], v[16:19], v[72:75], v[120:123]\n\t" \
  "v_mfma_f32_16x16x32_bf16 v[116:119], v[8:11], v[72:75], v[116:119]\n\t" \
  "ds_read_u16_d16_hi v172, %[ke] offset:896\n\t" \
  "s_waitcnt lgkmcnt(8)\n\t" \
  "v_mfma_f32_16x16x32_bf16 v[124:127], v[20:23], v[76:79], v[124:127]\n\t" \
  "ds_read_b64 v[64:65], %[ua] offset:2304\n\t" \
  "ds_read_b64 v[66:67], %[ua] offset:2312\n\t" \
  "v_mfma_f32_16x16x32_bf16 v[124:127], v[36:39], v[80:83], v[124:127]\n\t" \
  "ds_read_u16_d16_hi v173, %[ke] offset:2048\n\t" \
  "ds_read_b64 v[68:69], %[ua] offset:3264\n\t" \
  "v_mfma_f32_16x16x32_bf16 v[120:123], v[12:15], v[80:83], v[120:123]\n\t" \
  "v_mfma_f32_16x16x32_bf16 v[116:119], v[4:7], v[80:83], v[116:119]\n\t" \
  "s_waitcnt lgkmcnt(9)\n\t" \
  "v_mfma_f32_16x16x32_bf16 v[124:127], v[24:27], v[84:87], v[124:127]\n\t" \
  "ds_read_b64 v[70:71], %[ua] offset:3272\n\t" \
  "ds_read_b64 v[72:73], %[ua] offset:2368\n\t" \
  "v_mfma_f32_16x16x32_bf16 v[120:123], v[0:3], v[84:87], v[120:123]\n\t" \
  "ds_read_b64 v[74:75], %[ua] offset:2376\n\t" \
  "s_waitcnt lgkmcnt(8)\n\t" \
  "v_mfma_f32_16x16x32_bf16 v[124:127], v[32:35], v[88:91], v[124:127]\n\t" \
  "ds_read_u16_d16_hi v174, %[ke] offset:3200\n\t" \
  "ds_read_b64 v[76:77], %[ua] offset:3200\n\t" \
  "v_mfma_f32_16x16x32_bf16 v[120:123], v[8:11], v[88:91], v[120:123]\n\t" \
  "v_mfma_f32_16x16x32_bf16 v[116:119], v[0:3], v[88:91], v[116:119]\n\t" \
  "v_mfma_f32_16x16x32_bf16 v[124:127], v[28:31], v[92:95], v[124:127]\n\t" \
  "ds_read_b64 v[78:79], %[ua] offset:3208\n\t" \
  "ds_read_b64 v[80:81], %[ua] offset:2432\n\t" \
  "s_waitcnt lgkmcnt(9)\n\t" \
  "v_mfma_f32_16x16x32_bf16 v[120:123], v[4:7], v[92:95], v[120:123]\n\t" \
  "ds_read_b64 v[82:83], %[ua] offset:2440\n\t" \
  "v_mfma_f32_16x16x32_bf16 v[140:143], v[60:63], v[64:67], 0\n\t" \
  "ds_read_b64 v[84:85], %[ua] offset:3136\n\t" \
  "ds_read_b64 v[86:87], %[ua] offset:3144\n\t" \
  "v_mfma_f32_16x16x32_bf16 v[136:139], v[36:39], v[64:67], 0\n\t" \
  "v_mfma_f32_16x16x32_bf16 v[132:135], v[28:31], v[64:67], 0\n\t" \
  "s_waitcnt lgkmcnt(9)\n\t" \
  "v_mfma_f32_16x16x32_bf16 v[128:131], v[4:7], v[64:67], 0\n\t" \
  "ds_read_b64 v[88:89], %[ua] offset:2496\n\t" \
  "v_mfma_f32_16x16x32_bf16 v[140:143], v[0:3], v[68:71], v[140:143]\n\t" \
  "ds_read_b64 v[90:91], %[ua] offset:2504\n\t" \
  "ds_read_u16_d16_hi v175, %[ke] offset:4352\n\t" \
  "s_waitcnt lgkmcnt(9)\n\t" \
  "v_mfma_f32_16x16x32_bf16 v[140:143], v[56:59], v[72:75], v[140:143]\n\t" \
  "ds_read_b64 v[92:93], %[ua] offset:3072\n\t" \
  "ds_read_b64 v[94:95], %[ua] offset:3080\n\t" \
  "v_mfma_f32_16x16x32_bf16 v[136:139], v[32:35], v[72:75], v[136:139]\n\t" \
  "v_mfma_f32_16x16x32_bf16 v[132:135], v[24:27], v[72:75], v[132:135]\n\t" \
  "v_add_f32 v97, v97, v112\n\t" \
  "v_mfma_f32_16x16x32_bf16 v[128:131], v[0:3], v[72:75], v[128:131]\n\t" \
  "ds_read_b64 v[64:65], %[ua] offset:2560\n\t" \
  "v_add_f32 v98, v98, v113\n\t" \
  "s_waitcnt lgkmcnt(8)\n\t" \
  "v_mfma_f32_16x16x32_bf16 v[140:143], v[4:7], v[76:79], v[140:143]\n\t" \
  "ds_read_b64 v[66:67], %[ua] offset:2568\n\t" \
  "ds_read_b64 v[68:69], %[ua] offset:3008\n\t" \
  "v_add_f32 v99, v99, v114\n\t" \
  "v_mfma_f32_16x16x32_bf16 v[140:143], v[52:55], v[80:83], v[140:143]\n\t" \
  "ds_read_b64 v[70:71], %[ua] offset:3016\n\t" \
  "ds_read_b64 v[72:73], %[ua] offset:2624\n\t" \
  "v_add_f32 v101, v101, v116\n\t" \
  "v_mfma_f32_16x16x32_bf16 v[136:139], v[28:31], v[80:83], v[136:139]\n\t" \
  "v_add_f32 v102, v102, v117\n\t" \
  "v_mfma_f32_16x16x32_bf16 v[132:135], v[20:23], v[80:83], v[132:135]\n\t" \
  "v_add_f32 v103, v103, v118\n\t" \
  "s_waitcnt lgkmcnt(8)\n\t" \
  "v_mfma_f32_16x16x32_bf16 v[140:143], v[8:11], v[84:87], v[140:143]\n\t" \
  "ds_read_b64 v[74:75], %[ua] offset:2632\n\t" \
  "ds_read_b64 v[76:77], %[ua] offset:2944\n\t" \
  "v_add_f32 v105, v105, v120\n\t" \
  "v_mfma_f32_16x16x32_bf16 v[140:143], v[48:51], v[88:91], v[140:143]\n\t" \
  "ds_read_b64 v[78:79], %[ua] offset:2952\n\t" \
  "ds_read_b64 v[80:81], %[ua] offset:2688\n\t" \
  "v_add_f32 v106, v106, v121\n\t" \
  "s_waitcnt lgkmcnt(9)\n\t" \
  "v_mfma_f32_16x16x32_bf16 v[136:139], v[24:27], v[88:91], v[136:139]\n\t" \
  "ds_read_b64 v[82:83], %[ua] offset:2696\n\t" \
  "v_add_f32 v107, v107, v122\n\t" \
  "v_mfma_f32_16x16x32_bf16 v[132:135], v[16:19], v[88:91], v[132:135]\n\t" \
  "v_add_f32 v109, v109, v124\n\t" \
  "v_mfma_f32_16x16x32_bf16 v[140:143], v[12:15], v[92:95], v[140:143]\n\t" \
  "ds_read_b64 v[84:85], %[ua] offset:2880\n\t" \
  "ds_read_b64 v[86:87], %[ua] offset:2888\n\t" \
  "v_add_f32 v110, v110, v125\n\t" \
  "s_waitcnt lgkmcnt(8)\n\t" \
  "v_mfma_f32_16x16x32_bf16 v[140:143], v[44:47], v[64:67], v[140:143]\n\t" \
  "ds_read_b64 v[88:89], %[ua] offset:2752\n\t" \
  "ds_read_b64 v[90:91], %[ua] offset:2760\n\t" \
  "v_add_f32 v111, v111, v126\n\t" \
  "v_mfma_f32_16x16x32_bf16 v[136:139], v[20:23], v[64:67], v[136:139]\n\t" \
  "v_mfma_f32_16x16x32_bf16 v[132:135], v[12:15], v[64:67], v[132:135]\n\t" \
  "v_mfma_f32_16x16x32_bf16 v[140:143], v[16:19], v[68:71], v[140:143]\n\t" \
  "ds_read_b64 v[92:93], %[ua] offset:2816\n\t" \
  "ds_read_b64 v[94:95], %[ua] offset:2824\n\t" \
  "s_waitcnt lgkmcnt(8)\n\t" \
  "v_mfma_f32_16x16x32_bf16 v[140:143], v[40:43], v[72:75], v[140:143]\n\t" \
  "ds_read_b64 v[64:65], %[ua] offset:3456\n\t" \
  "ds_read_b64 v[66:67], %[ua] offset:3464\n\t" \
  "v_mfma_f32_16x16x32_bf16 v[136:139], v[16:19], v[72:75], v[136:139]\n\t" \
  "v_mfma_f32_16x16x32_bf16 v[132:135], v[8:11], v[72:75], v[132:135]\n\t" \
  "v_mfma_f32_16x16x32_bf16 v[140:143], v[20:23], v[76:79], v[140:143]\n\t" \
  "ds_read_b64 v[68:69], %[ua] offset:4416\n\t" \
  "ds_read_b64 v[70:71], %[ua] offset:4424\n\t" \
  "s_waitcnt lgkmcnt(8)\n\t" \
  "v_mfma_f32_16x16x32_bf16 v[140:143], v[36:39], v[80:83], v[140:143]\n\t" \
  "ds_read_b64 v[72:73], %[ua] offset:3520\n\t" \
  "ds_read_b64 v[74:75], %[ua] offset:3528\n\t" \
  "v_mfma_f32_16x16x32_bf16 v[136:139], v[12:15], v[80:83], v[136:139]\n\t" \
  "v_mfma_f32_16x16x32_bf16 v[132:135], v[4:7], v[80:83], v[132:135]\n\t" \
  "v_mfma_f32_16x16x32_bf16 v[140:143], v[24:27], v[84:87], v[140:143]\n\t" \
  "ds_read_b64 v[76:77], %[ua] offset:4352\n\t" \
  "ds_read_b64 v[78:79], %[ua] offset:4360\n\t" \
  "v_mfma_f32_16x16x32_bf16 v[136:139], v[0:3], v[84:87], v[136:139]\n\t" \
  "s_waitcnt lgkmcnt(8)\n\t" \
  "v_mfma_f32_16x16x32_bf16 v[140:143], v[32:35], v[88:91], v[140:143]\n\t" \
  "ds_read_b64 v[80:81], %[ua] offset:3584\n\t" \
  "ds_read_b64 v[82:83], %[ua] offset:3592\n\t" \
  "v_mfma_f32_16x16x32_bf16 v[136:139], v[8:11], v[88:91], v[136:139]\n\t" \
  "v_mfma_f32_16x16x32_bf16 v[132:135], v[0:3], v[88:91], v[132:135]\n\t" \
  "v_mfma_f32_16x16x32_bf16 v[140:143], v[28:31], v[92:95], v[140:143]\n\t" \
  "ds_read_b64 v[84:85], %[ua] offset:4288\n\t" \
  "ds_read_b64 v[86:87], %[ua] offset:4296\n\t" \
  "v_mfma_f32_16x16x32_bf16 v[136:139], v[4:7], v[92:95], v[136:139]\n\t" \
  "s_waitcnt lgkmcnt(8)\n\t" \
  "v_mfma_f32_16x16x32_bf16 v[156:159], v[60:63], v[64:67], 0\n\t" \
  "ds_read_b64 v[88:89], %[ua] offset:3648\n\t" \
  "ds_read_b64 v[90:91], %[ua] offset:3656\n\t" \
  "v_mfma_f32_16x16x32_bf16 v[152:155], v[36:39], v[64:67], 0\n\t" \
  "v_mfma_f32_16x16x32_bf16 v[148:151], v[28:31], v[64:67], 0\n\t" \
  "v_mfma_f32_16x16x32_bf16 v[144:147], v[4:7], v[64:67], 0\n\t" \
  "v_mfma_f32_16x16x32_bf16 v[156:159], v[0:3], v[68:71], v[156:159]\n\t" \
  "ds_read_b64 v[92:93], %[ua] offset:4224\n\t" \
  "ds_read_b64 v[94:95], %[ua] offset:4232\n\t" \
  "s_waitcnt lgkmcnt(8)\n\t" \
  "v_mfma_f32_16x16x32_bf16 v[156:159], v[56:59], v[72:75], v[156:159]\n\t" \
  "ds_read_b64 v[64:65], %[ua] offset:3712\n\t" \
  "ds_read_b64 v[66:67], %[ua] offset:3720\n\t" \
  "v_mfma_f32_16x16x32_bf16 v[152:155], v[32:35], v[72:75], v[152:155]\n\t" \
  "v_mfma_f32_16x16x32_bf16 v[148:151], v[24:27], v[72:75], v[148:151]\n\t" \
  "v_add_f32 v98, v98, v128\n\t" \
  "v_mfma_f32_16x16x32_bf16 v[144:147], v[0:3], v[72:75], v[144:147]\n\t" \
  "v_add_f32 v99, v99, v129\n\t" \
  "v_mfma_f32_16x16x32_bf16 v[156:159], v[4:7], v[76:79], v[156:159]\n\t" \
  "ds_read_b64 v[68:69], %[ua] offset:4160\n\t" \
  "ds_read_b64 v[70:71], %[ua] offset:4168\n\t" \
  "v_add_f32 v130, v130, v115\n\t" \
  "s_waitcnt lgkmcnt(8)\n\t" \
  "v_mfma_f32_16x16x32_bf16 v[156:159], v[52:55], v[80:83], v[156:159]\n\t" \
  "ds_read_b64 v[72:73], %[ua] offset:3776\n\t" \
  "ds_read_b64 v[74:75], %[ua] offset:3784\n\t" \
  "v_add_f32 v102, v102, v132\n\t" \
  "v_mfma_f32_16x16x32_bf16 v[152:155], v[28:31], v[80:83], v[152:155]\n\t" \
  "v_add_f32 v103, v103, v133\n\t" \
  "v_mfma_f32_16x16x32_bf16 v[148:151], v[20:23], v[80:83], v[148:151]\n\t" \
  "v_add_f32 v134, v134, v119\n\t" \
  "v_mfma_f32_16x16x32_bf16 v[156:159], v[8:11], v[84:87], v[156:159]\n\t" \
  "ds_read_b64 v[76:77], %[ua] offset:4096\n\t" \
  "ds_read_b64 v[78:79], %[ua] offset:4104\n\t" \
  "v_add_f32 v106, v106, v136\n\t" \
  "s_waitcnt lgkmcnt(8)\n\t" \
  "v_mfma_f32_16x16x32_bf16 v[156:159], v[48:51], v[88:91], v[156:159]\n\t" \
  "ds_read_b64 v[80:81], %[ua] offset:3840\n\t" \
  "ds_read_b64 v[82:83], %[ua] offset:3848\n\t" \
  "v_add_f32 v107, v107, v137\n\t" \
  "v_mfma_f32_16x16x32_bf16 v[152:155], v[24:27], v[88:91], v[152:155]\n\t" \
  "v_add_f32 v138, v138, v123\n\t" \
  "v_mfma_f32_16x16x32_bf16 v[148:151], v[16:19], v[88:91], v[148:151]\n\t" \
  "v_add_f32 v110, v110, v140\n\t" \
  "v_mfma_f32_16x16x32_bf16 v[156:159], v[12:15], v[92:95], v[156:159]\n\t" \
  "ds_read_b64 v[84:85], %[ua] offset:4032\n\t" \
  "ds_read_b64 v[86:87], %[ua] offset:4040\n\t" \
  "v_add_f32 v111, v111, v141\n\t" \
  "s_waitcnt lgkmcnt(8)\n\t" \
  "v_mfma_f32_16x16x32_bf16 v[156:159], v[44:47], v[64:67], v[156:159]\n\t" \
  "ds_read_b64 v[88:89], %[ua] offset:3904\n\t" \
  "ds_read_b64 v[90:91], %[ua] offset:3912\n\t" \
  "v_add_f32 v142, v142, v127\n\t" \
  "v_mfma_f32_16x16x32_bf16 v[152:155], v[20:23], v[64:67], v[152:155]\n\t" \
  "v_fmac_f32 v96, %[dv], v160\n\t" \
  "v_mfma_f32_16x16x32_bf16 v[148:151], v[12:15], v[64:67], v[148:151]\n\t" \
  "v_fmac_f32 v97, %[dv], v161\n\t" \
  "v_mfma_f32_16x16x32_bf16 v[156:159], v[16:19], v[68:71], v[156:159]\n\t" \
  "ds_read_b64 v[92:93], %[ua] offset:3968\n\t" \
  "ds_read_b64 v[94:95], %[ua] offset:3976\n\t" \
  "v_fmac_f32 v98, %[dv], v162\n\t" \
  "s_waitcnt lgkmcnt(8)\n\t" \
  "v_mfma_f32_16x16x32_bf16 v[156:159], v[40:43], v[72:75], v[156:159]\n\t" \
  "v_fmac_f32 v99, %[dv], v163\n\t" \
  "v_mfma_f32_16x16x32_bf16 v[152:155], v[16:19], v[72:75], v[152:155]\n\t" \
  "v_fmac_f32 v100, %[dv], v164\n\t" \
  "v_mfma_f32_16x16x32_bf16 v[148:151], v[8:11], v[72:75], v[148:151]\n\t" \
  "v_fmac_f32 v101, %[dv], v165\n\t" \
  "v_mfma_f32_16x16x32_bf16 v[156:159], v[20:23], v[76:79], v[156:159]\n\t" \
  "v_fmac_f32 v102, %[dv], v166\n\t" \
  "s_waitcnt lgkmcnt(4)\n\t" \
  "v_mfma_f32_16x16x32_bf16 v[156:159], v[36:39], v[80:83], v[156:159]\n\t" \
  "v_fmac_f32 v103, %[dv], v167\n\t" \
  "v_mfma_f32_16x16x32_bf16 v[152:155], v[12:15], v[80:83], v[152:155]\n\t" \
  "v_fmac_f32 v104, %[dv], v168\n\t" \
  "v_mfma_f32_16x16x32_bf16 v[148:151], v[4:7], v[80:83], v[148:151]\n\t" \
  "v_fmac_f32 v105, %[dv], v169\n\t" \
  "v_mfma_f32_16x16x32_bf16 v[156:159], v[24:27], v[84:87], v[156:159]\n\t" \
  "v_fmac_f32 v106, %[dv], v170\n\t" \
  "v_mfma_f32_16x16x32_bf16 v[152:155], v[0:3], v[84:87], v[152:155]\n\t" \
  "v_fmac_f32 v107, %[dv], v171\n\t" \
  "s_waitcnt lgkmcnt(0)\n\t" \
  "v_mfma_f32_16x16x32_bf16 v[156:159], v[32:35], v[88:91], v[156:159]\n\t" \
  "v_fmac_f32 v108, %[dv], v172\n\t" \
  "v_mfma_f32_16x16x32_bf16 v[152:155], v[8:11], v[88:91], v[152:155]\n\t" \
  "v_fmac_f32 v109, %[dv], v173\n\t" \
  "v_mfma_f32_16x16x32_bf16 v[148:151], v[0:3], v[88:91], v[148:151]\n\t" \
  "v_fmac_f32 v110, %[dv], v174\n\t" \
  "v_mfma_f32_16x16x32_bf16 v[156:159], v[28:31], v[92:95], v[156:159]\n\t" \
  "v_fmac_f32 v111, %[dv], v175\n\t" \
  "v_mfma_f32_16x16x32_bf16 v[152:155], v[4:7], v[92:95], v[152:155]\n\t" \
  "s_nop 7\n\t" \
  "v_add_f32 v99, v99, v144\n\t" \
  "v_add_f32 v145, v145, v130\n\t" \
  "v_add_f32 v146, v146, v131\n\t" \
  "v_add_f32 v103, v103, v148\n\t" \
  "v_add_f32 v149, v149, v134\n\t" \
  "v_add_f32 v150, v150, v135\n\t" \
  "v_add_f32 v107, v107, v152\n\t" \
  "v_add_f32 v153, v153, v138\n\t" \
  "v_add_f32 v154, v154, v139\n\t" \
  "v_add_f32 v111, v111, v156\n\t" \
  "v_add_f32 v157, v157, v142\n\t" \
  "v_add_f32 v158, v158, v143\n\t" \
  "ds_bpermute_b32 v0, %[src], v145\n\t" \
  "ds_bpermute_b32 v1, %[src], v146\n\t" \
  "ds_bpermute_b32 v2, %[src], v147\n\t" \
  "ds_bpermute_b32 v4, %[src], v149\n\t" \
  "ds_bpermute_b32 v5, %[src], v150\n\t" \
  "ds_bpermute_b32 v6, %[src], v151\n\t" \
  "ds_bpermute_b32 v8, %[src], v153\n\t" \
  "ds_bpermute_b32 v9, %[src], v154\n\t" \
  "ds_bpermute_b32 v10, %[src], v155\n\t" \
  "ds_bpermute_b32 v12, %[src], v157\n\t" \
  "ds_bpermute_b32 v13, %[src], v158\n\t" \
  "ds_bpermute_b32 v14, %[src], v159\n\t" \
  "s_brev_b64 exec, 1\n\t" \
  "s_waitcnt lgkmcnt(8)\n\t" \
  "ds_write_b128 %[cx], v[144:147] offset:16\n\t" \
  "ds_write_b128 %[cx], v[148:151] offset:64\n\t" \
  "ds_write_b128 %[cx], v[152:155] offset:80\n\t" \
  "ds_write_b128 %[cx], v[156:159] offset:128\n\t" \
  "s_mov_b64 exec, -2\n\t" \
  "s_waitcnt lgkmcnt(0)\n\t" \
  "s_barrier\n\t" \
  "v_add_f32 v96, v96, v0\n\t" \
  "v_add_f32 v97, v97, v1\n\t" \
  "v_add_f32 v98, v98, v2\n\t" \
  "v_add_f32 v100, v100, v4\n\t" \
  "v_add_f32 v101, v101, v5\n\t" \
  "v_add_f32 v102, v102, v6\n\t" \
  "v_add_f32 v104, v104, v8\n\t" \
  "v_add_f32 v105, v105, v9\n\t" \
  "v_add_f32 v106, v106, v10\n\t" \
  "v_add_f32 v108, v108, v12\n\t" \
  "v_add_f32 v109, v109, v13\n\t" \
  "v_add_f32 v110, v110, v14\n\t" \
  "s_mov_b64 exec, 1\n\t" \
  "ds_read_b128 v[4:7], %[cx] offset:48\n\t" \
  "ds_read_b128 v[8:11], %[cx] offset:64\n\t" \
  "ds_read_b128 v[12:15], %[cx] offset:112\n\t" \
  "s_waitcnt lgkmcnt(0)\n\t" \
  "v_add_f32 v100, v100, v5\n\t" \
  "v_add_f32 v101, v101, v6\n\t" \
  "v_add_f32 v102, v102, v7\n\t" \
  "v_add_f32 v104, v104, v9\n\t" \
  "v_add_f32 v105, v105, v10\n\t" \
  "v_add_f32 v106, v106, v11\n\t" \
  "v_add_f32 v108, v108, v13\n\t" \
  "v_add_f32 v109, v109, v14\n\t" \
  "v_add_f32 v110, v110, v15\n\t" \
  "s_mov_b64 exec, -1\n\t" \
  "v_cvt_pk_bf16_f32 v96, v96, v97\n\t" \
  "v_cvt_pk_bf16_f32 v97, v98, v99\n\t" \
  "v_cvt_pk_bf16_f32 v100, v100, v101\n\t" \
  "v_cvt_pk_bf16_f32 v101, v102, v103\n\t" \
  "v_cvt_pk_bf16_f32 v104, v104, v105\n\t" \
  "v_cvt_pk_bf16_f32 v105, v106, v107\n\t" \
  "v_cvt_pk_bf16_f32 v108, v108, v109\n\t" \
  "v_cvt_pk_bf16_f32 v109, v110, v111\n\t" \
  "ds_bpermute_b32 v0, %[src2], v96\n\t" \
  "ds_bpermute_b32 v1, %[src2], v97\n\t" \
  "ds_bpermute_b32 v2, %[src2], v100\n\t" \
  "ds_bpermute_b32 v3, %[src2], v101\n\t" \
  "ds_bpermute_b32 v4, %[src2], v104\n\t" \
  "ds_bpermute_b32 v5, %[src2], v105\n\t" \
  "ds_bpermute_b32 v6, %[src2], v108\n\t" \
  "ds_bpermute_b32 v7, %[src2], v109\n\t" \
  "s_mov_b32 s40, 0\n\t" \
  "s_waitcnt lgkmcnt(6)\n\t" \
  "buffer_store_dwordx2 v[0:1], %[yo], %[rs], s40 offen sc1\n\t" \
  "s_mov_b32 s40, 884736\n\t" \
  "s_waitcnt lgkmcnt(4)\n\t" \
  "buffer_store_dwordx2 v[2:3], %[yo], %[rs], s40 offen sc1\n\t" \
  "s_mov_b32 s40, 1179648\n\t" \
  "s_waitcnt lgkmcnt(2)\n\t" \
  "buffer_store_dwordx2 v[4:5], %[yo], %[rs], s40 offen sc1\n\t" \
  "s_mov_b32 s40, 2064384\n\t" \
  "s_waitcnt lgkmcnt(0)\n\t" \
  "buffer_store_dwordx2 v[6:7], %[yo], %[rs], s40 offen sc1\n\t" \
  ""
#define CONV_1 \
  "v_mov_b32 v160, 0\n\t" \
  "v_mov_b32 v161, 0\n\t" \
  "v_mov_b32 v162, 0\n\t" \
  "v_mov_b32 v163, 0\n\t" \
  "v_mov_b32 v164, 0\n\t" \
  "v_mov_b32 v165, 0\n\t" \
  "v_mov_b32 v166, 0\n\t" \
  "v_mov_b32 v167, 0\n\t" \
  "v_mov_b32 v168, 0\n\t" \
  "v_mov_b32 v169, 0\n\t" \
  "v_mov_b32 v170, 0\n\t" \
  "v_mov_b32 v171, 0\n\t" \
  "v_mov_b32 v172, 0\n\t" \
  "v_mov_b32 v173, 0\n\t" \
  "v_mov_b32 v174, 0\n\t" \
  "v_mov_b32 v175, 0\n\t" \
  "ds_read_b64_tr_b16 v[0:1], %[ha] offset:32\n\t" \
  "ds_read_b64_tr_b16 v[2:3], %[ha] offset:0\n\t" \
  "ds_read_b64_tr_b16 v[4:5], %[ha] offset:288\n\t" \
  "ds_read_b64_tr_b16 v[6:7], %[ha] offset:256\n\t" \
  "ds_read_b64_tr_b16 v[8:9], %[ha] offset:544\n\t" \
  "ds_read_b64_tr_b16 v[10:11], %[ha] offset:512\n\t" \
  "ds_read_b64_tr_b16 v[12:13], %[ha] offset:800\n\t" \
  "ds_read_b64_tr_b16 v[14:15], %[ha] offset:768\n\t" \
  "s_waitcnt lgkmcnt(7)\n\t" \
  "ds_read_b64_tr_b16 v[16:17], %[ha] offset:1056\n\t" \
  "ds_read_b64_tr_b16 v[18:19], %[ha] offset:1024\n\t" \
  "ds_read_b64_tr_b16 v[20:21], %[ha] offset:1312\n\t" \
  "ds_read_b64_tr_b16 v[22:23], %[ha] offset:1280\n\t" \
  "ds_read_b64_tr_b16 v[24:25], %[ha] offset:1568\n\t" \
  "ds_read_b64_tr_b16 v[26:27], %[ha] offset:1536\n\t" \
  "ds_read_b64_tr_b16 v[28:29], %[ha] offset:1824\n\t" \
  "ds_read_b64_tr_b16 v[30:31], %[ha] offset:1792\n\t" \
  "s_waitcnt lgkmcnt(7)\n\t" \
  "ds_read_b64_tr_b16 v[32:33], %[ha] offset:2080\n\t" \
  "ds_read_b64_tr_b16 v[34:35], %[ha] offset:2048\n\t" \
  "ds_read_b64_tr_b16 v[36:37], %[ha] offset:2336\n\t" \
  "ds_read_b64_tr_b16 v[38:39], %[ha] offset:2304\n\t" \
  "ds_read_b64_tr_b16 v[40:41], %[ha] offset:2592\n\t" \
  "ds_read_b64_tr_b16 v[42:43], %[ha] offset:2560\n\t" \
  "ds_read_b64_tr_b16 v[44:45], %[ha] offset:2848\n\t" \
  "ds_read_b64_tr_b16 v[46:47], %[ha] offset:2816\n\t" \
  "s_waitcnt lgkmcnt(7)\n\t" \
  "ds_read_b64_tr_b16 v[48:49], %[ha] offset:3104\n\t" \
  "ds_read_b64_tr_b16 v[50:51], %[ha] offset:3072\n\t" \
  "ds_read_b64_tr_b16 v[52:53], %[ha] offset:3360\n\t" \
  "ds_read_b64_tr_b16 v[54:55], %[ha] offset:3328\n\t" \
  "s_waitcnt lgkmcnt(0)\n\t" \
  "s_cmp_lg_u32 %[nobar], 0\n\t" \
  "s_cbranch_scc1 L_A_%=\n\t" \
  "s_barrier\n\t" \
  "L_A_%=:\n\t" \
  "ds_read_b64 v[64:65], %[ua] offset:0\n\t" \
  "ds_read_b64 v[66:67], %[ua] offset:8\n\t" \
  "ds_read_b64 v[68:69], %[ua] offset:832\n\t" \
  "ds_read_b64 v[70:71], %[ua] offset:840\n\t" \
  "ds_read_b64 v[72:73], %[ua] offset:64\n\t" \
  "ds_read_b64 v[74:75], %[ua] offset:72\n\t" \
  "ds_read_u16_d16_hi v160, %[ke] offset:128\n\t" \
  "ds_read_b64 v[76:77], %[ua] offset:768\n\t" \
  "ds_read_b64 v[78:79], %[ua] offset:776\n\t" \
  "ds_read_b64 v[80:81], %[ua] offset:128\n\t" \
  "ds_read_b64 v[82:83], %[ua] offset:136\n\t" \
  "ds_read_u16_d16_hi v161, %[ke] offset:1280\n\t" \
  "s_waitcnt lgkmcnt(8)\n\t" \
  "v_mfma_f32_16x16x32_bf16 v[108:111], v[52:55], v[64:67], 0\n\t" \
  "ds_read_b64 v[84:85], %[ua] offset:704\n\t" \
  "ds_read_b64 v[86:87], %[ua] offset:712\n\t" \
  "v_mfma_f32_16x16x32_bf16 v[104:107], v[44:47], v[64:67], 0\n\t" \
  "v_mfma_f32_16x16x32_bf16 v[100:103], v[20:23], v[64:67], 0\n\t" \
  "v_mfma_f32_16x16x32_bf16 v[96:99], v[12:15], v[64:67], 0\n\t" \
  "v_mfma_f32_16x16x32_bf16 v[108:111], v[0:3], v[68:71], v[108:111]\n\t" \
  "ds_read_u16_d16_hi v162, %[ke] offset:2432\n\t" \
  "ds_read_b64 v[88:89], %[ua] offset:192\n\t" \
  "s_waitcnt lgkmcnt(9)\n\t" \
  "v_mfma_f32_16x16x32_bf16 v[108:111], v[48:51], v[72:75], v[108:111]\n\t" \
  "ds_read_b64 v[90:91], %[ua] offset:200\n\t" \
  "ds_read_u16_d16_hi v163, %[ke] offset:3584\n\t" \
  "v_mfma_f32_16x16x32_bf16 v[104:107], v[40:43], v[72:75], v[104:107]\n\t" \
  "ds_read_b64 v[92:93], %[ua] offset:640\n\t" \
  "v_mfma_f32_16x16x32_bf16 v[100:103], v[16:19], v[72:75], v[100:103]\n\t" \
  "v_mfma_f32_16x16x32_bf16 v[96:99], v[8:11], v[72:75], v[96:99]\n\t" \
  "s_waitcnt lgkmcnt(8)\n\t" \
  "v_mfma_f32_16x16x32_bf16 v[108:111], v[4:7], v[76:79], v[108:111]\n\t" \
  "ds_read_b64 v[94:95], %[ua] offset:648\n\t" \
  "ds_read_b64 v[64:65], %[ua] offset:256\n\t" \
  "v_mfma_f32_16x16x32_bf16 v[108:111], v[44:47], v[80:83], v[108:111]\n\t" \
  "ds_read_b64 v[66:67], %[ua] offset:264\n\t" \
  "ds_read_u16_d16_hi v164, %[ke] offset:256\n\t" \
  "s_waitcnt lgkmcnt(9)\n\t" \
  "v_mfma_f32_16x16x32_bf16 v[104:107], v[36:39], v[80:83], v[104:107]\n\t" \
  "ds_read_b64 v[68:69], %[ua] offset:576\n\t" \
  "v_mfma_f32_16x16x32_bf16 v[100:103], v[12:15], v[80:83], v[100:103]\n\t" \
  "v_mfma_f32_16x16x32_bf16 v[96:99], v[4:7], v[80:83], v[96:99]\n\t" \
  "v_mfma_f32_16x16x32_bf16 v[108:111], v[8:11], v[84:87], v[108:111]\n\t" \
  "ds_read_b64 v[70:71], %[ua] offset:584\n\t" \
  "ds_read_b64 v[72:73], %[ua] offset:320\n\t" \
  "s_waitcnt lgkmcnt(9)\n\t" \
  "v_mfma_f32_16x16x32_bf16 v[104:107], v[0:3], v[84:87], v[104:107]\n\t" \
  "ds_read_b64 v[74:75], %[ua] offset:328\n\t" \
  "v_mfma_f32_16x16x32_bf16 v[108:111], v[40:43], v[88:91], v[108:111]\n\t" \
  "ds_read_u16_d16_hi v165, %[ke] offset:1408\n\t" \
  "ds_read_b64 v[76:77], %[ua] offset:512\n\t" \
  "v_mfma_f32_16x16x32_bf16 v[104:107], v[32:35], v[88:91], v[104:107]\n\t" \
  "v_mfma_f32_16x16x32_bf16 v[100:103], v[8:11], v[88:91], v[100:103]\n\t" \
  "s_waitcnt lgkmcnt(9)\n\t" \
  "v_mfma_f32_16x16x32_bf16 v[96:99], v[0:3], v[88:91], v[96:99]\n\t" \
  "ds_read_b64 v[78:79], %[ua] offset:520\n\t" \
  "v_mfma_f32_16x16x32_bf16 v[108:111], v[12:15], v[92:95], v[108:111]\n\t" \
  "ds_read_b64 v[80:81], %[ua] offset:384\n\t" \
  "ds_read_b64 v[82:83], %[ua] offset:392\n\t" \
  "v_mfma_f32_16x16x32_bf16 v[104:107], v[4:7], v[92:95], v[104:107]\n\t" \
  "s_waitcnt lgkmcnt(9)\n\t" \
  "v_mfma_f32_16x16x32_bf16 v[108:111], v[36:39], v[64:67], v[108:111]\n\t" \
  "ds_read_b64 v[84:85], %[ua] offset:448\n\t" \
  "ds_read_b64 v[86:87], %[ua] offset:456\n\t" \
  "v_mfma_f32_16x16x32_bf16 v[104:107], v[28:31], v[64:67], v[104:107]\n\t" \
  "v_mfma_f32_16x16x32_bf16 v[100:103], v[4:7], v[64:67], v[100:103]\n\t" \
  "ds_read_u16_d16_hi v166, %[ke] offset:2560\n\t" \
  "s_waitcnt lgkmcnt(8)\n\t" \
  "v_mfma_f32_16x16x32_bf16 v[108:111], v[16:19], v[68:71], v[108:111]\n\t" \
  "ds_read_b64 v[88:89], %[ua] offset:1152\n\t" \
  "ds_read_b64 v[90:91], %[ua] offset:1160\n\t" \
  "v_mfma_f32_16x16x32_bf16 v[104:107], v[8:11], v[68:71], v[104:107]\n\t" \
  "v_mfma_f32_16x16x32_bf16 v[108:111], v[32:35], v[72:75], v[108:111]\n\t" \
  "ds_read_u16_d16_hi v167, %[ke] offset:3712\n\t" \
  "ds_read_b64 v[92:93], %[ua] offset:1984\n\t" \
  "v_mfma_f32_16x16x32_bf16 v[104:107], v[24:27], v[72:75], v[104:107]\n\t" \
  "s_waitcnt lgkmcnt(9)\n\t" \
  "v_mfma_f32_16x16x32_bf16 v[100:103], v[0:3], v[72:75], v[100:103]\n\t" \
  "ds_read_b64 v[94:95], %[ua] offset:1992\n\t" \
  "v_mfma_f32_16x16x32_bf16 v[108:111], v[20:23], v[76:79], v[108:111]\n\t" \
  "ds_read_b64 v[64:65], %[ua] offset:1216\n\t" \
  "ds_read_b64 v[66:67], %[ua] offset:1224\n\t" \
  "v_mfma_f32_16x16x32_bf16 v[104:107], v[12:15], v[76:79], v[104:107]\n\t" \
  "s_waitcnt lgkmcnt(8)\n\t" \
  "v_mfma_f32_16x16x32_bf16 v[108:111], v[28:31], v[80:83], v[108:111]\n\t" \
  "ds_read_u16_d16_hi v168, %[ke] offset:640\n\t" \
  "ds_read_b64 v[68:69], %[ua] offset:1920\n\t" \
  "v_mfma_f32_16x16x32_bf16 v[104:107], v[20:23], v[80:83], v[104:107]\n\t" \
  "v_mfma_f32_16x16x32_bf16 v[108:111], v[24:27], v[84:87], v[108:111]\n\t" \
  "ds_read_b64 v[70:71], %[ua] offset:1928\n\t" \
  "ds_read_b64 v[72:73], %[ua] offset:1280\n\t" \
  "s_waitcnt lgkmcnt(9)\n\t" \
  "v_mfma_f32_16x16x32_bf16 v[104:107], v[16:19], v[84:87], v[104:107]\n\t" \
  "ds_read_b64 v[74:75], %[ua] offset:1288\n\t" \
  "v_mfma_f32_16x16x32_bf16 v[124:127], v[52:55], v[88:91], 0\n\t" \
  "ds_read_u16_d16_hi v169, %[ke] offset:1792\n\t" \
  "ds_read_b64 v[76:77], %[ua] offset:1856\n\t" \
  "v_mfma_f32_16x16x32_bf16 v[120:123], v[44:47], v[88:91], 0\n\t" \
  "v_mfma_f32_16x16x32_bf16 v[116:119], v[20:23], v[88:91], 0\n\t" \
  "s_waitcnt lgkmcnt(9)\n\t" \
  "v_mfma_f32_16x16x32_bf16 v[112:115], v[12:15], v[88:91], 0\n\t" \
  "ds_read_b64 v[78:79], %[ua] offset:1864\n\t" \
  "s_cbranch_scc1 L_B_%=\n\t" \
  "s_waitcnt vmcnt(0)\n\t" \
  "s_barrier\n\t" \
  "L_B_%=:\n\t" \
  "v_mfma_f32_16x16x32_bf16 v[124:127], v[0:3], v[92:95], v[124:127]\n\t" \
  "ds_read_b64 v[80:81], %[ua] offset:1344\n\t" \
  "ds_read_b64 v[82:83], %[ua] offset:1352\n\t" \
  "s_waitcnt lgkmcnt(9)\n\t" \
  "v_mfma_f32_16x16x32_bf16 v[124:127], v[48:51], v[64:67], v[124:127]\n\t" \
  "ds_read_u16_d16_hi v170, %[ke] offset:2944\n\t" \
  "ds_read_b64 v[84:85], %[ua] offset:1792\n\t" \
  "v_mfma_f32_16x16x32_bf16 v[120:123], v[40:43], v[64:67], v[120:123]\n\t" \
  "v_mfma_f32_16x16x32_bf16 v[116:119], v[16:19], v[64:67], v[116:119]\n\t" \
  "v_mfma_f32_16x16x32_bf16 v[112:115], v[8:11], v[64:67], v[112:115]\n\t" \
  "ds_read_b64 v[86:87], %[ua] offset:1800\n\t" \
  "s_waitcnt lgkmcnt(8)\n\t" \
  "v_mfma_f32_16x16x32_bf16 v[124:127], v[4:7], v[68:71], v[124:127]\n\t" \
  "ds_read_b64 v[88:89], %[ua] offset:1408\n\t" \
  "ds_read_b64 v[90:91], %[ua] offset:1416\n\t" \
  "v_mfma_f32_16x16x32_bf16 v[124:127], v[44:47], v[72:75], v[124:127]\n\t" \
  "ds_read_u16_d16_hi v171, %[ke] offset:4096\n\t" \
  "ds_read_b64 v[92:93], %[ua] offset:1728\n\t" \
  "v_mfma_f32_16x16x32_bf16 v[120:123], v[36:39], v[72:75], v[120:123]\n\t" \
  "v_mfma_f32_16x16x32_bf16 v[116:119], v[12:15], v[72:75], v[116:119]\n\t" \
  "s_waitcnt lgkmcnt(9)\n\t" \
  "v_mfma_f32_16x16x32_bf16 v[112:115], v[4:7], v[72:75], v[112:115]\n\t" \
  "ds_read_b64 v[94:95], %[ua] offset:1736\n\t" \
  "v_mfma_f32_16x16x32_bf16 v[124:127], v[8:11], v[76:79], v[124:127]\n\t" \
  "ds_read_b64 v[64:65], %[ua] offset:1472\n\t" \
  "ds_read_b64 v[66:67], %[ua] offset:1480\n\t" \
  "v_mfma_f32_16x16x32_bf16 v[120:123], v[0:3], v[76:79], v[120:123]\n\t" \
  "s_waitcnt lgkmcnt(9)\n\t" \
  "v_mfma_f32_16x16x32_bf16 v[124:127], v[40:43], v[80:83], v[124:127]\n\t" \
  "ds_read_u16_d16_hi v172, %[ke] offset:768\n\t" \
  "ds_read_b64 v[68:69], %[ua] offset:1664\n\t" \
  "v_mfma_f32_16x16x32_bf16 v[120:123], v[32:35], v[80:83], v[120:123]\n\t" \
  "v_mfma_f32_16x16x32_bf16 v[116:119], v[8:11], v[80:83], v[116:119]\n\t" \
  "ds_read_b64 v[70:71], %[ua] offset:1672\n\t" \
  "v_mfma_f32_16x16x32_bf16 v[112:115], v[0:3], v[80:83], v[112:115]\n\t" \
  "s_waitcnt lgkmcnt(8)\n\t" \
  "v_mfma_f32_16x16x32_bf16 v[124:127], v[12:15], v[84:87], v[124:127]\n\t" \
  "ds_read_b64 v[72:73], %[ua] offset:1536\n\t" \
  "ds_read_b64 v[74:75], %[ua] offset:1544\n\t" \
  "v_mfma_f32_16x16x32_bf16 v[120:123], v[4:7], v[84:87], v[120:123]\n\t" \
  "v_mfma_f32_16x16x32_bf16 v[124:127], v[36:39], v[88:91], v[124:127]\n\t" \
  "ds_read_u16_d16_hi v173, %[ke] offset:1920\n\t" \
  "ds_read_b64 v[76:77], %[ua] offset:1600\n\t" \
  "s_waitcnt lgkmcnt(9)\n\t" \
  "v_mfma_f32_16x16x32_bf16 v[120:123], v[28:31], v[88:91], v[120:123]\n\t" \
  "ds_read_b64 v[78:79], %[ua] offset:1608\n\t" \
  "v_mfma_f32_16x16x32_bf16 v[116:119], v[4:7], v[88:91], v[116:119]\n\t" \
  "v_mfma_f32_16x16x32_bf16 v[124:127], v[16:19], v[92:95], v[124:127]\n\t" \
  "ds_read_b64 v[80:81], %[ua] offset:2304\n\t" \
  "ds_read_b64 v[82:83], %[ua] offset:2312\n\t" \
  "v_mfma_f32_16x16x32_bf16 v[120:123], v[8:11], v[92:95], v[120:123]\n\t" \
  "s_waitcnt lgkmcnt(9)\n\t" \
  "v_mfma_f32_16x16x32_bf16 v[124:127], v[32:35], v[64:67], v[124:127]\n\t" \
  "ds_read_u16_d16_hi v174, %[ke] offset:3072\n\t" \
  "ds_read_b64 v[84:85], %[ua] offset:3136\n\t" \
  "v_mfma_f32_16x16x32_bf16 v[120:123], v[24:27], v[64:67], v[120:123]\n\t" \
  "ds_read_b64 v[86:87], %[ua] offset:3144\n\t" \
  "v_mfma_f32_16x16x32_bf16 v[116:119], v[0:3], v[64:67], v[116:119]\n\t" \
  "s_waitcnt lgkmcnt(8)\n\t" \
  "v_mfma_f32_16x16x32_bf16 v[124:127], v[20:23], v[68:71], v[124:127]\n\t" \
  "ds_read_b64 v[88:89], %[ua] offset:2368\n\t" \
  "ds_read_b64 v[90:91], %[ua] offset:2376\n\t" \
  "v_mfma_f32_16x16x32_bf16 v[120:123], v[12:15], v[68:71], v[120:123]\n\t" \
  "v_mfma_f32_16x16x32_bf16 v[124:127], v[28:31], v[72:75], v[124:127]\n\t" \
  "ds_read_u16_d16_hi v175, %[ke] offset:4224\n\t" \
  "ds_read_b64 v[92:93], %[ua] offset:3072\n\t" \
  "s_waitcnt lgkmcnt(9)\n\t" \
  "v_mfma_f32_16x16x32_bf16 v[120:123], v[20:23], v[72:75], v[120:123]\n\t" \
  "ds_read_b64 v[94:95], %[ua] offset:3080\n\t" \
  "v_mfma_f32_16x16x32_bf16 v[124:127], v[24:27], v[76:79], v[124:127]\n\t" \
  "ds_read_b64 v[64:65], %[ua] offset:2432\n\t" \
  "ds_read_b64 v[66:67], %[ua] offset:2440\n\t" \
  "v_mfma_f32_16x16x32_bf16 v[120:123], v[16:19], v[76:79], v[120:123]\n\t" \
  "s_waitcnt lgkmcnt(9)\n\t" \
  "v_mfma_f32_16x16x32_bf16 v[140:143], v[52:55], v[80:83], 0\n\t" \
  "ds_read_b64 v[68:69], %[ua] offset:3008\n\t" \
  "ds_read_b64 v[70:71], %[ua] offset:3016\n\t" \
  "v_mfma_f32_16x16x32_bf16 v[136:139], v[44:47], v[80:83], 0\n\t" \
  "v_mfma_f32_16x16x32_bf16 v[132:135], v[20:23], v[80:83], 0\n\t" \
  "ds_read_b64 v[72:73], %[ua] offset:2496\n\t" \
  "v_mfma_f32_16x16x32_bf16 v[128:131], v[12:15], v[80:83], 0\n\t" \
  "s_waitcnt lgkmcnt(8)\n\t" \
  "v_mfma_f32_16x16x32_bf16 v[140:143], v[0:3], v[84:87], v[140:143]\n\t" \
  "ds_read_b64 v[74:75], %[ua] offset:2504\n\t" \
  "ds_read_b64 v[76:77], %[ua] offset:2944\n\t" \
  "v_mfma_f32_16x16x32_bf16 v[140:143], v[48:51], v[88:91], v[140:143]\n\t" \
  "ds_read_b64 v[78:79], %[ua] offset:2952\n\t" \
  "ds_read_b64 v[80:81], %[ua] offset:2560\n\t" \
  "v_mfma_f32_16x16x32_bf16 v[136:139], v[40:43], v[88:91], v[136:139]\n\t" \
  "s_waitcnt lgkmcnt(9)\n\t" \
  "v_mfma_f32_16x16x32_bf16 v[132:135], v[16:19], v[88:91], v[132:135]\n\t" \
  "ds_read_b64 v[82:83], %[ua] offset:2568\n\t" \
  "v_add_f32 v97, v97, v112\n\t" \
  "v_mfma_f32_16x16x32_bf16 v[128:131], v[8:11], v[88:91], v[128:131]\n\t" \
  "v_add_f32 v98, v98, v113\n\t" \
  "v_mfma_f32_16x16x32_bf16 v[140:143], v[4:7], v[92:95], v[140:143]\n\t" \
  "ds_read_b64 v[84:85], %[ua] offset:2880\n\t" \
  "ds_read_b64 v[86:87], %[ua] offset:2888\n\t" \
  "v_add_f32 v99, v99, v114\n\t" \
  "s_waitcnt lgkmcnt(8)\n\t" \
  "v_mfma_f32_16x16x32_bf16 v[140:143], v[44:47], v[64:67], v[140:143]\n\t" \
  "ds_read_b64 v[88:89], %[ua] offset:2624\n\t" \
  "ds_read_b64 v[90:91], %[ua] offset:2632\n\t" \
  "v_add_f32 v101, v101, v116\n\t" \
  "v_mfma_f32_16x16x32_bf16 v[136:139], v[36:39], v[64:67], v[136:139]\n\t" \
  "v_add_f32 v102, v102, v117\n\t" \
  "v_mfma_f32_16x16x32_bf16 v[132:135], v[12:15], v[64:67], v[132:135]\n\t" \
  "v_add_f32 v103, v103, v118\n\t" \
  "v_mfma_f32_16x16x32_bf16 v[128:131], v[4:7], v[64:67], v[128:131]\n\t" \
  "v_add_f32 v105, v105, v120\n\t" \
  "v_mfma_f32_16x16x32_bf16 v[140:143], v[8:11], v[68:71], v[140:143]\n\t" \
  "ds_read_b64 v[92:93], %[ua] offset:2816\n\t" \
  "ds_read_b64 v[94:95], %[ua] offset:2824\n\t" \
  "v_add_f32 v106, v106, v121\n\t" \
  "v_mfma_f32_16x16x32_bf16 v[136:139], v[0:3], v[68:71], v[136:139]\n\t" \
  "v_add_f32 v107, v107, v122\n\t" \
  "s_waitcnt lgkmcnt(8)\n\t" \
  "v_mfma_f32_16x16x32_bf16 v[140:143], v[40:43], v[72:75], v[140:143]\n\t" \
  "ds_read_b64 v[64:65], %[ua] offset:2688\n\t" \
  "ds_read_b64 v[66:67], %[ua] offset:2696\n\t" \
  "v_add_f32 v109, v109, v124\n\t" \
  "v_mfma_f32_16x16x32_bf16 v[136:139], v[32:35], v[72:75], v[136:139]\n\t" \
  "v_add_f32 v110, v110, v125\n\t" \
  "v_mfma_f32_16x16x32_bf16 v[132:135], v[8:11], v[72:75], v[132:135]\n\t" \
  "v_add_f32 v111, v111, v126\n\t" \
  "v_mfma_f32_16x16x32_bf16 v[128:131], v[0:3], v[72:75], v[128:131]\n\t" \
  "v_mfma_f32_16x16x32_bf16 v[140:143], v[12:15], v[76:79], v[140:143]\n\t" \
  "ds_read_b64 v[68:69], %[ua] offset:2752\n\t" \
  "ds_read_b64 v[70:71], %[ua] offset:2760\n\t" \
  "v_mfma_f32_16x16x32_bf16 v[136:139], v[4:7], v[76:79], v[136:139]\n\t" \
  "s_waitcnt lgkmcnt(8)\n\t" \
  "v_mfma_f32_16x16x32_bf16 v[140:143], v[36:39], v[80:83], v[140:143]\n\t" \
  "ds_read_b64 v[72:73], %[ua] offset:3456\n\t" \
  "ds_read_b64 v[74:75], %[ua] offset:3464\n\t" \
  "v_mfma_f32_16x16x32_bf16 v[136:139], v[28:31], v[80:83], v[136:139]\n\t" \
  "v_mfma_f32_16x16x32_bf16 v[132:135], v[4:7], v[80:83], v[132:135]\n\t" \
  "v_mfma_f32_16x16x32_bf16 v[140:143], v[16:19], v[84:87], v[140:143]\n\t" \
  "ds_read_b64 v[76:77], %[ua] offset:4288\n\t" \
  "ds_read_b64 v[78:79], %[ua] offset:4296\n\t" \
  "v_mfma_f32_16x16x32_bf16 v[136:139], v[8:11], v[84:87], v[136:139]\n\t" \
  "s_waitcnt lgkmcnt(8)\n\t" \
  "v_mfma_f32_16x16x32_bf16 v[140:143], v[32:35], v[88:91], v[140:143]\n\t" \
  "ds_read_b64 v[80:81], %[ua] offset:3520\n\t" \
  "ds_read_b64 v[82:83], %[ua] offset:3528\n\t" \
  "v_mfma_f32_16x16x32_bf16 v[136:139], v[24:27], v[88:91], v[136:139]\n\t" \
  "v_mfma_f32_16x16x32_bf16 v[132:135], v[0:3], v[88:91], v[132:135]\n\t" \
  "v_mfma_f32_16x16x32_bf16 v[140:143], v[20:23], v[92:95], v[140:143]\n\t" \
  "ds_read_b64 v[84:85], %[ua] offset:4224\n\t" \
  "ds_read_b64 v[86:87], %[ua] offset:4232\n\t" \
  "v_mfma_f32_16x16x32_bf16 v[136:139], v[12:15], v[92:95], v[136:139]\n\t" \
  "s_waitcnt lgkmcnt(8)\n\t" \
  "v_mfma_f32_16x16x32_bf16 v[140:143], v[28:31], v[64:67], v[140:143]\n\t" \
  "ds_read_b64 v[88:89], %[ua] offset:3584\n\t" \
  "ds_read_b64 v[90:91], %[ua] offset:3592\n\t" \
  "v_mfma_f32_16x16x32_bf16 v[136:139], v[20:23], v[64:67], v[136:139]\n\t" \
  "v_mfma_f32_16x16x32_bf16 v[140:143], v[24:27], v[68:71], v[140:143]\n\t" \
  "ds_read_b64 v[92:93], %[ua] offset:4160\n\t" \
  "ds_read_b64 v[94:95], %[ua] offset:4168\n\t" \
  "v_mfma_f32_16x16x32_bf16 v[136:139], v[16:19], v[68:71], v[136:139]\n\t" \
  "s_waitcnt lgkmcnt(8)\n\t" \
  "v_mfma_f32_16x16x32_bf16 v[156:159], v[52:55], v[72:75], 0\n\t" \
  "ds_read_b64 v[64:65], %[ua] offset:3648\n\t" \
  "ds_read_b64 v[66:67], %[ua] offset:3656\n\t" \
  "v_mfma_f32_16x16x32_bf16 v[152:155], v[44:47], v[72:75], 0\n\t" \
  "v_mfma_f32_16x16x32_bf16 v[148:151], v[20:23], v[72:75], 0\n\t" \
  "v_mfma_f32_16x16x32_bf16 v[144:147], v[12:15], v[72:75], 0\n\t" \
  "v_mfma_f32_16x16x32_bf16 v[156:159], v[0:3], v[76:79], v[156:159]\n\t" \
  "ds_read_b64 v[68:69], %[ua] offset:4096\n\t" \
  "ds_read_b64 v[70:71], %[ua] offset:4104\n\t" \
  "s_waitcnt lgkmcnt(8)\n\t" \
  "v_mfma_f32_16x16x32_bf16 v[156:159], v[48:51], v[80:83], v[156:159]\n\t" \
  "ds_read_b64 v[72:73], %[ua] offset:3712\n\t" \
  "ds_read_b64 v[74:75], %[ua] offset:3720\n\t" \
  "v_mfma_f32_16x16x32_bf16 v[152:155], v[40:43], v[80:83], v[152:155]\n\t" \
  "v_mfma_f32_16x16x32_bf16 v[148:151], v[16:19], v[80:83], v[148:151]\n\t" \
  "v_add_f32 v98, v98, v128\n\t" \
  "v_mfma_f32_16x16x32_bf16 v[144:147], v[8:11], v[80:83], v[144:147]\n\t" \
  "v_add_f32 v99, v99, v129\n\t" \
  "v_mfma_f32_16x16x32_bf16 v[156:159], v[4:7], v[84:87], v[156:159]\n\t" \
  "ds_read_b64 v[76:77], %[ua] offset:4032\n\t" \
  "ds_read_b64 v[78:79], %[ua] offset:4040\n\t" \
  "v_add_f32 v130, v130, v115\n\t" \
  "s_waitcnt lgkmcnt(8)\n\t" \
  "v_mfma_f32_16x16x32_bf16 v[156:159], v[44:47], v[88:91], v[156:159]\n\t" \
  "ds_read_b64 v[80:81], %[ua] offset:3776\n\t" \
  "ds_read_b64 v[82:83], %[ua] offset:3784\n\t" \
  "v_add_f32 v102, v102, v132\n\t" \
  "v_mfma_f32_16x16x32_bf16 v[152:155], v[36:39], v[88:91], v[152:155]\n\t" \
  "v_add_f32 v103, v103, v133\n\t" \
  "v_mfma_f32_16x16x32_bf16 v[148:151], v[12:15], v[88:91], v[148:151]\n\t" \
  "v_add_f32 v134, v134, v119\n\t" \
  "v_mfma_f32_16x16x32_bf16 v[144:147], v[4:7], v[88:91], v[144:147]\n\t" \
  "v_add_f32 v106, v106, v136\n\t" \
  "v_mfma_f32_16x16x32_bf16 v[156:159], v[8:11], v[92:95], v[156:159]\n\t" \
  "ds_read_b64 v[84:85], %[ua] offset:3968\n\t" \
  "ds_read_b64 v[86:87], %[ua] offset:3976\n\t" \
  "v_add_f32 v107, v107, v137\n\t" \
  "v_mfma_f32_16x16x32_bf16 v[152:155], v[0:3], v[92:95], v[152:155]\n\t" \
  "v_add_f32 v138, v138, v123\n\t" \
  "s_waitcnt lgkmcnt(8)\n\t" \
  "v_mfma_f32_16x16x32_bf16 v[156:159], v[40:43], v[64:67], v[156:159]\n\t" \
  "ds_read_b64 v[88:89], %[ua] offset:3840\n\t" \
  "ds_read_b64 v[90:91], %[ua] offset:3848\n\t" \
  "v_add_f32 v110, v110, v140\n\t" \
  "v_mfma_f32_16x16x32_bf16 v[152:155], v[32:35], v[64:67], v[152:155]\n\t" \
  "v_add_f32 v111, v111, v141\n\t" \
  "v_mfma_f32_16x16x32_bf16 v[148:151], v[8:11], v[64:67], v[148:151]\n\t" \
  "v_add_f32 v142, v142, v127\n\t" \
  "v_mfma_f32_16x16x32_bf16 v[144:147], v[0:3], v[64:67], v[144:147]\n\t" \
  "v_fmac_f32 v96, %[dv], v160\n\t" \
  "v_mfma_f32_16x16x32_bf16 v[156:159], v[12:15], v[68:71], v[156:159]\n\t" \
  "ds_read_b64 v[92:93], %[ua] offset:3904\n\t" \
  "ds_read_b64 v[94:95], %[ua] offset:3912\n\t" \
  "v_fmac_f32 v97, %[dv], v161\n\t" \
  "v_mfma_f32_16x16x32_bf16 v[152:155], v[4:7], v[68:71], v[152:155]\n\t" \
  "v_fmac_f32 v98, %[dv], v162\n\t" \
  "s_waitcnt lgkmcnt(8)\n\t" \
  "v_mfma_f32_16x16x32_bf16 v[156:159], v[36:39], v[72:75], v[156:159]\n\t" \
  "v_fmac_f32 v99, %[dv], v163\n\t" \
  "v_mfma_f32_16x16x32_bf16 v[152:155], v[28:31], v[72:75], v[152:155]\n\t" \
  "v_fmac_f32 v100, %[dv], v164\n\t" \
  "v_mfma_f32_16x16x32_bf16 v[148:151], v[4:7], v[72:75], v[148:151]\n\t" \
  "v_fmac_f32 v101, %[dv], v165\n\t" \
  "v_mfma_f32_16x16x32_bf16 v[156:159], v[16:19], v[76:79], v[156:159]\n\t" \
  "v_fmac_f32 v102, %[dv], v166\n\t" \
  "v_mfma_f32_16x16x32_bf16 v[152:155], v[8:11], v[76:79], v[152:155]\n\t" \
  "v_fmac_f32 v103, %[dv], v167\n\t" \
  "s_waitcnt lgkmcnt(4)\n\t" \
  "v_mfma_f32_16x16x32_bf16 v[156:159], v[32:35], v[80:83], v[156:159]\n\t" \
  "v_fmac_f32 v104, %[dv], v168\n\t" \
  "v_mfma_f32_16x16x32_bf16 v[152:155], v[24:27], v[80:83], v[152:155]\n\t" \
  "v_fmac_f32 v105, %[dv], v169\n\t" \
  "v_mfma_f32_16x16x32_bf16 v[148:151], v[0:3], v[80:83], v[148:151]\n\t" \
  "v_fmac_f32 v106, %[dv], v170\n\t" \
  "v_mfma_f32_16x16x32_bf16 v[156:159], v[20:23], v[84:87], v[156:159]\n\t" \
  "v_fmac_f32 v107, %[dv], v171\n\t" \
  "v_mfma_f32_16x16x32_bf16 v[152:155], v[12:15], v[84:87], v[152:155]\n\t" \
  "v_fmac_f32 v108, %[dv], v172\n\t" \
  "s_waitcnt lgkmcnt(0)\n\t" \
  "v_mfma_f32_16x16x32_bf16 v[156:159], v[28:31], v[88:91], v[156:159]\n\t" \
  "v_fmac_f32 v109, %[dv], v173\n\t" \
  "v_mfma_f32_16x16x32_bf16 v[152:155], v[20:23], v[88:91], v[152:155]\n\t" \
  "v_fmac_f32 v110, %[dv], v174\n\t" \
  "v_mfma_f32_16x16x32_bf16 v[156:159], v[24:27], v[92:95], v[156:159]\n\t" \
  "v_fmac_f32 v111, %[dv], v175\n\t" \
  "v_mfma_f32_16x16x32_bf16 v[152:155], v[16:19], v[92:95], v[152:155]\n\t" \
  "s_nop 7\n\t" \
  "v_add_f32 v99, v99, v144\n\t" \
  "v_add_f32 v145, v145, v130\n\t" \
  "v_add_f32 v146, v146, v131\n\t" \
  "v_add_f32 v103, v103, v148\n\t" \
  "v_add_f32 v149, v149, v134\n\t" \
  "v_add_f32 v150, v150, v135\n\t" \
  "v_add_f32 v107, v107, v152\n\t" \
  "v_add_f32 v153, v153, v138\n\t" \
  "v_add_f32 v154, v154, v139\n\t" \
  "v_add_f32 v111, v111, v156\n\t" \
  "v_add_f32 v157, v157, v142\n\t" \
  "v_add_f32 v158, v158, v143\n\t" \
  "ds_bpermute_b32 v0, %[src], v145\n\t" \
  "ds_bpermute_b32 v1, %[src], v146\n\t" \
  "ds_bpermute_b32 v2, %[src], v147\n\t" \
  "ds_bpermute_b32 v4, %[src], v149\n\t" \
  "ds_bpermute_b32 v5, %[src], v150\n\t" \
  "ds_bpermute_b32 v6, %[src], v151\n\t" \
  "ds_bpermute_b32 v8, %[src], v153\n\t" \
  "ds_bpermute_b32 v9, %[src], v154\n\t" \
  "ds_bpermute_b32 v10, %[src], v155\n\t" \
  "ds_bpermute_b32 v12, %[src], v157\n\t" \
  "ds_bpermute_b32 v13, %[src], v158\n\t" \
  "ds_bpermute_b32 v14, %[src], v159\n\t" \
  "s_brev_b64 exec, 1\n\t" \
  "s_waitcnt lgkmcnt(8)\n\t" \
  "ds_write_b128 %[cx], v[144:147] offset:32\n\t" \
  "ds_write_b128 %[cx], v[148:151] offset:48\n\t" \
  "ds_write_b128 %[cx], v[152:155] offset:96\n\t" \
  "ds_write_b128 %[cx], v[156:159] offset:112\n\t" \
  "s_mov_b64 exec, -2\n\t" \
  "s_waitcnt lgkmcnt(0)\n\t" \
  "s_barrier\n\t" \
  "v_add_f32 v96, v96, v0\n\t" \
  "v_add_f32 v97, v97, v1\n\t" \
  "v_add_f32 v98, v98, v2\n\t" \
  "v_add_f32 v100, v100, v4\n\t" \
  "v_add_f32 v101, v101, v5\n\t" \
  "v_add_f32 v102, v102, v6\n\t" \
  "v_add_f32 v104, v104, v8\n\t" \
  "v_add_f32 v105, v105, v9\n\t" \
  "v_add_f32 v106, v106, v10\n\t" \
  "v_add_f32 v108, v108, v12\n\t" \
  "v_add_f32 v109, v109, v13\n\t" \
  "v_add_f32 v110, v110, v14\n\t" \
  "s_mov_b64 exec, 1\n\t" \
  "ds_read_b128 v[0:3], %[cx] offset:16\n\t" \
  "ds_read_b128 v[4:7], %[cx] offset:32\n\t" \
  "ds_read_b128 v[8:11], %[cx] offset:80\n\t" \
  "ds_read_b128 v[12:15], %[cx] offset:96\n\t" \
  "s_waitcnt lgkmcnt(0)\n\t" \
  "v_add_f32 v96, v96, v1\n\t" \
  "v_add_f32 v97, v97, v2\n\t" \
  "v_add_f32 v98, v98, v3\n\t" \
  "v_add_f32 v100, v100, v5\n\t" \
  "v_add_f32 v101, v101, v6\n\t" \
  "v_add_f32 v102, v102, v7\n\t" \
  "v_add_f32 v104, v104, v9\n\t" \
  "v_add_f32 v105, v105, v10\n\t" \
  "v_add_f32 v106, v106, v11\n\t" \
  "v_add_f32 v108, v108, v13\n\t" \
  "v_add_f32 v109, v109, v14\n\t" \
  "v_add_f32 v110, v110, v15\n\t" \
  "s_mov_b64 exec, -1\n\t" \
  "v_cvt_pk_bf16_f32 v96, v96, v97\n\t" \
  "v_cvt_pk_bf16_f32 v97, v98, v99\n\t" \
  "v_cvt_pk_bf16_f32 v100, v100, v101\n\t" \
  "v_cvt_pk_bf16_f32 v101, v102, v103\n\t" \
  "v_cvt_pk_bf16_f32 v104, v104, v105\n\t" \
  "v_cvt_pk_bf16_f32 v105, v106, v107\n\t" \
  "v_cvt_pk_bf16_f32 v108, v108, v109\n\t" \
  "v_cvt_pk_bf16_f32 v109, v110, v111\n\t" \
  "ds_bpermute_b32 v0, %[src2], v96\n\t" \
  "ds_bpermute_b32 v1, %[src2], v97\n\t" \
  "ds_bpermute_b32 v2, %[src2], v100\n\t" \
  "ds_bpermute_b32 v3, %[src2], v101\n\t" \
  "ds_bpermute_b32 v4, %[src2], v104\n\t" \
  "ds_bpermute_b32 v5, %[src2], v105\n\t" \
  "ds_bpermute_b32 v6, %[src2], v108\n\t" \
  "ds_bpermute_b32 v7, %[src2], v109\n\t" \
  "s_mov_b32 s40, 294912\n\t" \
  "s_waitcnt lgkmcnt(6)\n\t" \
  "buffer_store_dwordx2 v[0:1], %[yo], %[rs], s40 offen sc1\n\t" \
  "s_mov_b32 s40, 589824\n\t" \
  "s_waitcnt lgkmcnt(4)\n\t" \
  "buffer_store_dwordx2 v[2:3], %[yo], %[rs], s40 offen sc1\n\t" \
  "s_mov_b32 s40, 1474560\n\t" \
  "s_waitcnt lgkmcnt(2)\n\t" \
  "buffer_store_dwordx2 v[4:5], %[yo], %[rs], s40 offen sc1\n\t" \
  "s_mov_b32 s40, 1769472\n\t" \
  "s_waitcnt lgkmcnt(0)\n\t" \
  "buffer_store_dwordx2 v[6:7], %[yo], %[rs], s40 offen sc1\n\t" \
  ""
#define ENTRY \
  "s_load_dwordx2 s[56:57], %[ka], 0x60\n\t" \
  "s_load_dwordx2 s[48:49], %[ka], 0x0\n\t" \
  "s_load_dwordx4 s[52:55], %[ka], 0x30\n\t" \
  "s_getpc_b64 s[44:45]\n\t" \
  "v_readfirstlane_b32 %[w1k], %[tid]\n\t" \
  "v_and_b32 v0, 63, %[tid]\n\t" \
  "s_lshr_b32 s40, %[bid], 3\n\t" \
  "s_and_b32 s41, %[bid], 7\n\t" \
  "s_lshl_b32 %[w1k], %[w1k], 4\n\t" \
  "s_mov_b32 s47, 0x20000\n\t" \
  "s_mul_i32 %[m], s40, 57\n\t" \
  "s_lshr_b32 %[m], %[m], 9\n\t" \
  "s_mul_i32 s43, %[m], 9\n\t" \
  "s_sub_u32 %[c], s40, s43\n\t" \
  "s_cmp_lt_u32 s40, 27\n\t" \
  "s_cbranch_scc1 L_T_%=\n\t" \
  "s_and_b32 s44, s44, 0xffffff00\n\t" \
  "s_mov_b32 s46, 77696\n\t" \
  "s_sub_u32 s42, s40, 27\n\t" \
  "s_lshl_b32 s42, s42, 11\n\t" \
  "s_mul_i32 s43, %[w1k], 10\n\t" \
  "s_add_u32 s42, s42, s43\n\t" \
  "s_mov_b32 m0, 163328\n\t" \
  "v_lshlrev_b32 v1, 7, v0\n\t" \
  "s_mov_b64 exec, 0xffff\n\t" \
  "buffer_load_dword v1, s[44:47], s42 offen lds\n\t" \
  "s_mov_b64 exec, -1\n\t" \
  "s_waitcnt lgkmcnt(0)\n\t" \
  "s_branch L_E_%=\n\t" \
  "L_T_%=:\n\t" \
  "s_cmp_lt_u32 %[w1k], 6144\n\t" \
  "s_cbranch_scc0 L_V_%=\n\t" \
  "v_mov_b32 v6, -1\n\t" \
  "v_mov_b32 v7, -1\n\t" \
  "v_mov_b64 v[8:9], v[6:7]\n\t" \
  "v_lshlrev_b32 v5, 4, v0\n\t" \
  "s_mul_i32 s42, s40, 11264\n\t" \
  "s_add_u32 s42, s42, %[w1k]\n\t" \
  "s_mul_i32 s60, s41, 294912\n\t" \
  "s_add_u32 s60, s60, 1048576\n\t" \
  "s_lshl_b32 s61, s41, 10\n\t" \
  "s_add_u32 s61, s61, 212992\n\t" \
  "s_brev_b32 s58, 16\n\t" \
  "s_mov_b32 s59, s47\n\t" \
  "s_add_u32 s43, s60, s42\n\t" \
  "s_cmp_lt_u32 s42, 294912\n\t" \
  "s_cselect_b32 s43, s43, s61\n\t" \
  "s_waitcnt lgkmcnt(0)\n\t" \
  "s_and_b32 s57, s57, 0xffff\n\t" \
  "buffer_store_dwordx4 v[6:9], v5, s[56:59], s43 offen sc1\n\t" \
  "s_add_u32 s42, s42, 6144\n\t" \
  "s_add_u32 s43, s60, s42\n\t" \
  "s_cmp_lt_u32 %[w1k], 5120\n\t" \
  "s_cbranch_scc0 L_E_%=\n\t" \
  "s_cmp_lt_u32 s42, 294912\n\t" \
  "s_cbranch_scc0 L_E_%=\n\t" \
  "buffer_store_dwordx4 v[6:9], v5, s[56:59], s43 offen sc1\n\t" \
  "s_branch L_E_%=\n\t" \
  "L_V_%=:\n\t" \
  "s_and_b32 s46, %[w1k], 1024\n\t" \
  "v_lshrrev_b32 v1, 3, v0\n\t" \
  "s_lshr_b32 s43, s46, 7\n\t" \
  "v_add_u32 v1, s43, v1\n\t" \
  "v_mul_u32_u24 v1, 0x480, v1\n\t" \
  "s_lshr_b32 s43, s46, 8\n\t" \
  "v_and_b32 v3, 7, v0\n\t" \
  "v_xor_b32 v4, s43, v3\n\t" \
  "v_lshrrev_b32 v2, 4, v0\n\t" \
  "s_lshl_b32 s61, s41, 8\n\t" \
  "s_mul_i32 s42, %[m], 87\n\t" \
  "s_add_u32 s43, s61, s42\n\t" \
  "s_sub_u32 s43, s43, 2\n\t" \
  "s_mul_i32 s43, s43, 0x480\n\t" \
  "v_xor_b32 v5, v2, v4\n\t" \
  "v_lshlrev_b32 v5, 4, v5\n\t" \
  "v_add3_u32 v5, v1, v5, s43\n\t" \
  "v_add_u32 v6, 18432, v5\n\t" \
  "s_mul_i32 s42, %[m], 3\n\t" \
  "s_add_u32 s42, s42, %[c]\n\t" \
  "s_add_u32 s42, s42, s41\n\t" \
  "s_mul_i32 s43, s42, 57\n\t" \
  "s_lshr_b32 s43, s43, 9\n\t" \
  "s_mul_i32 s43, s43, 9\n\t" \
  "s_sub_u32 s42, s42, s43\n\t" \
  "s_lshl_b32 s42, s42, 7\n\t" \
  "s_mov_b32 s40, s42\n\t" \
  "v_lshlrev_b32 v7, 1, v2\n\t" \
  "v_and_b32 v8, 2, v7\n\t" \
  "v_xor_b32 v8, v8, v4\n\t" \
  "v_lshlrev_b32 v8, 4, v8\n\t" \
  "s_lshl_b32 s43, %[c], 7\n\t" \
  "v_add_u32 v1, s43, v1\n\t" \
  "v_add_u32 v8, v1, v8\n\t" \
  "s_mov_b32 s50, 2359296\n\t" \
  "s_mov_b32 s51, s47\n\t" \
  "s_add_u32 s44, s46, 73728\n\t" \
  "s_mov_b32 s45, s46\n\t" \
  "s_waitcnt lgkmcnt(0)\n\t" \
  "s_and_b32 s49, s49, 0xffff\n\t" \
  "s_mov_b64 s[64:65], s[54:55]\n\t" \
  "s_and_b32 s53, s53, 0xffff\n\t" \
  "s_mov_b32 s54, 663552\n\t" \
  "s_mov_b32 s55, s47\n\t" \
  "L_L_%=:\n\t" \
  "s_mov_b32 m0, s44\n\t" \
  "s_mov_b32 s43, s42\n\t" \
  "buffer_load_dwordx4 v5, s[48:51], s42 offen lds\n\t" \
  "s_add_u32 m0, m0, 2048\n\t" \
  "s_nop 0\n\t" \
  "buffer_load_dwordx4 v6, s[48:51], s43 offen lds\n\t" \
  "s_add_u32 m0, m0, 2048\n\t" \
  "s_add_u32 s43, s43, 18432\n\t" \
  "buffer_load_dwordx4 v6, s[48:51], s43 offen lds\n\t" \
  "s_add_u32 m0, m0, 2048\n\t" \
  "s_add_u32 s43, s43, 18432\n\t" \
  "buffer_load_dwordx4 v6, s[48:51], s43 offen lds\n\t" \
  "s_add_u32 m0, m0, 2048\n\t" \
  "s_add_u32 s43, s43, 18432\n\t" \
  "buffer_load_dwordx4 v6, s[48:51], s43 offen lds\n\t" \
  "s_cmp_eq_u32 s46, 0\n\t" \
  "s_cselect_b32 s59, 18432, 0\n\t" \
  "s_cselect_b32 s60, 2048, 0\n\t" \
  "s_add_u32 m0, m0, s60\n\t" \
  "s_add_u32 s43, s43, s59\n\t" \
  "buffer_load_dwordx4 v6, s[48:51], s43 offen lds\n\t" \
  "s_mul_i32 s43, s42, 576\n\t" \
  "s_mov_b32 m0, s45\n\t" \
  "s_nop 0\n\t" \
  "buffer_load_dwordx4 v8, s[52:55], s43 offen lds\n\t" \
  "s_add_u32 m0, m0, 2048\n\t" \
  "s_add_u32 s43, s43, 18432\n\t" \
  "buffer_load_dwordx4 v8, s[52:55], s43 offen lds\n\t" \
  "s_add_u32 m0, m0, 2048\n\t" \
  "s_add_u32 s43, s43, 18432\n\t" \
  "buffer_load_dwordx4 v8, s[52:55], s43 offen lds\n\t" \
  "s_add_u32 m0, m0, 2048\n\t" \
  "s_add_u32 s43, s43, 18432\n\t" \
  "buffer_load_dwordx4 v8, s[52:55], s43 offen lds\n\t" \
  "s_add_u32 s42, s42, 128\n\t" \
  "s_cmp_ge_u32 s42, 1152\n\t" \
  "s_cselect_b32 s42, 0, s42\n\t" \
  "s_add_u32 s44, s44, 12288\n\t" \
  "s_add_u32 s45, s45, 8192\n\t" \
  "s_cmp_lt_u32 s45, 49152\n\t" \
  "s_cbranch_scc1 L_L_%=\n\t" \
  "s_waitcnt vmcnt(30)\n\t" \
  "s_barrier\n\t" \
  "s_load_dwordx4 s[68:71], %[ka], 0x8\n\t" \
  "s_load_dwordx2 s[76:77], %[ka], 0x20\n\t" \
  "s_load_dword %[tag], s[56:57], 0x0\n\t" \
  "s_mov_b32 s58, s42\n\t" \
  "L_M_%=:\n\t" \
  "s_mul_i32 s43, s58, 576\n\t" \
  "s_mov_b32 m0, s45\n\t" \
  "s_nop 0\n\t" \
  "buffer_load_dwordx4 v8, s[52:55], s43 offen lds\n\t" \
  "s_add_u32 m0, m0, 2048\n\t" \
  "s_add_u32 s43, s43, 18432\n\t" \
  "buffer_load_dwordx4 v8, s[52:55], s43 offen lds\n\t" \
  "s_add_u32 m0, m0, 2048\n\t" \
  "s_add_u32 s43, s43, 18432\n\t" \
  "buffer_load_dwordx4 v8, s[52:55], s43 offen lds\n\t" \
  "s_add_u32 m0, m0, 2048\n\t" \
  "s_add_u32 s43, s43, 18432\n\t" \
  "buffer_load_dwordx4 v8, s[52:55], s43 offen lds\n\t" \
  "s_add_u32 s58, s58, 128\n\t" \
  "s_cmp_ge_u32 s58, 1152\n\t" \
  "s_cselect_b32 s58, 0, s58\n\t" \
  "s_add_u32 s45, s45, 8192\n\t" \
  "s_cmp_lt_u32 s45, 73728\n\t" \
  "s_cbranch_scc1 L_M_%=\n\t" \
  "v_xor_b32 v8, v7, v3\n\t" \
  "v_lshlrev_b32 v8, 4, v8\n\t" \
  "v_add_u32 %[vw2], v1, v8\n\t" \
  "s_and_b32 s65, s65, 0xffff\n\t" \
  "s_mov_b64 s[66:67], s[54:55]\n\t" \
  "v_lshrrev_b32 v8, 4, v0\n\t" \
  "v_and_b32 v8, 2, v8\n\t" \
  "v_xor_b32 v8, v8, v4\n\t" \
  "v_lshlrev_b32 v8, 4, v8\n\t" \
  "s_mul_i32 s43, %[m], 96\n\t" \
  "s_add_u32 s43, s43, s61\n\t" \
  "s_mul_i32 s43, s43, 0x480\n\t" \
  "v_add3_u32 v9, v1, v8, s43\n\t" \
  "v_add_u32 v10, 0xfffff280, v9\n\t" \
  "v_add_u32 v8, 14976, v9\n\t" \
  "v_lshrrev_b32 v11, 2, v0\n\t" \
  "s_lshr_b32 s59, s46, 6\n\t" \
  "v_add_u32 v11, s59, v11\n\t" \
  "v_min_u32 v11, 26, v11\n\t" \
  "s_lshl_b32 s43, s41, 12\n\t" \
  "s_add_u32 s43, s43, 65536\n\t" \
  "v_lshl_add_u32 %[vfs], v11, 7, s43\n\t" \
  "s_lshl_b32 s43, %[bid], 13\n\t" \
  "s_lshl_b32 s59, s46, 2\n\t" \
  "s_add_u32 s43, s43, s59\n\t" \
  "v_lshl_add_u32 v11, v0, 4, s43\n\t" \
  "s_waitcnt lgkmcnt(0)\n\t" \
  "s_add_u32 %[tag], %[tag], 1\n\t" \
  "s_mov_b64 s[72:73], s[70:71]\n\t" \
  "s_and_b32 s69, s69, 0xffff\n\t" \
  "s_and_b32 s73, s73, 0xffff\n\t" \
  "s_mov_b64 s[70:71], s[50:51]\n\t" \
  "s_mov_b64 s[74:75], s[50:51]\n\t" \
  "s_waitcnt vmcnt(12)\n\t" \
  "s_barrier\n\t" \
  "s_add_u32 s44, s46, 73728\n\t" \
  "L_Q_%=:\n\t" \
  "s_mov_b32 m0, s44\n\t" \
  "s_mov_b32 s43, s42\n\t" \
  "buffer_load_dwordx4 v5, s[48:51], s42 offen lds\n\t" \
  "s_add_u32 m0, m0, 2048\n\t" \
  "s_nop 0\n\t" \
  "buffer_load_dwordx4 v6, s[48:51], s43 offen lds\n\t" \
  "s_add_u32 m0, m0, 2048\n\t" \
  "s_add_u32 s43, s43, 18432\n\t" \
  "buffer_load_dwordx4 v6, s[48:51], s43 offen lds\n\t" \
  "s_add_u32 m0, m0, 2048\n\t" \
  "s_add_u32 s43, s43, 18432\n\t" \
  "buffer_load_dwordx4 v6, s[48:51], s43 offen lds\n\t" \
  "s_add_u32 m0, m0, 2048\n\t" \
  "s_add_u32 s43, s43, 18432\n\t" \
  "buffer_load_dwordx4 v6, s[48:51], s43 offen lds\n\t" \
  "s_cmp_eq_u32 s46, 0\n\t" \
  "s_cselect_b32 s59, 18432, 0\n\t" \
  "s_cselect_b32 s60, 2048, 0\n\t" \
  "s_add_u32 m0, m0, s60\n\t" \
  "s_add_u32 s43, s43, s59\n\t" \
  "buffer_load_dwordx4 v6, s[48:51], s43 offen lds\n\t" \
  "s_add_u32 s42, s42, 128\n\t" \
  "s_cmp_ge_u32 s42, 1152\n\t" \
  "s_cselect_b32 s42, 0, s42\n\t" \
  "s_add_u32 s44, s44, 12288\n\t" \
  "s_cmp_lt_u32 s44, 110592\n\t" \
  "s_cbranch_scc1 L_Q_%=\n\t" \
  "s_mov_b32 s45, s46\n\t" \
  "s_mov_b32 s58, s40\n\t" \
  "L_W_%=:\n\t" \
  "s_mul_i32 s43, s58, 576\n\t" \
  "s_mov_b32 m0, s45\n\t" \
  "s_nop 0\n\t" \
  "buffer_load_dwordx4 %[vw2], s[64:67], s43 offen lds\n\t" \
  "s_add_u32 m0, m0, 2048\n\t" \
  "s_add_u32 s43, s43, 18432\n\t" \
  "buffer_load_dwordx4 %[vw2], s[64:67], s43 offen lds\n\t" \
  "s_add_u32 m0, m0, 2048\n\t" \
  "s_add_u32 s43, s43, 18432\n\t" \
  "buffer_load_dwordx4 %[vw2], s[64:67], s43 offen lds\n\t" \
  "s_add_u32 m0, m0, 2048\n\t" \
  "s_add_u32 s43, s43, 18432\n\t" \
  "buffer_load_dwordx4 %[vw2], s[64:67], s43 offen lds\n\t" \
  "s_add_u32 s58, s58, 128\n\t" \
  "s_cmp_ge_u32 s58, 1152\n\t" \
  "s_cselect_b32 s58, 0, s58\n\t" \
  "s_add_u32 s45, s45, 8192\n\t" \
  "s_cmp_lt_u32 s45, 24576\n\t" \
  "s_cbranch_scc1 L_W_%=\n\t" \
  "s_waitcnt vmcnt(12)\n\t" \
  "s_barrier\n\t" \
  "global_load_dword %[fs], %[vfs], s[56:57] sc1\n\t" \
  "s_cmp_gt_u32 %[c], 6\n\t" \
  "s_cbranch_scc1 L_K_%=\n\t" \
  "s_add_u32 m0, s46, 110592\n\t" \
  "s_mov_b32 s43, 0\n\t" \
  "buffer_load_dwordx4 v10, s[68:71], 0 offen nt lds\n\t" \
  "s_add_u32 m0, m0, 2048\n\t" \
  "s_nop 0\n\t" \
  "buffer_load_dwordx4 v8, s[68:71], s43 offen nt lds\n\t" \
  "s_add_u32 m0, m0, 2048\n\t" \
  "s_add_u32 s43, s43, 18432\n\t" \
  "buffer_load_dwordx4 v8, s[68:71], s43 offen nt lds\n\t" \
  "s_add_u32 m0, m0, 2048\n\t" \
  "s_add_u32 s43, s43, 18432\n\t" \
  "buffer_load_dwordx4 v8, s[68:71], s43 offen nt lds\n\t" \
  "s_add_u32 m0, m0, 2048\n\t" \
  "s_add_u32 s43, s43, 18432\n\t" \
  "buffer_load_dwordx4 v8, s[68:71], s43 offen nt lds\n\t" \
  "s_add_u32 m0, m0, 2048\n\t" \
  "s_add_u32 s43, s43, 18432\n\t" \
  "buffer_load_dwordx4 v8, s[68:71], s43 offen nt lds\n\t" \
  "s_cmp_eq_u32 s46, 0\n\t" \
  "s_cselect_b32 s59, 18432, 0\n\t" \
  "s_cselect_b32 s60, 2048, 0\n\t" \
  "s_add_u32 m0, m0, s60\n\t" \
  "s_add_u32 s43, s43, s59\n\t" \
  "buffer_load_dwordx4 v8, s[68:71], s43 offen nt lds\n\t" \
  "s_add_u32 m0, s46, 123904\n\t" \
  "s_mov_b32 s43, 0\n\t" \
  "buffer_load_dwordx4 v9, s[72:75], 0 offen nt lds\n\t" \
  "s_add_u32 m0, m0, 2048\n\t" \
  "s_add_u32 s43, s43, 18432\n\t" \
  "buffer_load_dwordx4 v9, s[72:75], s43 offen nt lds\n\t" \
  "s_add_u32 m0, m0, 2048\n\t" \
  "s_add_u32 s43, s43, 18432\n\t" \
  "buffer_load_dwordx4 v9, s[72:75], s43 offen nt lds\n\t" \
  "s_add_u32 m0, m0, 2048\n\t" \
  "s_add_u32 s43, s43, 18432\n\t" \
  "buffer_load_dwordx4 v9, s[72:75], s43 offen nt lds\n\t" \
  "s_add_u32 m0, m0, 2048\n\t" \
  "s_add_u32 s43, s43, 18432\n\t" \
  "buffer_load_dwordx4 v9, s[72:75], s43 offen nt lds\n\t" \
  "s_add_u32 m0, m0, 2048\n\t" \
  "s_add_u32 s43, s43, 18432\n\t" \
  "buffer_load_dwordx4 v9, s[72:75], s43 offen nt lds\n\t" \
  "s_waitcnt vmcnt(13)\n\t" \
  "s_branch L_H_%=\n\t" \
  "L_K_%=:\n\t" \
  "s_waitcnt vmcnt(0)\n\t" \
  "L_H_%=:\n\t" \
  "s_cmp_eq_u32 s46, 0\n\t" \
  "s_cselect_b32 s43, 0, 4352\n\t" \
  "s_add_u32 m0, s43, 141344\n\t" \
  "s_nop 0\n\t" \
  "global_load_lds_dwordx4 v11, s[76:77] offset:0 nt\n\t" \
  "global_load_lds_dwordx4 v11, s[76:77] offset:1024 nt\n\t" \
  "global_load_lds_dwordx4 v11, s[76:77] offset:2048 nt\n\t" \
  "global_load_lds_dwordx4 v11, s[76:77] offset:3072 nt\n\t" \
  "s_mov_b32 s43, 0\n\t" \
  "L_C_%=:\n\t" \
  "v_cmp_ne_u32 vcc, %[tag], %[fs]\n\t" \
  "s_cbranch_vccz L_D_%=\n\t" \
  "s_add_u32 s43, s43, 1\n\t" \
  "s_cmp_gt_u32 s43, 400000\n\t" \
  "s_cbranch_scc1 L_D_%=\n\t" \
  "global_load_dword %[fs], %[vfs], s[56:57] sc1\n\t" \
  "s_waitcnt vmcnt(0)\n\t" \
  "s_branch L_C_%=\n\t" \
  "L_D_%=:\n\t" \
  "s_barrier\n\t" \
  "L_E_%=:\n\t" \
  ""
#define P0_CLOB "v0","v1","v2","v3","v4","v5","v6","v7","v8","v9","v10","v11","v12","v13","v14","v15","v16","v17","v18","v19","v20","v21","v22","v23","v24","v25","v26","v27","v28","v29","v30","v31","v32","v33","v34","v35","v36","v37","v38","v39","v40","v41","v42","v43","v44","v45","v46","v47","v48","v49","v50","v51","v52","v53","v54","v55","v56","v57","v58","v59","v60","v61","v62","v63","v64","v65","v66","v67","v68","v69","v70","v71","v72","v73","v74","v75","v76","v77","v78","v79","v80","v81","v82","v83","v84","v85","v86","v87","v88","v89","v90","v91","v92","v93","v94","v95","v96","s60","s61"
#define P01_CLOB "s40","s41","s42","s43","s44","s45","s46","s47","s48","s49","s50","s51","s52","s53","s54","s55","s56","s57","s58","s59","s60","s61"
#define P1_CLOB "v0","v1","v2","v3","v4","v5","v6","v7","v8","v9","v10","v11","v12","v13","v14","v15","v16","v17","v18","v19","v20","v21","v22","v23","v24","v25","v26","v27","v28","v29","v30","v31","v32","v33","v34","v35","v36","v37","v38","v39","v40","v41","v42","v43","v44","v45","v46","v47","v48","v49","v50","v51","v52","v53","v54","v55","v56","v57","v58","v59","v60","v61","v62","v63","v64","v65","v66","v67","v68","v69","v70","v71","v72","v73","v74","v75","v76","v77","v78","v79","v80","v81","v82","v83","v84","v85","v86","v87","v88","v89","v90","v91","v92","v93","v94","v95","v96","v97","v98","v99","v100","v101","v102","v103","v104","v105","v106","v107","v108","v109","v110","v111","v112","v113","v114","v115","v116","v117","v118","v119","v120","v121","v122","v123","v124","v125","v126","v127","v128","v129","v130","v131","v132","v133","v134","v135","v136","v137","v138","v139","v140","v141","v142","v143","v144","v145","v146","v147","v148","v149","v150","v151","v152","v153","v154","v155","v156","v157","v158","v159","v160","v161","v162","v163","v164","v165","v166","v167","v168","v169","v170","v171","v172","v173","v174","v175","v176","s40","s41","s42","s43","s60","s61","vcc"
#define CONV_CLOB "v0","v1","v2","v3","v4","v5","v6","v7","v8","v9","v10","v11","v12","v13","v14","v15","v16","v17","v18","v19","v20","v21","v22","v23","v24","v25","v26","v27","v28","v29","v30","v31","v32","v33","v34","v35","v36","v37","v38","v39","v40","v41","v42","v43","v44","v45","v46","v47","v48","v49","v50","v51","v52","v53","v54","v55","v56","v57","v58","v59","v60","v61","v62","v63","v64","v65","v66","v67","v68","v69","v70","v71","v72","v73","v74","v75","v76","v77","v78","v79","v80","v81","v82","v83","v84","v85","v86","v87","v88","v89","v90","v91","v92","v93","v94","v95","v96","v97","v98","v99","v100","v101","v102","v103","v104","v105","v106","v107","v108","v109","v110","v111","v112","v113","v114","v115","v116","v117","v118","v119","v120","v121","v122","v123","v124","v125","v126","v127","v128","v129","v130","v131","v132","v133","v134","v135","v136","v137","v138","v139","v140","v141","v142","v143","v144","v145","v146","v147","v148","v149","v150","v151","v152","v153","v154","v155","v156","v157","v158","v159","v160","v161","v162","v163","v164","v165","v166","v167","v168","v169","v170","v171","v172","v173","v174","v175","s40","s60","s61"
#define ENTRY_CLOB "v0","v1","v2","v3","v4","v5","v6","v7","v8","v9","v10","v11","s40","s41","s42","s43","s44","s45","s46","s47","s48","s49","s50","s51","s52","s53","s54","s55","s56","s57","s58","s59","s60","s61","s62","s63","s64","s65","s66","s67","s68","s69","s70","s71","s72","s73","s74","s75","s76","s77"
typedef unsigned short u16;
typedef unsigned v4u __attribute__((ext_vector_type(4)));
typedef unsigned v2u __attribute__((ext_vector_type(2)));
typedef short s16x4 __attribute__((ext_vector_type(4)));
typedef short bf16x8 __attribute__((ext_vector_type(8)));
typedef float f32x4 __attribute__((ext_vector_type(4)));
typedef float f32x2 __attribute__((ext_vector_type(2)));
typedef __bf16 bf16x2_t __attribute__((ext_vector_type(2)));
typedef __amdgpu_buffer_rsrc_t rsrc_t;
#define LAS __attribute__((address_space(3)))
#define DI __device__ __forceinline__
constexpr int S = 2048, D = 576;
constexpr unsigned TB = S * D * 2;
constexpr unsigned OFF_EP = 0, OFF_F1 = 0x10000, OFF_FX = 0x18000, OFF_FP = 0x28000, OFF_FY = 0x30000, OFF_W2B = 0x31000;
constexpr unsigned OFF_MQ = 0x34000, OFF_G = 0x36000, OFF_MEAN = 0x38000, OFF_SS = 0x40000, OFF_FM = 0x80000, OFF_FU = 48u << 20;
constexpr unsigned OFF_HS = 1u << 20, OFF_XF = 4u << 20, OFF_UB = 7u << 20, OFF_Y = 10u << 20, OFF_HX = 13u << 20, OFF_P = 32u << 20, OFF_P4 = 40u << 20, OFF_PC = 44u << 20, OFF_PC4 = 46u << 20;
constexpr unsigned L_QH = 73728, L_K = 110592, L_V = L_K + 13312, L_CH = 141312, L_RED = 151552, L_SPARE = 159744;
constexpr unsigned L_CS = L_SPARE, L_ST = L_SPARE + 512, L_T = L_SPARE + 2048, L_CU = 122880, L_CX = 158720;
constexpr int AUX_NT = 2, AUX_SC1 = 16;
#define PF4 2
#define ROTM 3
#define PRE_A 3
#define PRE_B 6
#define BPRE 1
#define NOPREP_SLEEP 0
#define W2MID 6
#define SPRE 10
#define OUT_AUX 0
#define PREPB 0
#define GATE 0
#define EARLY4 1
#define PF3 3
#define PF_AUX 0
constexpr unsigned CODE_BYTES = 69632;

DI float asf(unsigned u) { return __builtin_bit_cast(float, u); }
DI float fmul1(float a, float b) { float r; asm("v_mul_f32 %0, %1, %2" : "=v"(r) : "v"(a), "v"(b)); return r; }
DI float ffma1(float a, float b, float c) { float r; asm("v_fma_f32 %0, %1, %2, %3" : "=v"(r) : "v"(a), "v"(b), "v"(c)); return r; }
DI float bflo(unsigned u) { return __builtin_bit_cast(float, u << 16); }
DI float bfhi(unsigned u) { return __builtin_bit_cast(float, u & 0xffff0000u); }
DI unsigned pk2(float lo, float hi) { f32x2 x = {lo, hi}; return __builtin_bit_cast(unsigned, __builtin_convertvector(x, bf16x2_t)); }
DI float dot2(unsigned a, unsigned b, float c) { return __builtin_amdgcn_fdot2_f32_bf16(__builtin_bit_cast(bf16x2_t, a), __builtin_bit_cast(bf16x2_t, b), c, false); }
DI float gelu(float x) {
  const float c0 = -2.f * 0.7978845608028654f * 1.4426950408889634f, c1 = c0 * 0.044715f;
  float e = __builtin_amdgcn_exp2f(x * (c0 + c1 * x * x));
  return x * __builtin_amdgcn_rcpf(1.f + e);
}
DI v2u trread(unsigned a) { return __builtin_bit_cast(v2u, __builtin_amdgcn_ds_read_tr16_b64_v4i16((LAS s16x4*)(uintptr_t)a)); }
DI bf16x8 cat(v2u a, v2u b) { return __builtin_bit_cast(bf16x8, (v4u){a[0], a[1], b[0], b[1]}); }
DI f32x4 mfma(bf16x8 a, v4u b, f32x4 c) { return __builtin_amdgcn_mfma_f32_16x16x32_bf16(a, __builtin_bit_cast(bf16x8, b), c, 0, 0, 0); }
template <class T> DI T ldsr(unsigned a) { return *(LAS T*)(uintptr_t)a; }
template <class T> DI void ldsw(unsigned a, T v) { *(LAS T*)(uintptr_t)a = v; }
DI rsrc_t mk(const void* p, unsigned n) { return __builtin_amdgcn_make_buffer_rsrc((void*)p, 0, n, 0x00020000); }
DI unsigned rfl(unsigned x) { return __builtin_amdgcn_readfirstlane(x); }
template <int POL> DI void glds(rsrc_t rs, unsigned voff, unsigned lds) {
  lds = rfl(lds);
  if (POL == 0) asm volatile("s_mov_b32 m0, %2\n\ts_nop 0\n\tbuffer_load_dwordx4 %0, %1, 0 offen lds" :: "v"(voff), "s"(rs), "s"(lds) : "memory");
  if (POL == 1) asm volatile("s_mov_b32 m0, %2\n\ts_nop 0\n\tbuffer_load_dwordx4 %0, %1, 0 offen nt lds" :: "v"(voff), "s"(rs), "s"(lds) : "memory");
  if (POL == 2) asm volatile("s_mov_b32 m0, %2\n\ts_nop 0\n\tbuffer_load_dwordx4 %0, %1, 0 offen sc1 lds" :: "v"(voff), "s"(rs), "s"(lds) : "memory");
}
#define PIN(x) asm volatile("" : "+v"(x))
#define PINS(x) asm volatile("" : "+s"(x))
#define VM0() asm volatile("s_waitcnt vmcnt(0)" ::: "memory")
#define VMN(n) asm volatile("s_waitcnt vmcnt(" #n ")" ::: "memory")
#define BAR() asm volatile("s_waitcnt lgkmcnt(0)\n\ts_barrier" ::: "memory")
DI void st32(rsrc_t rs, unsigned off, unsigned v) { __builtin_amdgcn_raw_buffer_store_b32(v, rs, off, 0, AUX_SC1); }
DI void st64(rsrc_t rs, unsigned off, v2u v) { __builtin_amdgcn_raw_buffer_store_b64(v, rs, off, 0, AUX_SC1); }
DI void st128(rsrc_t rs, unsigned off, v4u v) { __builtin_amdgcn_raw_buffer_store_b128(v, rs, off, 0, AUX_SC1); }
DI void st32z(rsrc_t rs, unsigned off, float v, unsigned z) {
  asm volatile("s_cmp_lg_u32 %3, 0\n\ts_cbranch_scc1 L_Z%=\n\tbuffer_store_dword %0, %1, %2, 0 offen sc1\nL_Z%=:" :: "v"(v), "v"(off), "s"(rs), "s"(z) : "memory", "scc");
}
DI void flagz(rsrc_t rs, unsigned voff, unsigned base, unsigned tag, unsigned z) {
  unsigned t;
  asm volatile("s_cmp_lg_u32 %5, 0\n\ts_cbranch_scc1 L_F%=\n\tv_mov_b32 %0, %4\n\ts_mov_b64 exec, 0xffff\n\tbuffer_store_dword %0, %1, %2, %3 offen sc1\n\ts_mov_b64 exec, -1\nL_F%=:"
               : "=&v"(t) : "v"(voff), "s"(rs), "s"(base), "s"(tag), "s"(z) : "memory", "scc");
}
DI unsigned ld32(rsrc_t rs, unsigned off) { return __builtin_amdgcn_raw_buffer_load_b32(rs, off, 0, AUX_SC1); }
DI float ldf(rsrc_t rs, unsigned off) { return __builtin_bit_cast(float, ld32(rs, off)); }
DI v2u ld64(rsrc_t rs, unsigned off) { return __builtin_amdgcn_raw_buffer_load_b64(rs, off, 0, AUX_SC1); }
DI v4u ld128(rsrc_t rs, unsigned off) { return __builtin_amdgcn_raw_buffer_load_b128(rs, off, 0, AUX_SC1); }
DI void poll(rsrc_t rs, unsigned off, unsigned tag) {
  #pragma unroll 1
  for (int spin = 0; spin < 400000; ++spin) {
    unsigned f;
    asm volatile("s_nop 4\n\tbuffer_load_dword %0, %1, %2, 0 offen sc1\n\ts_waitcnt vmcnt(0)" : "=v"(f) : "v"(off), "s"(rs) : "memory");
    if (__all(f == tag)) break;
    __builtin_amdgcn_s_sleep(2);
  }
}
DI float xsum16_32(float v) {
  unsigned u = __builtin_bit_cast(unsigned, v);
  v2u a = __builtin_amdgcn_permlane16_swap(u, u, false, false); v = asf(a[0]) + asf(a[1]); u = __builtin_bit_cast(unsigned, v);
  v2u b = __builtin_amdgcn_permlane32_swap(u, u, false, false); return asf(b[0]) + asf(b[1]);
}
#define STV_OP
#define STAMP(idx)

DI f32x4 sel(bool c, f32x4 a, f32x4 b) { return c ? a : b; }
DI int shiftof(unsigned col) { unsigned grp = col / 144; return (grp & 1 ? 1 : -1) * (int)(1 + (grp >> 1)); }
template <int C> DI float dppadd(float a) { return a + asf(__builtin_amdgcn_update_dpp(0, __builtin_bit_cast(int, a), C, 0xf, 0xf, false)); }
DI float wsum(float a) { a = xsum16_32(a); a = dppadd<0x128>(a); a = dppadd<0x124>(a); a = dppadd<0x122>(a); return dppadd<0x121>(a); }
DI unsigned nofill(unsigned u) { return min(u, 0xfffffffeu); }
DI float nofillf(float f) { return asf(nofill(__builtin_bit_cast(unsigned, f))); }
constexpr unsigned OOB = 0x7fffff00u;

__global__ void __launch_bounds__(512, 2) fused(const u16* __restrict__ q, const u16* __restrict__ k, const u16* __restrict__ v, const u16* __restrict__ sk,
    const u16* __restrict__ hk, const u16* __restrict__ sD, const u16* __restrict__ w1, const u16* __restrict__ w2, const u16* __restrict__ w3,
    const u16* __restrict__ w4, const u16* __restrict__ gw, u16* __restrict__ out, char* ws) {
  unsigned vw2, vfs, fs, w1k, m, c, tag;
  STAMP(0);
  asm volatile(ENTRY : [vw2] "=&v"(vw2), [vfs] "=&v"(vfs), [fs] "=&v"(fs), [tag] "=&s"(tag), [w1k] "=&s"(w1k), [m] "=&s"(m), [c] "=&s"(c) STV_OP
    : [bid] "s"(blockIdx.x), [tid] "v"(threadIdx.x), [ka] "s"(__builtin_amdgcn_kernarg_segment_ptr()) : "memory", "scc", "vcc", ENTRY_CLOB);
  const int wave = (w1k >> 10) & 7, lane = threadIdx.x & 63;
  const unsigned bid = blockIdx.x, x = bid & 7, bi = bid >> 3;
  const int i16 = lane & 15, g = lane >> 4, q4 = (lane & 15) >> 2, p4 = lane & 3;
  const rsrc_t rq = mk(q, TB), rw = mk(ws, 0x8000000), rw2 = mk(w2, D * D * 2);
  const int R0 = 256 * x;
  const unsigned kq = x & 3, jj = bi & 3, rt = (x >> 2) * 8 + (bi >> 2), sl = kq * 4 + jj, wi = wave & 3;
  const unsigned shx = rfl(OFF_HX + (rt * 64 + wi) * 18 * 1024), sfl = OFF_FM + rt * 16 * 1024, rot = rt & ROTM;
  auto prefill = [&](unsigned h0, unsigned h1) {
    const unsigned bb = (sl >> 1) * 1152 + wi * (8 * 9216) + (sl & 1) * 640, b0 = rfl(bb + rot * 128), b1 = rfl(bb + ((rot + 1) & 3) * 128 - 576 * 9216);
    const unsigned g3 = (lane >> 3) * 9216 + ((lane & 7) ^ (g << 1)) * 16;
    asm volatile("s_nop 4\n\t" CORE_L0 :: [w3] "s"(w3), [wi] "s"(wi), [b0] "s"(b0), [b1] "s"(b1), [h0] "s"(h0), [h1] "s"(h1), [g3] "v"(g3) : "memory", "scc", CORE_P_CLOB);
  };
  const unsigned c0 = bid < 216 ? 2 * bid : (bid < 248 ? 448 + 4 * (bid - 216) : 432 + 2 * (bid - 248));
  const unsigned fua = OFF_FU + ((c0 >> 5) * 64 + lane) * 128, fxa = OFF_FX + ((rt >> 1) * 32 + 9 * (rt & 1) + min(lane >> 1, 17)) * 128;
  float rhoo[2], meano;
  auto rhotab = [&]() {
    float t[4];
    { unsigned u[4][5]; meano = ldf(rw, OFF_MEAN + ((rt * 4 + kq) * 32 + (lane & 31)) * 4);
#pragma unroll
      for (int n = 0; n < 4; ++n) { const unsigned rb16 = rt * 8 + (n < 2 ? wi : kq) * 2 + (n & 1);
#pragma unroll
        for (int s = 0; s < 5; ++s) u[n][s] = ld32(rw, (g == 3 && s >= 3) ? OOB : OFF_SS + (rb16 * 20 + 5 * g + s) * 64 + i16 * 4); }
      BAR();
#pragma unroll
      for (int n = 0; n < 4; ++n) { t[n] = 0;
#pragma unroll
        for (int s = 0; s < 5; ++s) t[n] += asf(u[n][s]); }
    }
#pragma unroll
    for (int n = 0; n < 4; ++n) t[n] = __builtin_amdgcn_rsqf(xsum16_32(t[n]) * (1.f / D) + 1e-5f);
    if (g < 2) ldsw<float>(L_SPARE + (wi * 32 + lane) * 4, -0.46866869f * (g ? t[1] : t[0]));
    rhoo[0] = t[2]; rhoo[1] = t[3]; PIN(rhoo[0]); PIN(rhoo[1]); PIN(meano);
  };
  auto prep = [&](unsigned lk, unsigned lv, unsigned tb, v2u skw) {
    const unsigned chg = (wave & 1) * 2 + (g >> 1), zb = wave >> 1, rp = g & 1;
    const float w0 = bflo(skw[0]), w1f = bfhi(skw[0]), w2f = bflo(skw[1]), w3f = bfhi(skw[1]);
    auto ad = [&](unsigned r) { return r * 128 + ((chg ^ ((r >> 2) & 3)) << 5) + 8 * p4; };
    const unsigned r0 = 32 * zb + 2 * rp + 4 * q4;
    v4u o[2];
#pragma unroll
    for (int hf = 0; hf < 2; ++hf) {
      float kk[5][4];
#pragma unroll
      for (int ph = 0; ph < 5; ++ph) { v2u t = trread(lk + ad(r0 + ph) + 2048 * hf); kk[ph][0] = bflo(t[0]); kk[ph][1] = bfhi(t[0]); kk[ph][2] = bflo(t[1]); kk[ph][3] = bfhi(t[1]); }
#pragma unroll
      for (int r = 0; r < 2; ++r) {
        v2u t = trread(lv + ad(r0 + r) + 2048 * hf); float vf[4] = {bflo(t[0]), bfhi(t[0]), bflo(t[1]), bfhi(t[1])}, y[4];
#pragma unroll
        for (int e = 0; e < 4; ++e) y[e] = (kk[r + 3][e] * w0 + kk[r + 2][e] * w1f + kk[r + 1][e] * w2f + kk[r][e] * w3f) * vf[e];
        o[r][2 * hf] = pk2(y[0], y[1]); o[r][2 * hf + 1] = pk2(y[2], y[3]);
      }
    }
    tb += wave * 2560;
    ldsw<v4u>(tb + ((g >> 1) * 16 + i16) * 80 + rp * 32, o[0]); ldsw<v4u>(tb + ((g >> 1) * 16 + i16) * 80 + rp * 32 + 16, o[1]);
  };
  auto ustore = [&](unsigned tb, unsigned uoff) {
    const v4u a = ldsr<v4u>(tb + (lane >> 2) * 80 + p4 * 16), b = ldsr<v4u>(tb + (16 + (lane >> 2)) * 80 + p4 * 16);
    st128(rw, uoff, a); st128(rw, uoff + 16 * 4096, b);
  };
  auto conv = [&]() {
    const unsigned ch = wave >> 1, hs = wave & 1;
    unsigned ha = L_CH + ch * 4352 + 8 * (4 + 8 * g - q4 + p4), ua = L_CU + ch * 4608 + 56 + 8 * i16 - 16 * g;
    float dv; { const unsigned dw = *(const __attribute__((address_space(4))) unsigned*)(uintptr_t)(sD + c0 + (ch & 2)); dv = (ch & 1) ? bfhi(dw) : bflo(dw); PIN(dv); }
    const unsigned yo = OFF_Y + ((lane >> 3) * D + c0 + ch) * 64 + (lane & 7) * 8, cx = L_CX + ch * 144, ke = L_CU + ch * 4608 + 128 + (4 * i16 + g) * 2;
    const unsigned src = (g > 0 ? lane - 16 : ((i16 + 15) & 15) + 48) * 4, src2 = ((lane & 3) * 16 + (lane >> 2)) * 4, nobar = (bid - 216) < 32u;
    unsigned dummy;
#define CONV_OPS : "=s"(dummy) STV_OP : [ha] "v"(ha), [ua] "v"(ua), [yo] "v"(yo), [cx] "v"(cx), [ke] "v"(ke), [src] "v"(src), [src2] "v"(src2), [dv] "v"(dv), [rs] "s"(rw), [nobar] "s"(rfl(nobar)) : "memory", "scc", CONV_CLOB
    if (hs == 0) asm volatile(CONV_0 CONV_OPS); else asm volatile(CONV_1 CONV_OPS);
    STAMP(12);
  };
  const bool hbig = bid >= 216 && bid < 248;
  if (bi >= 27) {
    const unsigned hs = bi - 27, hw = hs * 8 + wave;
    const unsigned o1 = lane * 16, o2 = lane < 8 ? 1024 + lane * 16 : OOB;
    const unsigned pcol = 7 + (hs & 1), prow = R0 + 128 * (hs >> 1);
    v2u skw = {0, 0};
    v4u ra[9], rb[9];
    if (hbig) { const rsrc_t rk = mk(k, TB), rv = mk(v, TB);
      const unsigned vv = (prow + 8 * wave + (lane >> 3)) * 1152 + pcol * 128 + ((lane & 7) ^ (((2 * wave + (lane >> 5)) & 3) << 1)) * 16, vk = vv - 3 * 1152;
      glds<1>(rk, vk, w1k); glds<1>(rk, vk + 64 * 1152, 8192 + w1k); glds<1>(rv, vv, 20480 + w1k); glds<1>(rv, vv + 64 * 1152, 28672 + w1k);
      if (wave == 0) glds<1>(rk, vk + 128 * 1152, 16384);
      skw = *(const v2u*)(sk + (64 * pcol + 32 * (wave & 1) + 16 * (g >> 1) + i16) * 4);
    } else {
#pragma unroll
      for (int n = 0; n < 9; ++n) { ra[n] = __builtin_amdgcn_raw_buffer_load_b128(rw2, o1, ((x * 8 + wave) * 9 + n) * 1152, 0); rb[n] = (v4u){0, 0, 0, 0}; if (lane < 8) rb[n] = __builtin_amdgcn_raw_buffer_load_b128(rw2, o1 + 1024, ((x * 8 + wave) * 9 + n) * 1152, 0); }
    }
    { const rsrc_t rh = mk(hk, TB);
      glds<1>(rh, c0 * 4096 + w1k + lane * 16, L_CH + (wave >> 2) * 4352 + 32 + wi * 1024);
      if (hbig) glds<1>(rh, c0 * 4096 + 8192 + w1k + lane * 16, L_CH + (2 + (wave >> 2)) * 4352 + 32 + wi * 1024);
      if (lane < 2) ldsw<v4u>(L_CH + wi * 4352 + lane * 16, (v4u){0, 0, 0, 0});
      if (lane < 16) ldsw<v4u>(L_CU + (2 * wave + (lane >> 3)) * 1152 + (lane & 7) * 16, (v4u){0, 0, 0, 0}); }
    asm volatile("s_load_dword %0, %1, 0x0\n\ts_waitcnt lgkmcnt(0)\n\ts_add_u32 %0, %0, 1" : "=&s"(tag) : "s"(ws) : "scc");
    STAMP(1);
    VM0();
    PIN(skw);
    BAR();
    STAMP(2);
    if (hbig) {
      prep(0, 20480, 40960, skw); ustore(40960 + wave * 2560, OFF_UB + (64 * pcol + 32 * (wave & 1) + (lane >> 2)) * 4096 + ((prow >> 5) + (wave >> 1)) * 64 + p4 * 16);
      STAMP(3);
      VM0();
      flagz(rw, ((2 * pcol + (wave & 1)) * 64 + (prow >> 5) + (wave >> 1)) * 128 + lane * 4, OFF_FU, tag, 0);
      STAMP(4);
    } else {
      float t1 = 0;
#pragma unroll
      for (int n = 0; n < 9; ++n) { float a = 0;
#pragma unroll
        for (int j = 0; j < 4; ++j) { a = dot2(ra[n][j], 0x3f803f80u, a); a = dot2(rb[n][j], 0x3f803f80u, a); }
        a = wsum(a); t1 = lane == n ? a : t1; }
      if (lane < 9) st64(rw, OFF_W2B + ((x * 8 + wave) * 9 + lane) * 8, (v2u){pk2(t1 * (1.f / D), 0.f), tag});
      const rsrc_t rg = mk(gw, D * 2);
      const v4u ga = __builtin_amdgcn_raw_buffer_load_b128(rg, o1, 0, 0), gb = __builtin_amdgcn_raw_buffer_load_b128(rg, o2, 0, 0);
      float gs = 0;
#pragma unroll 1
      for (int it = 0; it < 4; ++it) {
#pragma unroll
        for (int n = 0; n < 8; ++n) { const unsigned so = (R0 + wave * 32 + it * 8 + n) * 1152; ra[n] = __builtin_amdgcn_raw_buffer_load_b128(rq, o1, so, 0); rb[n] = __builtin_amdgcn_raw_buffer_load_b128(rq, 1024 + (lane & 7) * 16, so, 0); }
#pragma unroll
        for (int n = 0; n < 8; ++n) { float a = 0;
#pragma unroll
          for (int j = 0; j < 4; ++j) { a = dot2(ra[n][j], ga[j], a); a = dot2(rb[n][j], gb[j], a); }
          a = wsum(a); gs = lane == it * 8 + n ? a : gs; }
      }
      if (lane < 32) st32(rw, OFF_G + (R0 + wave * 32 + lane) * 4, __builtin_bit_cast(unsigned, __builtin_amdgcn_rcpf(1.f + __builtin_amdgcn_exp2f(-1.4426950408889634f * gs))));
    }
    STAMP(5);
    if (!hbig && wave >= 4) prefill(0, CORE_PRE);
    if (wave == 0) poll(rw, fua, tag);
    BAR();
    STAMP(8);
    glds<2>(rw, OFF_UB + (c0 + (wave >> 2)) * 4096 + lane * 64 + wi * 16, L_CU + wave * 1152 + 128);
    if (hbig) glds<2>(rw, OFF_UB + (c0 + 2 + (wave >> 2)) * 4096 + lane * 64 + wi * 16, L_CU + (8 + wave) * 1152 + 128);
    VM0();
    if (!hbig && wave >= 4) { BAR(); BAR(); }
    if (hbig) { BAR(); if (wave >= 4) prefill(0, SPRE); }
  } else {
    const unsigned r8 = lane >> 3, hp = wave & 1;
    unsigned kr = c + 3 * m + x; kr -= kr >= 18 ? 18 : (kr >= 9 ? 9 : 0);
    const int base1 = R0 + (m ? 16 + 80 * m : 0);
    const unsigned blk1 = wave >= 4 ? 4 + ((wave >> 1) & 1) : wave, nb1 = wave >= 4 ? hp : 0;
    const bool act = blk1 < (m ? 5u : 6u), doprep = c < 7 && wave < (m == 2 ? 4 : 6);
    unsigned red, aw1, at, vh, sh, sxf, qra, ssoff, mnoff, mqld, vg, vfu;
    auto pre1 = [&]() {
      red = L_RED + wi * 2048 + lane * 16;
      const unsigned row = base1 + 16 * blk1 + i16, rb16 = (base1 >> 4) + blk1;
      aw1 = ((lane >> 2) * 128 + ((lane >> 3) & 3) * 32 + 8 * p4 + nb1 * 4096) ^ (nb1 * 64); at = L_T + 8 * g + nb1 * 64; vh = lane * 16;
      sh = OFF_HS + (rb16 * 18 + nb1) * 1024; sxf = OFF_XF + (((rb16 >> 1) * 18 + 2 * c + nb1) * 2 + (rb16 & 1)) * 1024 + lane * 16;
      qra = L_QH + (16 * blk1 + i16) * 128 + (((4 * nb1 + (g >> 1)) ^ (i16 >> 1)) << 4) + (g & 1) * 8;
      ssoff = (OFF_SS + (rb16 * 20 + 2 * c + nb1) * 64 + i16 * 4) | (g << 28); mnoff = (OFF_MEAN + row * 4) | ((g | nb1) << 28); mqld = OFF_MQ + row * 4;
      vg = ((lane >> 1) << (wave >= 4 ? 11 : 10)) + (lane & 1) * 240;
      vfu = doprep ? OFF_FU + ((2 * c + hp) * 64 + x * 8 + 3 * m + (wave >> 1)) * 128 + lane * 4 : OOB;
      PIN(red); PIN(mqld); PIN(aw1); PIN(at); PIN(vh); PIN(qra); PIN(ssoff); PIN(mnoff); PIN(vg); PIN(vfu);
      PIN(sh); PIN(sxf);
    };
    f32x4 acc0, acc1, acc2, acc3, s1;
    if (wave < 6) {
      STAMP(17);
      pre1();
      const int base0 = R0 - 2 + 87 * m;
      const unsigned ax0 = L_QH + i16 * 128 + ((g ^ (i16 >> 1)) << 4), ax = ax0 + w1k * 2, ax2 = ax0 + hp * 8192;
      const unsigned aw = ((8 * g + q4) * 128 + (((q4 >> 1) | ((g & 1) << 1)) << 5) + 8 * p4) ^ (hp * 64);
      v2u skw; const unsigned sko = (64 * c + 32 * hp + 16 * (g >> 1) + i16) * 8;
      unsigned hoff[2][2], mqoff, split, sta, csa, uoff;
      { const int d0 = shiftof(64 * c + 32 * hp), d1 = shiftof(64 * c + 32 * hp + 16);
#pragma unroll
        for (int f = 0; f < 2; ++f) {
          const int li = 16 * (wave ^ f) + i16, sr = base0 + li; const bool ok = li < (m == 2 ? 86 : 87);
#pragma unroll
          for (int t = 0; t < 2; ++t) { const int tr = sr - (t ? d1 : d0); const unsigned a = OFF_HS + ((tr >> 4) * 18 + 2 * c + hp) * 1024 + (g * 16 + (tr & 15)) * 16 + 8 * t;
            hoff[f][t] = a | ((ok && (unsigned)(tr - R0) < 256u) ? 0u : 0x40000000u); PIN(hoff[f][t]); }
          if (f == 0) { mqoff = (OFF_MQ + sr * 4) | ((g == 0 && ok && (unsigned)(sr - R0) < 256u) ? 0u : 0x40000000u); PIN(mqoff); }
        }
        split = (unsigned)(d0 - d1); PINS(split);
        sta = L_ST + wave * 128 + i16 * 8; csa = L_CS + hp * 128 + g * 16;
        uoff = OFF_UB + (64 * c + 32 * hp + (lane >> 2)) * 4096 + (x * 8 + 3 * m + (wave >> 1)) * 64 + p4 * 16;
        PIN(sta); PIN(csa); PIN(uoff); }
      const unsigned vf1 = OFF_F1 + (x * 32 + bi) * 128 + lane * 4;
      STAMP(18);
#define P0_OUT [acc0] "=&v"(acc0), [acc1] "=&v"(acc1), [acc2] "=&v"(acc2), [acc3] "=&v"(acc3), [tag] "=&s"(tag), [skw] "=&v"(skw), [e0] "=&v"(e0) STV_OP
#define P0_IN [ax] "v"(ax), [aw] "v"(aw), [ws] "s"(ws), [sk] "s"(sk), [sko] "v"(sko)
      f32x4 e0;
      if (wave & 2) {
        f32x4 e1, e2, e3, e4, e5;
        asm volatile("s_nop 4\n\t" P0_2 : P0_OUT, [e1] "=&v"(e1), [e2] "=&v"(e2), [e3] "=&v"(e3), [e4] "=&v"(e4), [e5] "=&v"(e5) : P0_IN, [ax2] "v"(ax2) : "memory", "scc", P0_CLOB);
        auto dg = [&](f32x4 s) { return (p4 & 2) ? ((p4 & 1) ? s[3] : s[2]) : ((p4 & 1) ? s[1] : s[0]); };
        if (g == q4) { ldsw<f32x2>(sta, (f32x2){e0[0], dg(e1)}); ldsw<f32x2>(L_ST + hp * 512 + i16 * 8, (f32x2){e2[0], dg(e3)}); ldsw<f32x2>(L_ST + hp * 512 + 128 + i16 * 8, (f32x2){e4[0], dg(e5)}); }
      } else {
        if (wave < 4) asm volatile("s_nop 4\n\t" P0_0 : P0_OUT : P0_IN, [vf1] "v"(vf1), [w1k] "s"(w1k) : "memory", "scc", P0_CLOB);
        else asm volatile("s_nop 4\n\t" P0_1 : P0_OUT : P0_IN : "memory", "scc", P0_CLOB);
        if (i16 == 0) ldsw<f32x4>(csa + (wave >> 2) * 64, e0);
      }
      STAMP(1);
      BAR();
      STAMP(2);
      { const f32x4 cA = ldsr<f32x4>(csa), cB = ldsr<f32x4>(csa + 64);
        const f32x2 st0 = ldsr<f32x2>(sta), st1 = ldsr<f32x2>(sta ^ 128);
#pragma unroll
        for (int f = 0; f < 2; ++f) {
          const f32x2 sv = f ? st1 : st0;
          const float mean = sv[0] * (1.f / D), rho = __builtin_amdgcn_rsqf(fmaxf(sv[1] * (1.f / D) - mean * mean, 0.f) + 1e-5f);
          const f32x4 a = ((f ? acc2 : acc0) - mean * cA) * rho, b = ((f ? acc3 : acc1) - mean * cB) * rho;
          const v4u o = {nofill(pk2(gelu(a[0]), gelu(a[1]))), nofill(pk2(gelu(a[2]), gelu(a[3]))), nofill(pk2(gelu(b[0]), gelu(b[1]))), nofill(pk2(gelu(b[2]), gelu(b[3])))};
          { const v2u lo = {o[0], o[1]}, hi = {o[2], o[3]};
            asm volatile("s_cmp_lg_u32 %[sp], 0\n\ts_cbranch_scc1 L_A%=\n\tbuffer_store_dwordx4 %[o], %[a0], %[rs], 0 offen sc1\n\ts_branch L_B%=\n"
                         "L_A%=:\n\tbuffer_store_dwordx2 %[lo], %[a0], %[rs], 0 offen sc1\n\tbuffer_store_dwordx2 %[hi], %[a1], %[rs], 0 offen sc1\n"
                         "L_B%=:\n\ts_nop 1" :: [sp] "s"(split), [o] "v"(o), [lo] "v"(lo), [hi] "v"(hi), [a0] "v"(hoff[f][0]), [a1] "v"(hoff[f][1]), [rs] "s"(rw) : "memory", "scc"); }
          if (f == 0) st32z(rw, mqoff, nofillf(mean), c);
        } }
      STAMP(3);
      BAR();
      if (doprep) { prep(L_K, L_V, L_QH + 12288, skw); ustore(L_QH + 12288 + wave * 2560, uoff); VMN(2); } else VM0();
    } else {
      auto w2l = [&](int L) { const unsigned so = (kr + L - (kr + L >= 9 ? 9 : 0)) * (64 * 1152);
#pragma unroll
        for (int p = 0; p < 4; ++p) glds<0>(rw2, vw2 + so + p * (16 * 1152), L * 8192 + hp * 1024 + p * 2048); };
#pragma unroll
      for (int L = 3; L < W2MID; ++L) w2l(L);
      pre1();
      asm volatile("s_waitcnt vmcnt(%0)" :: "i"(4 + 4 * (W2MID - 3)) : "memory");
      STAMP(3);
      BAR();
#pragma unroll
      for (int L = W2MID; L < 9; ++L) w2l(L);
      { const unsigned vqr = (base1 + 8 * hp + r8) * 1152 + c * 128 + ((lane & 7) ^ (hp * 4 + g)) * 16;
#pragma unroll
        for (int p = 0; p < 6; ++p) glds<0>(rq, vqr + p * (16 * 1152), L_QH + hp * 1024 + p * 2048); }
      if (wave == 6) {
        if (lane < 4) ldsw<v4u>(L_CH + (lane >> 1) * 4352 + (lane & 1) * 16, (v4u){0, 0, 0, 0});
#pragma unroll 1
        for (int spin = 0; spin < 400000; ++spin) {
          v4u gr[5]; bool ok = true;
#pragma unroll
          for (int s = 0; s < 5; ++s) gr[s] = ld128(rw, OFF_W2B + ((s == 4 ? (lane & 31) : lane) + s * 64) * 16);
#pragma unroll
          for (int s = 0; s < 5; ++s) { ok &= gr[s][1] == tag && gr[s][3] == tag; int e = (s == 4 ? (lane & 31) : lane) + s * 64 - 32 * kr; e += e < 0 ? 288 : 0; ldsw<unsigned>(L_T + e * 4, gr[s][0] | (gr[s][2] << 16)); }
          if (__all(ok)) break;
          asm volatile("" ::: "memory");
        }
      }
      VM0();
    }
    sh = rfl(sh); tag = rfl(tag);
    auto early = [&](auto role_) {
      constexpr bool RB = decltype(role_)::value;
      STAMP(4);
      float mq = 0;
      if (!RB) asm volatile("s_nop 4\n\t" P1_A : [acc0] "=&v"(acc0), [acc1] "=&v"(acc1), [acc2] "=&v"(acc2), [acc3] "=&v"(acc3), [s1] "=&v"(s1), [mq] "=&v"(mq) STV_OP
            : [vmq] "v"(mqld), [rs] "s"(rw), [vh] "v"(vh), [sh] "s"(sh), [aw] "v"(aw1), [at] "v"(at), [vg] "v"(vg), [c] "s"(kr), [vtag] "v"(tag), [vfu] "v"(vfu) : "memory", "scc", P1_CLOB);
      else if (act) {
        asm volatile("s_nop 4\n\t" P1_B : [acc0] "=&v"(acc0), [acc1] "=&v"(acc1), [acc2] "=&v"(acc2), [acc3] "=&v"(acc3), [s1] "=&v"(s1), [mq] "=&v"(mq) STV_OP
            : [vmq] "v"(mqld), [rs] "s"(rw), [vh] "v"(vh), [sh] "s"(sh), [aw] "v"(aw1), [at] "v"(at), [vg] "v"(vg), [c] "s"(kr), [vtag] "v"(tag), [vfu] "v"(vfu) : "memory", "scc", P1_CLOB);
        ldsw<f32x4>(red, acc2); ldsw<f32x4>(red + 1024, acc3); ldsw<float>(L_ST + (wave & 3) * 256 + lane * 4, s1[0]);
      } else { BAR(); __builtin_amdgcn_s_sleep(48); }
      STAMP(6);
      auto finish = [&]() {
        float mean = mq + s1[0];
        if (RB) { acc0 += ldsr<f32x4>(red ^ 2048); acc1 += ldsr<f32x4>((red ^ 2048) + 1024); mean += ldsr<float>(L_ST + ((wave & 3) ^ 1) * 256 + lane * 4); }
#pragma unroll
        for (int f = 0; f < (RB ? 1 : 2); ++f) {
          const v2u qa = ldsr<v2u>(qra ^ (f * 64)), qb = ldsr<v2u>(qra ^ (f * 64 + 32));
          const f32x4 a = (f ? acc2 : acc0) - mean, b = (f ? acc3 : acc1) - mean;
          const v4u o = {pk2(a[0] + bflo(qa[0]), a[1] + bfhi(qa[0])), pk2(a[2] + bflo(qa[1]), a[3] + bfhi(qa[1])), pk2(b[0] + bflo(qb[0]), b[1] + bfhi(qb[0])), pk2(b[2] + bflo(qb[1]), b[3] + bfhi(qb[1]))};
          st128(rw, sxf + f * 2048, o);
          float ss = dot2(o[0], o[0], 0.f); ss = dot2(o[1], o[1], ss); ss = dot2(o[2], o[2], ss); ss = dot2(o[3], o[3], ss);
          ss = xsum16_32(ss);
          st32(rw, ssoff + f * 64, __builtin_bit_cast(unsigned, ss));
        }
        st32z(rw, mnoff, mean, c);
      };
      if (!RB) {
        finish();
        BAR();
        STAMP(7);
      } else {
        if (wave == 7) ldsw<v4u>(L_CU + (lane >> 3) * 1152 + (lane & 7) * 16, (v4u){0, 0, 0, 0});
        unsigned fu;
        asm volatile("s_nop 4\n\tbuffer_load_dword %0, %1, %2, 0 offen sc1\n\ts_waitcnt lgkmcnt(0)\n\ts_barrier\n\ts_waitcnt vmcnt(0)" : "=&v"(fu) : "v"(fua), "s"(rw) : "memory");
        const bool early_u = __all(fu == tag);
        if (early_u) {
#pragma unroll
          for (int n = 0; n < 2; ++n) glds<2>(rw, OFF_UB + (c0 + n) * 4096 + lane * 64 + wi * 16, L_CU + (4 * n + wi) * 1152 + 128);
        }
        if (act) finish();
        STAMP(7);
        if (!early_u) {
          poll(rw, fua, tag);
#pragma unroll
          for (int n = 0; n < 2; ++n) glds<2>(rw, OFF_UB + (c0 + n) * 4096 + lane * 64 + wi * 16, L_CU + (4 * n + wi) * 1152 + 128);
          VM0();
        } else if (act) VMN(2); else VM0();
        BAR();
        STAMP(9);
        if (PRE_A) prefill(0, PRE_A);
        asm volatile("s_waitcnt vmcnt(%0)\n\ts_barrier" :: "i"(3 * PRE_A) : "memory");
        flagz(rw, (x * 32 + bi) * 128 + lane * 4, OFF_FX, tag, wave ^ 7);
        STAMP(11);
        prefill(PRE_A, CORE_PRE);
      }
    };
    if (wave < 4) early(std::false_type{}); else early(std::true_type{});
  }
  if (hbig || wave < 4) conv();
  if (hbig) {
    STAMP(9);
    if (wave < 4) BAR();
  }
  if (wave >= 4) { STAMP(10); poll(rw, fxa, tag); STAMP(8); rhotab(); }
  STAMP(13);
  if (wave < 4) {
    const unsigned sp = OFF_P + (((rt * 4 + jj) * 4 + wi) * 4 + kq) * 8192, sp4 = OFF_P4 + ((rt * 4 + wi) * 64 + sl) * 1024, sx = OFF_XF + (rt * 4 + wi) * 36 * 1024;
    const unsigned sc = OFF_PC + (((rt * 4 + jj) * 4 + wi) * 4 + kq) * 2048, sc4 = OFF_PC4 + ((rt * 4 + wi) * 16 + sl) * 1024;
    asm volatile("s_nop 4\n\t" CORE_C :: [ws] "s"(ws), [wi] "s"(wi), [sl] "s"(sl), [shx] "s"(shx), [sp] "s"(sp), [sp4] "s"(sp4), [sx] "s"(sx), [sc] "s"(sc), [sc4] "s"(sc4), [ep] "s"(tag) : "memory", "scc", "vcc", CORE_C_CLOB, CORE_SC_CLOB);
    BAR();
    return;
  }
  { const unsigned r32 = rt * 4 + kq;
    const unsigned cbm = (2 * jj + (jj >> 1)) * 2 + (wi >> 1), rbm = wi & 1, cb4 = 8 + (jj >> 1), rb4 = jj & 1;
    const unsigned oa = (r32 * 32 + rbm * 16 + (lane >> 2)) * 1152 + cbm * 64 + p4 * 16, oo = (r32 * 32 + rb4 * 16 + i16) * 1152 + cb4 * 64 + g * 8;
    const unsigned spi = rfl(OFF_P + ((rt * 4 + jj) * 4 + kq) * 32768 + kq * 8192 + wi * 1024), ap4 = OFF_P4 + ((r32 * 4 + jj) * 16 + 4 * g) * 1024 + (wi * 16 + i16) * 16;
    const unsigned ac4 = OFF_PC4 + (r32 * 16 + 4 * g) * 1024 + (wi * 16 + i16) * 16 + jj * 4, sci = rfl(OFF_PC + (((rt * 4 + jj) * 4 + kq) * 4 + kq) * 2048 + wi * 4);
    const unsigned l3 = (bi * 4 + wi) * 45 + lane, r3 = l3 / 10, k3 = l3 - r3 * 10;
    const unsigned k5 = k3 >= 5 ? k3 - 5 : k3, pf3 = r3 * 9216 + kq * 2304 + (k3 >= 5 ? 1152 : 0) + (k5 + (k5 >= 3 ? 4 : 2)) * 128, pf4 = kq * 1327104 + ((bi * 4 + wi) * 27 + lane) * 128;
    const unsigned sxo = rfl(OFF_XF + ((r32 * 18 + cbm) * 2 + rbm) * 1024), sxo2 = OFF_XF + ((r32 * 18 + cb4) * 2 + rb4) * 1024, fyo = OFF_FY + bid * 4;
    const unsigned aq2 = (r32 * 32 + rb4 * 16 + (lane >> 2)) * 1152 + cb4 * 64 + p4 * 16, ar = OFF_G + (r32 * 32 + (lane & 31)) * 4;
    const unsigned ay = OFF_Y + (r32 * D + cbm * 32 + (lane >> 1)) * 64 + rbm * 32 + (lane & 1) * 16, ay2 = OFF_Y + (r32 * D + cb4 * 32 + (lane >> 1)) * 64 + rb4 * 32 + (lane & 1) * 16;
    unsigned dummy;
    asm volatile("s_nop 4\n\t" CORE_L1 : "=s"(dummy) STV_OP
      : [w3] "s"(w3), [w4] "s"(w4), [ws] "s"(ws), [wi] "s"(wi), [rot] "s"(rot), [sl] "s"(sl), [shx] "s"(shx), [sfl] "s"(sfl), [ep] "s"(tag), [q] "s"(q), [sxo] "s"(sxo), [sxo2] "s"(sxo2), [fyo] "s"(fyo),
        [spi] "s"(spi), [sci] "s"(sci), [out] "s"(out), [pf4] "v"(pf4), [pf3] "v"(pf3), [aq] "v"(oa), [aq2] "v"(aq2), [ar] "v"(ar), [ay] "v"(ay), [ay2] "v"(ay2), [ap4] "v"(ap4), [ac4] "v"(ac4), [oo] "v"(oo),
        [mean] "v"(meano), [rho0] "v"(rhoo[0]), [rho1] "v"(rhoo[1]) : "memory", "scc", "vcc", CORE_L_CLOB, CORE_S_CLOB); }
  if (bid == 0 && threadIdx.x == 256) st32(rw, OFF_EP, tag);
}

extern "C" void kernel_launch(void* const* d_in, const int* in_sizes, int n_in, void* d_out, int out_size, void* d_ws, size_t ws_size, hipStream_t stream) {
  (void)hipFuncSetAttribute((const void*)fused, hipFuncAttributeMaxDynamicSharedMemorySize, 163840);
  fused<<<256, 512, 163840, stream>>>((const u16*)d_in[0], (const u16*)d_in[1], (const u16*)d_in[2], (const u16*)d_in[3], (const u16*)d_in[4], (const u16*)d_in[5],
      (const u16*)d_in[7], (const u16*)d_in[8], (const u16*)d_in[9], (const u16*)d_in[10], (const u16*)d_in[11], (u16*)d_out, (char*)d_ws);
}
```
